```python
import torch

def _softmax(x, axis=-1):
    return torch.softmax(x, dim=axis)
def _gelu(x):
    return torch.nn.functional.gelu(x, approximate="tanh")
def _silu(x):
    return torch.nn.functional.silu(x)
def _sigmoid(x):
    return torch.sigmoid(x)
def _layernorm(x, axis=-1, eps=1e-5):
    m = x.mean(dim=axis, keepdim=True)
    v = x.var(dim=axis, keepdim=True, unbiased=False)
    return (x - m) / torch.sqrt(v + eps)
def _scatter_add(tgt, idx, src, axis=0):
    return tgt.clone().index_add_(axis, idx, src)
def _segment_sum(src, seg, n):
    out = torch.zeros((n,) + src.shape[1:], dtype=src.dtype)
    return out.index_add_(0, seg, src)
def _pad(x, pw):
    flat = tuple(v for pair in reversed(pw) for v in pair)
    return torch.nn.functional.pad(x, flat)

kernel_name = 'standalone__gqa_softmax__near_ridge'

def setup_inputs():

    _w = (lambda a: (lambda t: t.to(torch.bfloat16) if t.is_floating_point() else t)(torch.as_tensor(a)))
    B, Hq, Hkv, Sq, Sk, D = 8, 16, 8, 128, 2048, 256
    return {
        "q": _w((lambda *s: torch.randn(*s, dtype=torch.bfloat16) * 0.8)(B, Hq, Sq, D)),
        "k": _w((lambda *s: torch.randn(*s, dtype=torch.bfloat16) * 0.8)(B, Hkv, Sk, D)),
        "v": _w((lambda *s: torch.randn(*s, dtype=torch.bfloat16) * 0.05)(B, Hkv, Sk, D)),
    }

def reference(q, k, v):
    B, Hq, Sq, D = q.shape
    Hkv = k.shape[1]
    rep = Hq // Hkv
    k = (lambda x, n, axis: torch.repeat_interleave(x, n, dim=axis))(k, rep, axis=1)
    v = (lambda x, n, axis: torch.repeat_interleave(x, n, dim=axis))(v, rep, axis=1)
    scale = D ** -0.5
    scores = torch.einsum("bhqd,bhkd->bhqk", q, k) * scale
    p = _softmax(scores, axis=-1)
    return torch.einsum("bhqk,bhkd->bhqd", p, v)
```

```cpp
#include <hip/hip_runtime.h>
#include <hip/hip_bf16.h>
constexpr int B=8,HQ=16,HKV=8,SQ=128,SK=2048,D=256;
__device__ inline float bf2f(unsigned short x){ return __uint_as_float(((unsigned)x)<<16); }
__global__ __launch_bounds__(256) void naive(const unsigned short* q,const unsigned short* k,const unsigned short* v,__hip_bfloat16* o){
  __shared__ float s[SK]; __shared__ float qs[D]; __shared__ float red[256];
  int row=blockIdx.x; int bh=row/SQ; int b=bh/HQ,h=bh%HQ; int kvh=h/(HQ/HKV);
  int t=threadIdx.x;
  qs[t]=bf2f(q[(size_t)row*D+t]); __syncthreads();
  const unsigned short* kb=k+((size_t)(b*HKV+kvh))*SK*D; const unsigned short* vb=v+((size_t)(b*HKV+kvh))*SK*D;
  float mx=-1e30f;
  for(int j=t;j<SK;j+=256){ float a=0; for(int d=0;d<D;++d) a+=qs[d]*bf2f(kb[(size_t)j*D+d]); a*=0.0625f; s[j]=a; mx=fmaxf(mx,a);}
  red[t]=mx; __syncthreads();
  for(int st=128;st>0;st>>=1){ if(t<st) red[t]=fmaxf(red[t],red[t+st]); __syncthreads(); }
  mx=red[0]; __syncthreads();
  float sum=0; for(int j=t;j<SK;j+=256){ float e=__expf(s[j]-mx); s[j]=e; sum+=e; }
  red[t]=sum; __syncthreads();
  for(int st=128;st>0;st>>=1){ if(t<st) red[t]+=red[t+st]; __syncthreads(); }
  sum=red[0];
  float acc=0; for(int j=0;j<SK;++j) acc+=s[j]*bf2f(vb[(size_t)j*D+t]);
  o[(size_t)row*D+t]=__float2bfloat16(acc/sum);
}
extern "C" void kernel_launch(void* const* d_in, const int* in_sizes, int n_in, void* d_out, int out_size, void* d_ws, size_t ws_size, hipStream_t stream){
  naive<<<B*HQ*SQ,256,0,stream>>>((const unsigned short*)d_in[0],(const unsigned short*)d_in[1],(const unsigned short*)d_in[2],(__hip_bfloat16*)d_out);
}
```

```cpp
#include <hip/hip_runtime.h>
#include <hip/hip_bf16.h>

__global__ __launch_bounds__(256, 1) __attribute__((aligned(4096)))
void gqa_attn_kernel(const char* q, const char* k, const char* v, char* o, char* ws) {
  __shared__ __attribute__((aligned(1024))) char lds[163840];
  asm volatile(
    "s_load_dwordx4 s[12:15], %0, 0\n"
    "v_mbcnt_lo_u32_b32 v39, -1, 0\n"
    "v_mbcnt_hi_u32_b32 v39, -1, v39\n"
    "s_lshr_b32 s35, %2, 6\n"
    "s_getpc_b64 s[36:37]\n"
    "PCL_%=:\n"
    "s_mov_b32 s38, END_%=-PCL_%=\n"
    "s_mov_b32 s39, 0x20000\n"
    "s_bfe_u32 s43, %1, 0x40003\n"
    "s_lshl2_add_u32 s43, s43, s35\n"
    "s_lshl_b32 s43, s43, 10\n"
    "v_lshl_add_u32 v4, v39, 4, s43\n"
    "buffer_load_dwordx4 v[180:183], v4, s[36:39], 0 offen\n"
    "v_lshrrev_b32 v0, 3, v39\n"
    "v_lshrrev_b32 v2, 4, v39\n"
    "v_and_b32 v1, 7, v39\n"
    "v_xor_b32 v3, v1, v2\n"
    "v_lshlrev_b32 v3, 4, v3\n"
    "v_lshl_add_u32 v3, v0, 11, v3\n"
    "v_add_u32 v104, 3072, v3\n"
    "v_add_u32 v105, 2176, v3\n"
    "v_add_u32 v106, 1280, v3\n"
    "v_add_u32 v107, 384, v3\n"
    "s_and_b32 s43, %1, 7\n"
    "s_lshr_b32 s44, %1, 5\n"
    "s_lshl3_add_u32 s43, s43, s44\n"
    "s_lshl_b32 s44, s35, 14\n"
    "s_bfe_u32 s50, %1, 0x10003\n"
    "s_lshl1_add_u32 s50, s43, s50\n"
    "s_lshl_b32 s50, s50, 16\n"
    "s_add_u32 s50, s50, s44\n"
    "s_bfe_u32 s31, %1, 0x10004\n"
    "s_lshl1_add_u32 s42, s43, s31\n"
    "s_lshl_b32 s42, s42, 19\n"
    "s_sub_u32 s48, s50, 3072\n"
    "s_ashr_i32 s49, s48, 31\n"
    "s_mov_b32 s10, 19456\n"
    "s_mov_b32 s11, 0x20000\n"
    "s_lshl_b32 s28, s35, 12\n"
    "s_add_u32 s28, s28, %3\n"
    "s_and_b32 s29, %2, 64\n"
    "s_add_u32 m0, s28, 0x18000\n"
    "s_mov_b32 s40, 0x0\n"
    "v_xor_b32 v108, 64, v104\n"
    "v_xor_b32 v109, 64, v105\n"
    "v_xor_b32 v110, 64, v106\n"
    "v_xor_b32 v111, 64, v107\n"
    "s_waitcnt lgkmcnt(0)\n"
    "s_add_u32 s8, s12, s48\n"
    "s_addc_u32 s9, s13, s49\n"
    "s_load_dwordx4 s[16:19], %0, 16\n"
    "s_load_dwordx2 s[24:25], %0, 32\n"
    "buffer_load_dwordx4 v104, s[8:11], s40 offen offset:0 lds\n"
    "buffer_load_dwordx4 v105, s[8:11], s40 offen offset:1024 lds\n"
    "buffer_load_dwordx4 v106, s[8:11], s40 offen offset:2048 lds\n"
    "buffer_load_dwordx4 v107, s[8:11], s40 offen offset:3072 lds\n"
    "s_add_u32 m0, s28, 0x1c000\n"
    "s_mov_b32 s40, 0x400\n"
    "buffer_load_dwordx4 v108, s[8:11], s40 offen offset:0 lds\n"
    "buffer_load_dwordx4 v109, s[8:11], s40 offen offset:1024 lds\n"
    "buffer_load_dwordx4 v110, s[8:11], s40 offen offset:2048 lds\n"
    "buffer_load_dwordx4 v111, s[8:11], s40 offen offset:3072 lds\n"
    "s_add_u32 m0, s28, 0x20000\n"
    "s_mov_b32 s40, 0x200\n"
    "buffer_load_dwordx4 v104, s[8:11], s40 offen offset:0 lds\n"
    "buffer_load_dwordx4 v105, s[8:11], s40 offen offset:1024 lds\n"
    "s_add_u32 m0, s28, 0x24000\n"
    "s_mov_b32 s40, 0x600\n"
    "buffer_load_dwordx4 v108, s[8:11], s40 offen offset:0 lds\n"
    "s_bfe_u32 s44, s35, 0x10000\n"
    "s_lshl_b32 s44, s44, 9\n"
    "s_bfe_u32 s43, s35, 0x10001\n"
    "s_lshl_b32 s43, s43, 11\n"
    "s_or_b32 s44, s44, s43\n"
    "s_add_u32 s44, s42, s44\n"
    "v_lshlrev_b32 v3, 14, v0\n"
    "v_lshlrev_b32 v5, 11, v0\n"
    "v_sub_u32 v5, v3, v5\n"
    "v_xad_u32 v34, v104, s29, v5\n"
    "v_xad_u32 v35, v105, s29, v5\n"
    "v_xad_u32 v36, v106, s29, v5\n"
    "v_xad_u32 v37, v107, s29, v5\n"
    "v_lshlrev_b32 v2, 1, v2\n"
    "v_xor_b32 v1, v1, v2\n"
    "v_lshlrev_b32 v1, 4, v1\n"
    "v_add_u32 v38, v3, v1\n"
    "s_sub_u32 s48, s44, 3072\n"
    "s_ashr_i32 s49, s48, 31\n"
    "s_add_u32 s12, s14, s48\n"
    "s_addc_u32 s13, s15, s49\n"
    "s_mov_b32 s14, 524800\n"
    "s_bfe_u32 s44, s35, 0x10000\n"
    "s_lshl_b32 s44, s44, 9\n"
    "s_bfe_u32 s43, s35, 0x10001\n"
    "s_lshl_b32 s43, s43, 11\n"
    "s_or_b32 s44, s44, s43\n"
    "s_add_u32 s42, s42, s44\n"
    "s_waitcnt lgkmcnt(0)\n"
    "s_add_u32 s20, s18, s50\n"
    "s_addc_u32 s21, s19, 0\n"
    "s_mov_b32 s22, 16384\n"
    "s_add_u32 s16, s16, s42\n"
    "s_addc_u32 s17, s17, 0\n"
    "s_mov_b32 s18, 521728\n"
    "s_mov_b32 s26, 0x200000\n"
    "s_mov_b32 s15, s11\n"
    "s_mov_b32 s19, s11\n"
    "s_mov_b32 s23, s11\n"
    "s_mov_b32 s27, s11\n"
    "s_lshl_b32 s31, s31, 8\n"
    "s_xor_b32 s30, s31, 256\n"
    "s_lshl2_add_u32 s7, %1, s35\n"
    "s_lshl_b32 s7, s7, 11\n"
    "s_xor_b32 s33, s7, 0x20000\n"
    "s_lshl2_add_u32 s34, s35, %3\n"
    "s_add_u32 s34, s34, 52224\n"
    "v_bfe_u32 v0, v39, 1, 3\n"
    "v_lshrrev_b32 v1, 4, v39\n"
    "v_xor_b32 v0, v0, v1\n"
    "v_lshlrev_b32 v0, 4, v0\n"
    "v_and_b32 v1, 7, v39\n"
    "v_lshl_add_u32 v0, v1, 7, v0\n"
    "v_bfe_u32 v1, v39, 3, 1\n"
    "v_lshl_add_u32 v2, v1, 14, v0\n"
    "v_add_u32 v2, s28, v2\n"
    "v_add_u32 v112, 98304, v2\n"
    "v_xor_b32 v113, 64, v112\n"
    "v_lshl_add_u32 v2, v1, 12, v0\n"
    "v_add_u32 v24, %3, v2\n"
    "v_xor_b32 v25, 64, v24\n"
    "v_add_u32 v26, 0x10000, v24\n"
    "v_add_u32 v27, 0x10000, v25\n"
    "v_add_u32 v28, 0x20000, v24\n"
    "v_add_u32 v29, 0x20000, v25\n"
    "v_lshrrev_b32 v0, 5, v39\n"
    "v_bfe_u32 v1, v39, 2, 3\n"
    "v_lshlrev_b32 v1, 7, v1\n"
    "v_lshl_add_u32 v0, v0, 12, v1\n"
    "v_and_b32 v1, 3, v39\n"
    "v_lshl_add_u32 v0, v1, 3, v0\n"
    "v_bfe_u32 v1, v39, 3, 2\n"
    "v_lshl_add_u32 v0, v1, 5, v0\n"
    "v_add_u32 v30, %3, v0\n"
    "v_xor_b32 v31, 32, v30\n"
    "v_xor_b32 v32, 64, v30\n"
    "v_xor_b32 v33, 96, v30\n"
    "s_mov_b32 s41, 0\n"
    "s_mov_b32 s45, 0x3db8aa3b\n"
    "s_mov_b32 s46, 0xffff0000\n"
    "s_mov_b32 s47, 0x4e800000\n"
    "v_mov_b32 v0, 0x3f803f80\n"
    "v_mov_b32 v1, 0xff800000\n"
    "v_accvgpr_write_b32 a208, v0\n"
    "v_accvgpr_write_b32 a209, v0\n"
    "v_accvgpr_write_b32 a210, v0\n"
    "v_accvgpr_write_b32 a211, v0\n"
    "v_accvgpr_write_b32 a212, v1\n"
    "v_accvgpr_write_b32 a213, v1\n"
    "v_and_b32 v0, 1, v39\n"
    "v_lshlrev_b32 v0, 4, v0\n"
    "v_mov_b32 v1, 0xffff\n"
    "v_lshlrev_b32 v0, v0, v1\n"
    "s_mov_b32 vcc_lo, 0x00300003\n"
    "s_mov_b32 vcc_hi, 0x30000300\n"
    "s_mov_b32 s43, 0x3db93db9\n"
    "s_mov_b32 s50, 0xb92cb92c\n"
    "v_cndmask_b32 v1, 0, v0, vcc\n"
    "s_lshl_b64 vcc, vcc, 2\n"
    "v_cndmask_b32 v2, 0, v0, vcc\n"
    "v_mov_b32 v68, 0\n"
    "v_mov_b32 v69, 0\n"
    "v_mov_b32 v72, 0\n"
    "v_mov_b32 v73, 0\n"
    "v_mov_b32 v76, 0\n"
    "v_mov_b32 v77, 0\n"
    "v_and_b32 v70, s43, v1\n"
    "v_and_b32 v74, s50, v1\n"
    "v_and_b32 v71, s43, v2\n"
    "v_and_b32 v75, s50, v2\n"
    "buffer_load_dwordx4 v109, s[8:11], s40 offen offset:1024 lds\n"
    "s_waitcnt vmcnt(8)\n"
    "buffer_load_dwordx4 v110, s[8:11], s40 offen offset:2048 lds\n"
    "buffer_load_dwordx4 v111, s[8:11], s40 offen offset:3072 lds\n"
    "s_add_u32 m0, s28, 0x20000\n"
    "s_mov_b32 s40, 0x200\n"
    "buffer_load_dwordx4 v106, s[8:11], s40 offen offset:2048 lds\n"
    "buffer_load_dwordx4 v107, s[8:11], s40 offen offset:3072 lds\n"
    "s_mov_b64 exec, 0\n"
    "s_cmp_eq_u32 s35, 1\n"
    "s_cbranch_scc1 STUB1_%=\n"
    "s_cmp_eq_u32 s35, 2\n"
    "s_cbranch_scc1 STUB2_%=\n"
    "s_cmp_eq_u32 s35, 3\n"
    "s_cbranch_scc1 STUB3_%=\n"
    "s_branch STUB0_%=\n"
    "WRET_%=:\n"
    "s_mov_b64 exec, -1\n"
    "s_mov_b64 exec, 0\n"
    "s_cmp_eq_u32 s35, 1\n"
    "s_cbranch_scc1 FST1_%=\n"
    "s_cmp_eq_u32 s35, 2\n"
    "s_cbranch_scc1 FST2_%=\n"
    "s_cmp_eq_u32 s35, 3\n"
    "s_cbranch_scc1 FST3_%=\n"
    "s_branch FST0_%=\n"
    "WRET2_%=:\n"
    "s_mov_b64 exec, -1\n"
    "s_waitcnt vmcnt(8)\n"
    "ds_read_b128 v[192:195], v112 offset:0\n"
    "ds_read_b128 v[196:199], v113 offset:0\n"
    "ds_read_b128 v[200:203], v112 offset:1024\n"
    "ds_read_b128 v[204:207], v113 offset:1024\n"
    "ds_read_b128 v[208:211], v112 offset:2048\n"
    "ds_read_b128 v[212:215], v113 offset:2048\n"
    "ds_read_b128 v[216:219], v112 offset:3072\n"
    "ds_read_b128 v[220:223], v113 offset:3072\n"
    "s_add_u32 m0, s28, 0x0\n"
    "s_mov_b32 s40, 0x0\n"
    "buffer_load_dwordx4 v34, s[12:15], s40 offen offset:0 lds\n"
    "buffer_load_dwordx4 v35, s[12:15], s40 offen offset:1024 lds\n"
    "buffer_load_dwordx4 v36, s[12:15], s40 offen offset:2048 lds\n"
    "buffer_load_dwordx4 v37, s[12:15], s40 offen offset:3072 lds\n"
    "s_add_u32 m0, s28, 0x4000\n"
    "s_mov_b32 s40, 0x1000\n"
    "s_waitcnt lgkmcnt(7)\n"
    "v_mfma_f32_16x16x32_bf16 v[160:163], v[70:73], v[192:195], 0\n"
    "v_mfma_f32_16x16x32_bf16 v[164:167], v[68:71], v[192:195], 0\n"
    "v_mfma_f32_16x16x32_bf16 v[160:163], v[74:77], v[192:195], v[160:163]\n"
    "v_mfma_f32_16x16x32_bf16 v[164:167], v[72:75], v[192:195], v[164:167]\n"
    "s_waitcnt lgkmcnt(6)\n"
    "v_mfma_f32_16x16x32_bf16 v[168:171], v[70:73], v[196:199], 0\n"
    "v_mfma_f32_16x16x32_bf16 v[172:175], v[68:71], v[196:199], 0\n"
    "s_nop 3\n"
    "v_cvt_pk_bf16_f32 v184, v160, v161\n"
    "v_cvt_pk_bf16_f32 v185, v162, v163\n"
    "v_mfma_f32_16x16x32_bf16 v[168:171], v[74:77], v[196:199], v[168:171]\n"
    "v_cvt_pk_bf16_f32 v186, v164, v165\n"
    "v_cvt_pk_bf16_f32 v187, v166, v167\n"
    "v_mfma_f32_16x16x32_bf16 v[172:175], v[72:75], v[196:199], v[172:175]\n"
    "v_accvgpr_write_b32 a0, v184\n"
    "v_accvgpr_write_b32 a1, v185\n"
    "s_waitcnt lgkmcnt(5)\n"
    "v_mfma_f32_16x16x32_bf16 v[160:163], v[70:73], v[200:203], 0\n"
    "v_accvgpr_write_b32 a2, v186\n"
    "v_accvgpr_write_b32 a3, v187\n"
    "v_mfma_f32_16x16x32_bf16 v[164:167], v[68:71], v[200:203], 0\n"
    "v_cvt_pk_bf16_f32 v184, v168, v169\n"
    "v_cvt_pk_bf16_f32 v185, v170, v171\n"
    "v_mfma_f32_16x16x32_bf16 v[160:163], v[74:77], v[200:203], v[160:163]\n"
    "v_cvt_pk_bf16_f32 v186, v172, v173\n"
    "v_cvt_pk_bf16_f32 v187, v174, v175\n"
    "v_mfma_f32_16x16x32_bf16 v[164:167], v[72:75], v[200:203], v[164:167]\n"
    "v_accvgpr_write_b32 a4, v184\n"
    "v_accvgpr_write_b32 a5, v185\n"
    "s_waitcnt lgkmcnt(4)\n"
    "v_mfma_f32_16x16x32_bf16 v[168:171], v[70:73], v[204:207], 0\n"
    "v_accvgpr_write_b32 a6, v186\n"
    "v_accvgpr_write_b32 a7, v187\n"
    "v_mfma_f32_16x16x32_bf16 v[172:175], v[68:71], v[204:207], 0\n"
    "v_cvt_pk_bf16_f32 v184, v160, v161\n"
    "v_cvt_pk_bf16_f32 v185, v162, v163\n"
    "v_mfma_f32_16x16x32_bf16 v[168:171], v[74:77], v[204:207], v[168:171]\n"
    "v_cvt_pk_bf16_f32 v186, v164, v165\n"
    "v_cvt_pk_bf16_f32 v187, v166, v167\n"
    "v_mfma_f32_16x16x32_bf16 v[172:175], v[72:75], v[204:207], v[172:175]\n"
    "v_accvgpr_write_b32 a8, v184\n"
    "v_accvgpr_write_b32 a9, v185\n"
    "s_waitcnt lgkmcnt(3)\n"
    "v_mfma_f32_16x16x32_bf16 v[160:163], v[70:73], v[208:211], 0\n"
    "v_accvgpr_write_b32 a10, v186\n"
    "v_accvgpr_write_b32 a11, v187\n"
    "v_mfma_f32_16x16x32_bf16 v[164:167], v[68:71], v[208:211], 0\n"
    "v_cvt_pk_bf16_f32 v184, v168, v169\n"
    "v_cvt_pk_bf16_f32 v185, v170, v171\n"
    "v_mfma_f32_16x16x32_bf16 v[160:163], v[74:77], v[208:211], v[160:163]\n"
    "v_cvt_pk_bf16_f32 v186, v172, v173\n"
    "v_cvt_pk_bf16_f32 v187, v174, v175\n"
    "v_mfma_f32_16x16x32_bf16 v[164:167], v[72:75], v[208:211], v[164:167]\n"
    "v_accvgpr_write_b32 a12, v184\n"
    "v_accvgpr_write_b32 a13, v185\n"
    "s_waitcnt lgkmcnt(2)\n"
    "v_mfma_f32_16x16x32_bf16 v[168:171], v[70:73], v[212:215], 0\n"
    "v_accvgpr_write_b32 a14, v186\n"
    "v_accvgpr_write_b32 a15, v187\n"
    "v_mfma_f32_16x16x32_bf16 v[172:175], v[68:71], v[212:215], 0\n"
    "v_cvt_pk_bf16_f32 v184, v160, v161\n"
    "v_cvt_pk_bf16_f32 v185, v162, v163\n"
    "v_mfma_f32_16x16x32_bf16 v[168:171], v[74:77], v[212:215], v[168:171]\n"
    "v_cvt_pk_bf16_f32 v186, v164, v165\n"
    "v_cvt_pk_bf16_f32 v187, v166, v167\n"
    "v_mfma_f32_16x16x32_bf16 v[172:175], v[72:75], v[212:215], v[172:175]\n"
    "v_accvgpr_write_b32 a16, v184\n"
    "v_accvgpr_write_b32 a17, v185\n"
    "s_waitcnt lgkmcnt(1)\n"
    "v_mfma_f32_16x16x32_bf16 v[160:163], v[70:73], v[216:219], 0\n"
    "v_accvgpr_write_b32 a18, v186\n"
    "v_accvgpr_write_b32 a19, v187\n"
    "v_mfma_f32_16x16x32_bf16 v[164:167], v[68:71], v[216:219], 0\n"
    "v_cvt_pk_bf16_f32 v184, v168, v169\n"
    "v_cvt_pk_bf16_f32 v185, v170, v171\n"
    "v_mfma_f32_16x16x32_bf16 v[160:163], v[74:77], v[216:219], v[160:163]\n"
    "v_cvt_pk_bf16_f32 v186, v172, v173\n"
    "v_cvt_pk_bf16_f32 v187, v174, v175\n"
    "v_mfma_f32_16x16x32_bf16 v[164:167], v[72:75], v[216:219], v[164:167]\n"
    "v_accvgpr_write_b32 a20, v184\n"
    "v_accvgpr_write_b32 a21, v185\n"
    "s_waitcnt lgkmcnt(0)\n"
    "v_mfma_f32_16x16x32_bf16 v[168:171], v[70:73], v[220:223], 0\n"
    "v_accvgpr_write_b32 a22, v186\n"
    "v_accvgpr_write_b32 a23, v187\n"
    "v_mfma_f32_16x16x32_bf16 v[172:175], v[68:71], v[220:223], 0\n"
    "v_cvt_pk_bf16_f32 v184, v160, v161\n"
    "v_cvt_pk_bf16_f32 v185, v162, v163\n"
    "v_mfma_f32_16x16x32_bf16 v[168:171], v[74:77], v[220:223], v[168:171]\n"
    "v_cvt_pk_bf16_f32 v186, v164, v165\n"
    "v_cvt_pk_bf16_f32 v187, v166, v167\n"
    "v_mfma_f32_16x16x32_bf16 v[172:175], v[72:75], v[220:223], v[172:175]\n"
    "v_accvgpr_write_b32 a24, v184\n"
    "v_accvgpr_write_b32 a25, v185\n"
    "v_accvgpr_write_b32 a26, v186\n"
    "v_accvgpr_write_b32 a27, v187\n"
    "s_nop 0\n"
    "v_cvt_pk_bf16_f32 v184, v168, v169\n"
    "v_cvt_pk_bf16_f32 v185, v170, v171\n"
    "s_nop 0\n"
    "v_cvt_pk_bf16_f32 v186, v172, v173\n"
    "v_cvt_pk_bf16_f32 v187, v174, v175\n"
    "v_accvgpr_write_b32 a28, v184\n"
    "v_accvgpr_write_b32 a29, v185\n"
    "v_accvgpr_write_b32 a30, v186\n"
    "v_accvgpr_write_b32 a31, v187\n"
    "s_waitcnt vmcnt(8)\n"
    "ds_read_b128 v[224:227], v112 offset:32768\n"
    "ds_read_b128 v[228:231], v113 offset:32768\n"
    "ds_read_b128 v[232:235], v112 offset:33792\n"
    "ds_read_b128 v[236:239], v113 offset:33792\n"
    "buffer_load_dwordx4 v34, s[12:15], s40 offen offset:0 lds\n"
    "buffer_load_dwordx4 v35, s[12:15], s40 offen offset:1024 lds\n"
    "buffer_load_dwordx4 v36, s[12:15], s40 offen offset:2048 lds\n"
    "s_waitcnt lgkmcnt(3)\n"
    "v_mfma_f32_16x16x32_bf16 v[160:163], v[70:73], v[224:227], 0\n"
    "v_mfma_f32_16x16x32_bf16 v[164:167], v[68:71], v[224:227], 0\n"
    "v_mfma_f32_16x16x32_bf16 v[160:163], v[74:77], v[224:227], v[160:163]\n"
    "v_mfma_f32_16x16x32_bf16 v[164:167], v[72:75], v[224:227], v[164:167]\n"
    "s_waitcnt lgkmcnt(2)\n"
    "v_mfma_f32_16x16x32_bf16 v[168:171], v[70:73], v[228:231], 0\n"
    "v_mfma_f32_16x16x32_bf16 v[172:175], v[68:71], v[228:231], 0\n"
    "s_nop 3\n"
    "v_cvt_pk_bf16_f32 v184, v160, v161\n"
    "v_cvt_pk_bf16_f32 v185, v162, v163\n"
    "v_mfma_f32_16x16x32_bf16 v[168:171], v[74:77], v[228:231], v[168:171]\n"
    "v_cvt_pk_bf16_f32 v186, v164, v165\n"
    "v_cvt_pk_bf16_f32 v187, v166, v167\n"
    "v_mfma_f32_16x16x32_bf16 v[172:175], v[72:75], v[228:231], v[172:175]\n"
    "v_accvgpr_write_b32 a32, v184\n"
    "v_accvgpr_write_b32 a33, v185\n"
    "s_waitcnt lgkmcnt(1)\n"
    "v_mfma_f32_16x16x32_bf16 v[160:163], v[70:73], v[232:235], 0\n"
    "v_accvgpr_write_b32 a34, v186\n"
    "v_accvgpr_write_b32 a35, v187\n"
    "v_mfma_f32_16x16x32_bf16 v[164:167], v[68:71], v[232:235], 0\n"
    "v_cvt_pk_bf16_f32 v184, v168, v169\n"
    "v_cvt_pk_bf16_f32 v185, v170, v171\n"
    "v_mfma_f32_16x16x32_bf16 v[160:163], v[74:77], v[232:235], v[160:163]\n"
    "v_cvt_pk_bf16_f32 v186, v172, v173\n"
    "v_cvt_pk_bf16_f32 v187, v174, v175\n"
    "v_mfma_f32_16x16x32_bf16 v[164:167], v[72:75], v[232:235], v[164:167]\n"
    "v_accvgpr_write_b32 a36, v184\n"
    "v_accvgpr_write_b32 a37, v185\n"
    "s_waitcnt lgkmcnt(0)\n"
    "v_mfma_f32_16x16x32_bf16 v[168:171], v[70:73], v[236:239], 0\n"
    "v_accvgpr_write_b32 a38, v186\n"
    "v_accvgpr_write_b32 a39, v187\n"
    "v_mfma_f32_16x16x32_bf16 v[172:175], v[68:71], v[236:239], 0\n"
    "v_cvt_pk_bf16_f32 v184, v160, v161\n"
    "v_cvt_pk_bf16_f32 v185, v162, v163\n"
    "v_mfma_f32_16x16x32_bf16 v[168:171], v[74:77], v[236:239], v[168:171]\n"
    "v_cvt_pk_bf16_f32 v186, v164, v165\n"
    "v_cvt_pk_bf16_f32 v187, v166, v167\n"
    "v_mfma_f32_16x16x32_bf16 v[172:175], v[72:75], v[236:239], v[172:175]\n"
    "v_accvgpr_write_b32 a40, v184\n"
    "v_accvgpr_write_b32 a41, v185\n"
    "v_accvgpr_write_b32 a42, v186\n"
    "v_accvgpr_write_b32 a43, v187\n"
    "s_nop 0\n"
    "v_cvt_pk_bf16_f32 v184, v168, v169\n"
    "v_cvt_pk_bf16_f32 v185, v170, v171\n"
    "s_nop 0\n"
    "v_cvt_pk_bf16_f32 v186, v172, v173\n"
    "v_cvt_pk_bf16_f32 v187, v174, v175\n"
    "v_accvgpr_write_b32 a44, v184\n"
    "v_accvgpr_write_b32 a45, v185\n"
    "v_accvgpr_write_b32 a46, v186\n"
    "v_accvgpr_write_b32 a47, v187\n"
    "s_waitcnt vmcnt(7)\n"
    "ds_read_b128 v[240:243], v112 offset:34816\n"
    "ds_read_b128 v[244:247], v113 offset:34816\n"
    "ds_read_b128 v[248:251], v112 offset:35840\n"
    "ds_read_b128 v[252:255], v113 offset:35840\n"
    "buffer_load_dwordx4 v37, s[12:15], s40 offen offset:3072 lds\n"
    "s_add_u32 m0, s28, 0x8000\n"
    "s_mov_b32 s40, 0x2000\n"
    "buffer_load_dwordx4 v34, s[12:15], s40 offen offset:0 lds\n"
    "buffer_load_dwordx4 v35, s[12:15], s40 offen offset:1024 lds\n"
    "buffer_load_dwordx4 v36, s[12:15], s40 offen offset:2048 lds\n"
    "s_waitcnt lgkmcnt(3)\n"
    "v_mfma_f32_16x16x32_bf16 v[160:163], v[70:73], v[240:243], 0\n"
    "v_mfma_f32_16x16x32_bf16 v[164:167], v[68:71], v[240:243], 0\n"
    "v_mfma_f32_16x16x32_bf16 v[160:163], v[74:77], v[240:243], v[160:163]\n"
    "v_mfma_f32_16x16x32_bf16 v[164:167], v[72:75], v[240:243], v[164:167]\n"
    "s_waitcnt lgkmcnt(2)\n"
    "v_mfma_f32_16x16x32_bf16 v[168:171], v[70:73], v[244:247], 0\n"
    "v_mfma_f32_16x16x32_bf16 v[172:175], v[68:71], v[244:247], 0\n"
    "s_nop 3\n"
    "v_cvt_pk_bf16_f32 v184, v160, v161\n"
    "v_cvt_pk_bf16_f32 v185, v162, v163\n"
    "v_mfma_f32_16x16x32_bf16 v[168:171], v[74:77], v[244:247], v[168:171]\n"
    "v_cvt_pk_bf16_f32 v186, v164, v165\n"
    "v_cvt_pk_bf16_f32 v187, v166, v167\n"
    "v_mfma_f32_16x16x32_bf16 v[172:175], v[72:75], v[244:247], v[172:175]\n"
    "v_accvgpr_write_b32 a48, v184\n"
    "v_accvgpr_write_b32 a49, v185\n"
    "s_waitcnt lgkmcnt(1)\n"
    "v_mfma_f32_16x16x32_bf16 v[160:163], v[70:73], v[248:251], 0\n"
    "v_accvgpr_write_b32 a50, v186\n"
    "v_accvgpr_write_b32 a51, v187\n"
    "v_mfma_f32_16x16x32_bf16 v[164:167], v[68:71], v[248:251], 0\n"
    "v_cvt_pk_bf16_f32 v184, v168, v169\n"
    "v_cvt_pk_bf16_f32 v185, v170, v171\n"
    "v_mfma_f32_16x16x32_bf16 v[160:163], v[74:77], v[248:251], v[160:163]\n"
    "v_cvt_pk_bf16_f32 v186, v172, v173\n"
    "v_cvt_pk_bf16_f32 v187, v174, v175\n"
    "v_mfma_f32_16x16x32_bf16 v[164:167], v[72:75], v[248:251], v[164:167]\n"
    "v_accvgpr_write_b32 a52, v184\n"
    "v_accvgpr_write_b32 a53, v185\n"
    "s_waitcnt lgkmcnt(0)\n"
    "v_mfma_f32_16x16x32_bf16 v[168:171], v[70:73], v[252:255], 0\n"
    "v_accvgpr_write_b32 a54, v186\n"
    "v_accvgpr_write_b32 a55, v187\n"
    "v_mfma_f32_16x16x32_bf16 v[172:175], v[68:71], v[252:255], 0\n"
    "v_cvt_pk_bf16_f32 v184, v160, v161\n"
    "v_cvt_pk_bf16_f32 v185, v162, v163\n"
    "v_mfma_f32_16x16x32_bf16 v[168:171], v[74:77], v[252:255], v[168:171]\n"
    "v_cvt_pk_bf16_f32 v186, v164, v165\n"
    "v_cvt_pk_bf16_f32 v187, v166, v167\n"
    "v_mfma_f32_16x16x32_bf16 v[172:175], v[72:75], v[252:255], v[172:175]\n"
    "v_accvgpr_write_b32 a56, v184\n"
    "v_accvgpr_write_b32 a57, v185\n"
    "v_accvgpr_write_b32 a58, v186\n"
    "v_accvgpr_write_b32 a59, v187\n"
    "s_nop 0\n"
    "v_cvt_pk_bf16_f32 v184, v168, v169\n"
    "v_cvt_pk_bf16_f32 v185, v170, v171\n"
    "s_nop 0\n"
    "v_cvt_pk_bf16_f32 v186, v172, v173\n"
    "v_cvt_pk_bf16_f32 v187, v174, v175\n"
    "v_accvgpr_write_b32 a60, v184\n"
    "v_accvgpr_write_b32 a61, v185\n"
    "v_accvgpr_write_b32 a62, v186\n"
    "v_accvgpr_write_b32 a63, v187\n"
    "MAIN_%=:\n"
    "s_waitcnt vmcnt(7)\n"
    "s_barrier\n"
    "s_memrealtime s[36:37]\n"
    "ds_read_b128 a[64:67], v24 offset:0\n"
    "ds_read_b128 a[68:71], v25 offset:0\n"
    "ds_read_b128 a[72:75], v24 offset:1024\n"
    "ds_read_b128 a[76:79], v25 offset:1024\n"
    "buffer_load_dwordx4 v37, s[12:15], s40 offen offset:3072 lds\n"
    "ds_read_b128 a[80:83], v24 offset:2048\n"
    "s_add_u32 m0, s28, 0xc000\n"
    "s_mov_b32 s40, 0x3000\n"
    "buffer_load_dwordx4 v34, s[12:15], s40 offen offset:0 lds\n"
    "s_waitcnt lgkmcnt(3)\n"
    "v_mfma_f32_16x16x32_bf16 v[0:3], a[64:67], v[192:195], 0\n"
    "ds_read_b128 a[84:87], v25 offset:2048\n"
    "v_mfma_f32_16x16x32_bf16 v[4:7], a[64:67], v[224:227], 0\n"
    "s_nop 7\n"
    "v_mfma_f32_16x16x32_bf16 v[0:3], a[68:71], v[196:199], v[0:3]\n"
    "ds_read_b128 a[88:91], v24 offset:3072\n"
    "buffer_load_dwordx4 v35, s[12:15], s40 offen offset:1024 lds\n"
    "v_mfma_f32_16x16x32_bf16 v[4:7], a[68:71], v[228:231], v[4:7]\n"
    "s_waitcnt lgkmcnt(3)\n"
    "s_nop 8\n"
    "v_mfma_f32_16x16x32_bf16 v[0:3], a[72:75], v[200:203], v[0:3]\n"
    "ds_read_b128 a[92:95], v25 offset:3072\n"
    "v_mfma_f32_16x16x32_bf16 v[4:7], a[72:75], v[232:235], v[4:7]\n"
    "s_nop 8\n"
    "v_mfma_f32_16x16x32_bf16 v[0:3], a[76:79], v[204:207], v[0:3]\n"
    "ds_read_b128 a[96:99], v24 offset:8192\n"
    "v_mfma_f32_16x16x32_bf16 v[4:7], a[76:79], v[236:239], v[4:7]\n"
    "s_waitcnt lgkmcnt(3)\n"
    "s_nop 8\n"
    "v_mfma_f32_16x16x32_bf16 v[0:3], a[80:83], v[208:211], v[0:3]\n"
    "ds_read_b128 a[100:103], v25 offset:8192\n"
    "v_mfma_f32_16x16x32_bf16 v[4:7], a[80:83], v[240:243], v[4:7]\n"
    "s_nop 7\n"
    "v_mfma_f32_16x16x32_bf16 v[0:3], a[84:87], v[212:215], v[0:3]\n"
    "ds_read_b128 a[104:107], v24 offset:9216\n"
    "buffer_load_dwordx4 v36, s[12:15], s40 offen offset:2048 lds\n"
    "v_mfma_f32_16x16x32_bf16 v[4:7], a[84:87], v[244:247], v[4:7]\n"
    "s_waitcnt lgkmcnt(3)\n"
    "s_nop 8\n"
    "v_mfma_f32_16x16x32_bf16 v[0:3], a[88:91], v[216:219], v[0:3]\n"
    "ds_read_b128 a[108:111], v25 offset:9216\n"
    "v_mfma_f32_16x16x32_bf16 v[4:7], a[88:91], v[248:251], v[4:7]\n"
    "s_nop 8\n"
    "v_mfma_f32_16x16x32_bf16 v[0:3], a[92:95], v[220:223], v[0:3]\n"
    "ds_read_b128 a[112:115], v24 offset:10240\n"
    "v_mfma_f32_16x16x32_bf16 v[4:7], a[92:95], v[252:255], v[4:7]\n"
    "s_waitcnt lgkmcnt(3)\n"
    "s_nop 8\n"
    "v_mfma_f32_16x16x32_bf16 v[8:11], a[96:99], v[192:195], 0\n"
    "ds_read_b128 a[116:119], v25 offset:10240\n"
    "v_mfma_f32_16x16x32_bf16 v[12:15], a[96:99], v[224:227], 0\n"
    "v_accvgpr_read_b32 v18, a212\n"
    "v_accvgpr_read_b32 v22, a213\n"
    "v_max3_f32 v16, v0, v1, v2\n"
    "v_max3_f32 v20, v4, v5, v6\n"
    "s_nop 1\n"
    "v_mfma_f32_16x16x32_bf16 v[8:11], a[100:103], v[196:199], v[8:11]\n"
    "ds_read_b128 a[120:123], v24 offset:11264\n"
    "v_max_f32 v16, v16, v3\n"
    "v_max_f32 v20, v20, v7\n"
    "v_mul_f32 v16, s45, v16\n"
    "v_mul_f32 v20, s45, v20\n"
    "v_mfma_f32_16x16x32_bf16 v[12:15], a[100:103], v[228:231], v[12:15]\n"
    "s_waitcnt lgkmcnt(3)\n"
    "v_max_f32 v16, v16, v18\n"
    "v_max_f32 v20, v20, v22\n"
    "v_mov_b32 v17, v16\n"
    "v_mov_b32 v21, v20\n"
    "s_nop 1\n"
    "v_mfma_f32_16x16x32_bf16 v[8:11], a[104:107], v[200:203], v[8:11]\n"
    "ds_read_b128 a[124:127], v25 offset:11264\n"
    "v_permlane16_swap_b32 v16, v17\n"
    "v_permlane16_swap_b32 v20, v21\n"
    "v_max_f32 v16, v16, v17\n"
    "v_max_f32 v20, v20, v21\n"
    "v_mfma_f32_16x16x32_bf16 v[12:15], a[104:107], v[232:235], v[12:15]\n"
    "v_mov_b32 v17, v16\n"
    "v_mov_b32 v21, v20\n"
    "s_nop 0\n"
    "v_permlane32_swap_b32 v16, v17\n"
    "v_permlane32_swap_b32 v20, v21\n"
    "s_nop 1\n"
    "buffer_load_dwordx4 v37, s[12:15], s40 offen offset:3072 lds\n"
    "v_mfma_f32_16x16x32_bf16 v[8:11], a[108:111], v[204:207], v[8:11]\n"
    "s_add_u32 m0, s28, 0x10000\n"
    "s_mov_b32 s40, 0x400\n"
    "v_max_f32 v16, v16, v17\n"
    "v_max_f32 v20, v20, v21\n"
    "v_fma_f32 v0, v0, s45, -v16\n"
    "v_fma_f32 v1, v1, s45, -v16\n"
    "v_mfma_f32_16x16x32_bf16 v[12:15], a[108:111], v[236:239], v[12:15]\n"
    "s_waitcnt lgkmcnt(2)\n"
    "v_fma_f32 v2, v2, s45, -v16\n"
    "v_fma_f32 v3, v3, s45, -v16\n"
    "v_fma_f32 v4, v4, s45, -v20\n"
    "v_fma_f32 v5, v5, s45, -v20\n"
    "s_nop 1\n"
    "v_mfma_f32_16x16x32_bf16 v[8:11], a[112:115], v[208:211], v[8:11]\n"
    "v_fma_f32 v6, v6, s45, -v20\n"
    "v_fma_f32 v7, v7, s45, -v20\n"
    "v_exp_f32 v0, v0\n"
    "v_mfma_f32_16x16x32_bf16 v[12:15], a[112:115], v[240:243], v[12:15]\n"
    "v_sub_f32 v19, 0, v16\n"
    "v_exp_f32 v1, v1\n"
    "v_sub_f32 v18, 0, v16\n"
    "s_nop 1\n"
    "v_mfma_f32_16x16x32_bf16 v[8:11], a[116:119], v[212:215], v[8:11]\n"
    "buffer_load_dwordx4 v34, s[12:15], s40 offen offset:0 lds\n"
    "v_exp_f32 v2, v2\n"
    "v_sub_f32 v17, 0, v16\n"
    "v_exp_f32 v3, v3\n"
    "v_mfma_f32_16x16x32_bf16 v[12:15], a[116:119], v[244:247], v[12:15]\n"
    "s_waitcnt lgkmcnt(0)\n"
    "v_sub_f32 v16, 0, v16\n"
    "v_exp_f32 v4, v4\n"
    "s_nop 1\n"
    "s_waitcnt lgkmcnt(0)\n"
    "s_lshl_b32 s38, s36, 1\n"
    "s_sub_u32 s38, 0, s38\n"
    "s_waitcnt vmcnt(9)\n"
    "s_barrier\n"
    "v_mfma_f32_16x16x32_bf16 v[8:11], a[120:123], v[216:219], v[8:11]\n"
    "ds_read_b128 a[64:67], v24 offset:16384\n"
    "ds_read_b128 a[68:71], v25 offset:16384\n"
    "ds_read_b128 a[72:75], v24 offset:17408\n"
    "ds_read_b128 a[76:79], v25 offset:17408\n"
    "v_sub_f32 v23, 0, v20\n"
    "v_exp_f32 v5, v5\n"
    "v_sub_f32 v22, 0, v20\n"
    "v_mfma_f32_16x16x32_bf16 v[12:15], a[120:123], v[248:251], v[12:15]\n"
    "v_exp_f32 v6, v6\n"
    "v_sub_f32 v21, 0, v20\n"
    "v_exp_f32 v7, v7\n"
    "s_nop 1\n"
    "v_mfma_f32_16x16x32_bf16 v[8:11], a[124:127], v[220:223], v[8:11]\n"
    "ds_read_b128 a[80:83], v24 offset:18432\n"
    "v_sub_f32 v20, 0, v20\n"
    "v_cvt_pk_bf16_f32 v40, v0, v1\n"
    "v_mfma_f32_16x16x32_bf16 v[12:15], a[124:127], v[252:255], v[12:15]\n"
    "s_waitcnt lgkmcnt(3)\n"
    "v_cvt_pk_bf16_f32 v41, v2, v3\n"
    "v_cvt_pk_bf16_f32 v44, v4, v5\n"
    "v_cvt_pk_bf16_f32 v45, v6, v7\n"
    "s_nop 1\n"
    "s_memrealtime s[36:37]\n"
    "v_mfma_f32_16x16x32_bf16 v[0:3], a[64:67], a[0:3], v[16:19]\n"
    "ds_read_b128 a[84:87], v25 offset:18432\n"
    "v_mfma_f32_16x16x32_bf16 v[4:7], a[64:67], a[32:35], v[20:23]\n"
    "v_fma_f32 v8, v8, s45, v16\n"
    "v_exp_f32 v8, v8\n"
    "s_memtime s[96:97]\n"
    "s_memrealtime s[98:99]\n"
    "v_mfma_f32_16x16x32_bf16 v[0:3], a[68:71], a[4:7], v[0:3]\n"
    "ds_read_b128 a[88:91], v24 offset:19456\n"
    "buffer_load_dwordx4 v35, s[12:15], s40 offen offset:1024 lds\n"
    "v_mfma_f32_16x16x32_bf16 v[4:7], a[68:71], a[36:39], v[4:7]\n"
    "v_fma_f32 v9, v9, s45, v16\n"
    "v_exp_f32 v9, v9\n"
    "s_waitcnt lgkmcnt(3)\n"
    "v_mfma_f32_16x16x32_bf16 v[0:3], a[72:75], a[8:11], v[0:3]\n"
    "ds_read_b128 a[92:95], v25 offset:19456\n"
    "v_cvt_pk_bf16_f32 v42, v8, v9\n"
    "STUB0_%=:\n"
    "s_cbranch_execz STUB4_%=\n"
    "v_mfma_f32_16x16x32_bf16 v[4:7], a[72:75], a[40:43], v[4:7]\n"
    "v_fma_f32 v10, v10, s45, v16\n"
    "v_exp_f32 v10, v10\n"
    "v_mfma_f32_16x16x32_bf16 v[0:3], a[76:79], a[12:15], v[0:3]\n"
    "ds_read_b128 a[96:99], v24 offset:24576\n"
    "v_mfma_f32_16x16x32_bf16 v[4:7], a[76:79], a[44:47], v[4:7]\n"
    "v_fma_f32 v11, v11, s45, v16\n"
    "v_exp_f32 v11, v11\n"
    "s_waitcnt lgkmcnt(3)\n"
    "v_mfma_f32_16x16x32_bf16 v[0:3], a[80:83], a[16:19], v[0:3]\n"
    "ds_read_b128 a[100:103], v25 offset:24576\n"
    "v_cvt_pk_bf16_f32 v43, v10, v11\n"
    "v_mfma_f32_16x16x32_bf16 v[4:7], a[80:83], a[48:51], v[4:7]\n"
    "v_fma_f32 v12, v12, s45, v20\n"
    "v_exp_f32 v12, v12\n"
    "v_mfma_f32_16x16x32_bf16 v[0:3], a[84:87], a[20:23], v[0:3]\n"
    "ds_read_b128 a[104:107], v24 offset:25600\n"
    "buffer_load_dwordx4 v36, s[12:15], s40 offen offset:2048 lds\n"
    "v_mfma_f32_16x16x32_bf16 v[4:7], a[84:87], a[52:55], v[4:7]\n"
    "v_fma_f32 v13, v13, s45, v20\n"
    "v_exp_f32 v13, v13\n"
    "s_waitcnt lgkmcnt(3)\n"
    "v_mfma_f32_16x16x32_bf16 v[0:3], a[88:91], a[24:27], v[0:3]\n"
    "ds_read_b128 a[108:111], v25 offset:25600\n"
    "v_cvt_pk_bf16_f32 v46, v12, v13\n"
    "v_mfma_f32_16x16x32_bf16 v[4:7], a[88:91], a[56:59], v[4:7]\n"
    "v_fma_f32 v14, v14, s45, v20\n"
    "v_exp_f32 v14, v14\n"
    "v_mfma_f32_16x16x32_bf16 v[0:3], a[92:95], a[28:31], v[0:3]\n"
    "ds_read_b128 a[112:115], v24 offset:26624\n"
    "v_mfma_f32_16x16x32_bf16 v[4:7], a[92:95], a[60:63], v[4:7]\n"
    "v_fma_f32 v15, v15, s45, v20\n"
    "v_exp_f32 v15, v15\n"
    "s_waitcnt lgkmcnt(3)\n"
    "s_lshl1_add_u32 s39, s36, s38\n"
    "PL0_%=:\n"
    "s_add_u32 s39, s39, 3\n"
    "s_cmpk_lt_u32 s39, 156\n"
    "s_cbranch_scc1 PL0_%=\n"
    "s_memrealtime s[36:37]\n"
    "v_mfma_f32_16x16x32_bf16 v[8:11], a[96:99], a[0:3], v[16:19]\n"
    "ds_read_b128 a[116:119], v25 offset:26624\n"
    "v_cvt_pk_bf16_f32 v47, v14, v15\n"
    "v_mfma_f32_16x16x32_bf16 v[12:15], a[96:99], a[32:35], v[20:23]\n"
    "v_exp_f32 v0, v0\n"
    "v_mfma_f32_16x16x32_bf16 v[8:11], a[100:103], a[4:7], v[8:11]\n"
    "ds_read_b128 a[120:123], v24 offset:27648\n"
    "v_mfma_f32_16x16x32_bf16 v[12:15], a[100:103], a[36:39], v[12:15]\n"
    "v_exp_f32 v1, v1\n"
    "s_waitcnt lgkmcnt(3)\n"
    "s_nop 0\n"
    "v_mfma_f32_16x16x32_bf16 v[8:11], a[104:107], a[8:11], v[8:11]\n"
    "ds_read_b128 a[124:127], v25 offset:27648\n"
    "v_cvt_pk_bf16_f32 v56, v0, v1\n"
    "v_mfma_f32_16x16x32_bf16 v[12:15], a[104:107], a[40:43], v[12:15]\n"
    "v_exp_f32 v2, v2\n"
    "s_nop 0\n"
    "s_waitcnt vmcnt(7)\n"
    "s_barrier\n"
    "v_mfma_f32_16x16x32_bf16 a[200:203], a[208:211], v[40:43], 0\n"
    "buffer_load_dwordx4 v37, s[12:15], s40 offen offset:3072 lds\n"
    "v_mfma_f32_16x16x32_bf16 v[8:11], a[108:111], a[12:15], v[8:11]\n"
    "ds_read_b128 a[64:67], v24 offset:32768\n"
    "s_add_u32 m0, s28, 0x14000\n"
    "v_mfma_f32_16x16x32_bf16 v[12:15], a[108:111], a[44:47], v[12:15]\n"
    "v_exp_f32 v3, v3\n"
    "s_waitcnt lgkmcnt(3)\n"
    "s_mov_b32 s40, 0x1400\n"
    "v_mfma_f32_16x16x32_bf16 v[8:11], a[112:115], a[16:19], v[8:11]\n"
    "ds_read_b128 a[68:71], v25 offset:32768\n"
    "v_cvt_pk_bf16_f32 v57, v2, v3\n"
    "v_mfma_f32_16x16x32_bf16 v[12:15], a[112:115], a[48:51], v[12:15]\n"
    "v_exp_f32 v4, v4\n"
    "v_mfma_f32_16x16x32_bf16 v[8:11], a[116:119], a[20:23], v[8:11]\n"
    "ds_read_b128 a[72:75], v24 offset:33792\n"
    "buffer_load_dwordx4 v34, s[12:15], s40 offen offset:0 lds\n"
    "v_mfma_f32_16x16x32_bf16 v[12:15], a[116:119], a[52:55], v[12:15]\n"
    "v_exp_f32 v5, v5\n"
    "s_waitcnt lgkmcnt(3)\n"
    "s_nop 0\n"
    "v_mfma_f32_16x16x32_bf16 v[8:11], a[120:123], a[24:27], v[8:11]\n"
    "ds_read_b128 a[76:79], v25 offset:33792\n"
    "v_cvt_pk_bf16_f32 v60, v4, v5\n"
    "v_mfma_f32_16x16x32_bf16 v[12:15], a[120:123], a[56:59], v[12:15]\n"
    "v_exp_f32 v6, v6\n"
    "s_nop 0\n"
    "v_mfma_f32_16x16x32_bf16 v[8:11], a[124:127], a[28:31], v[8:11]\n"
    "ds_read_b128 a[80:83], v24 offset:34816\n"
    "v_mfma_f32_16x16x32_bf16 v[12:15], a[124:127], a[60:63], v[12:15]\n"
    "v_exp_f32 v7, v7\n"
    "s_waitcnt lgkmcnt(3)\n"
    "s_nop 0\n"
    "s_lshl1_add_u32 s39, s36, s38\n"
    "PL1_%=:\n"
    "s_add_u32 s39, s39, 3\n"
    "s_cmpk_lt_u32 s39, 203\n"
    "s_cbranch_scc1 PL1_%=\n"
    "s_memrealtime s[36:37]\n"
    "v_mfma_f32_16x16x32_bf16 v[0:3], a[64:67], a[0:3], v[16:19]\n"
    "ds_read_b128 a[84:87], v25 offset:34816\n"
    "v_cvt_pk_bf16_f32 v61, v6, v7\n"
    "v_mfma_f32_16x16x32_bf16 v[4:7], a[64:67], a[32:35], v[20:23]\n"
    "v_exp_f32 v8, v8\n"
    "v_mfma_f32_16x16x32_bf16 v[0:3], a[68:71], a[4:7], v[0:3]\n"
    "ds_read_b128 a[88:91], v24 offset:35840\n"
    "buffer_load_dwordx4 v35, s[12:15], s40 offen offset:1024 lds\n"
    "v_mfma_f32_16x16x32_bf16 v[4:7], a[68:71], a[36:39], v[4:7]\n"
    "v_exp_f32 v9, v9\n"
    "s_waitcnt lgkmcnt(3)\n"
    "s_nop 0\n"
    "v_mfma_f32_16x16x32_bf16 v[0:3], a[72:75], a[8:11], v[0:3]\n"
    "ds_read_b128 a[92:95], v25 offset:35840\n"
    "v_cvt_pk_bf16_f32 v58, v8, v9\n"
    "v_mfma_f32_16x16x32_bf16 v[4:7], a[72:75], a[40:43], v[4:7]\n"
    "v_exp_f32 v10, v10\n"
    "s_nop 0\n"
    "v_mfma_f32_16x16x32_bf16 v[0:3], a[76:79], a[12:15], v[0:3]\n"
    "ds_read_b128 a[96:99], v24 offset:40960\n"
    "v_mfma_f32_16x16x32_bf16 v[4:7], a[76:79], a[44:47], v[4:7]\n"
    "v_exp_f32 v11, v11\n"
    "s_waitcnt lgkmcnt(3)\n"
    "s_nop 0\n"
    "v_mfma_f32_16x16x32_bf16 v[0:3], a[80:83], a[16:19], v[0:3]\n"
    "ds_read_b128 a[100:103], v25 offset:40960\n"
    "v_cvt_pk_bf16_f32 v59, v10, v11\n"
    "v_mfma_f32_16x16x32_bf16 v[4:7], a[80:83], a[48:51], v[4:7]\n"
    "v_exp_f32 v12, v12\n"
    "v_mfma_f32_16x16x32_bf16 v[0:3], a[84:87], a[20:23], v[0:3]\n"
    "ds_read_b128 a[104:107], v24 offset:41984\n"
    "buffer_load_dwordx4 v36, s[12:15], s40 offen offset:2048 lds\n"
    "v_mfma_f32_16x16x32_bf16 v[4:7], a[84:87], a[52:55], v[4:7]\n"
    "v_exp_f32 v13, v13\n"
    "s_waitcnt lgkmcnt(3)\n"
    "s_nop 0\n"
    "v_mfma_f32_16x16x32_bf16 v[0:3], a[88:91], a[24:27], v[0:3]\n"
    "ds_read_b128 a[108:111], v25 offset:41984\n"
    "v_cvt_pk_bf16_f32 v62, v12, v13\n"
    "v_mfma_f32_16x16x32_bf16 v[4:7], a[88:91], a[56:59], v[4:7]\n"
    "v_exp_f32 v14, v14\n"
    "s_nop 0\n"
    "v_mfma_f32_16x16x32_bf16 v[0:3], a[92:95], a[28:31], v[0:3]\n"
    "ds_read_b128 a[112:115], v24 offset:43008\n"
    "v_mfma_f32_16x16x32_bf16 v[4:7], a[92:95], a[60:63], v[4:7]\n"
    "v_exp_f32 v15, v15\n"
    "s_waitcnt lgkmcnt(3)\n"
    "s_nop 0\n"
    "s_lshl1_add_u32 s39, s36, s38\n"
    "PL2_%=:\n"
    "s_add_u32 s39, s39, 3\n"
    "s_cmpk_lt_u32 s39, 250\n"
    "s_cbranch_scc1 PL2_%=\n"
    "s_memrealtime s[36:37]\n"
    "v_mfma_f32_16x16x32_bf16 v[8:11], a[96:99], a[0:3], v[16:19]\n"
    "ds_read_b128 a[116:119], v25 offset:43008\n"
    "v_cvt_pk_bf16_f32 v63, v14, v15\n"
    "v_mfma_f32_16x16x32_bf16 v[12:15], a[96:99], a[32:35], v[20:23]\n"
    "v_exp_f32 v0, v0\n"
    "v_mfma_f32_16x16x32_bf16 v[8:11], a[100:103], a[4:7], v[8:11]\n"
    "ds_read_b128 a[120:123], v24 offset:44032\n"
    "buffer_load_dwordx4 v37, s[12:15], s40 offen offset:3072 lds\n"
    "v_mfma_f32_16x16x32_bf16 v[12:15], a[100:103], a[36:39], v[12:15]\n"
    "v_exp_f32 v1, v1\n"
    "s_waitcnt lgkmcnt(3)\n"
    "s_add_u32 m0, s28, 0x18000\n"
    "v_mfma_f32_16x16x32_bf16 v[8:11], a[104:107], a[8:11], v[8:11]\n"
    "ds_read_b128 a[124:127], v25 offset:44032\n"
    "v_cvt_pk_bf16_f32 v104, v0, v1\n"
    "v_mfma_f32_16x16x32_bf16 v[12:15], a[104:107], a[40:43], v[12:15]\n"
    "v_exp_f32 v2, v2\n"
    "s_mov_b32 s40, 0x2400\n"
    "v_mfma_f32_16x16x32_bf16 a[204:207], a[208:211], v[44:47], 0\n"
    "s_waitcnt vmcnt(8)\n"
    "s_barrier\n"
    "v_mfma_f32_16x16x32_bf16 a[200:203], a[208:211], v[56:59], a[200:203]\n"
    "buffer_load_dwordx4 v34, s[12:15], s40 offen offset:0 lds\n"
    "v_mfma_f32_16x16x32_bf16 v[8:11], a[108:111], a[12:15], v[8:11]\n"
    "ds_read_b128 a[64:67], v24 offset:49152\n"
    "v_mfma_f32_16x16x32_bf16 v[12:15], a[108:111], a[44:47], v[12:15]\n"
    "v_exp_f32 v3, v3\n"
    "s_waitcnt lgkmcnt(3)\n"
    "s_nop 0\n"
    "v_mfma_f32_16x16x32_bf16 v[8:11], a[112:115], a[16:19], v[8:11]\n"
    "ds_read_b128 a[68:71], v25 offset:49152\n"
    "v_cvt_pk_bf16_f32 v105, v2, v3\n"
    "v_mfma_f32_16x16x32_bf16 v[12:15], a[112:115], a[48:51], v[12:15]\n"
    "v_exp_f32 v4, v4\n"
    "v_mfma_f32_16x16x32_bf16 v[8:11], a[116:119], a[20:23], v[8:11]\n"
    "ds_read_b128 a[72:75], v24 offset:50176\n"
    "buffer_load_dwordx4 v35, s[12:15], s40 offen offset:1024 lds\n"
    "v_mfma_f32_16x16x32_bf16 v[12:15], a[116:119], a[52:55], v[12:15]\n"
    "v_exp_f32 v5, v5\n"
    "s_waitcnt lgkmcnt(3)\n"
    "s_nop 0\n"
    "v_mfma_f32_16x16x32_bf16 v[8:11], a[120:123], a[24:27], v[8:11]\n"
    "ds_read_b128 a[76:79], v25 offset:50176\n"
    "v_cvt_pk_bf16_f32 v108, v4, v5\n"
    "v_mfma_f32_16x16x32_bf16 v[12:15], a[120:123], a[56:59], v[12:15]\n"
    "v_exp_f32 v6, v6\n"
    "s_nop 0\n"
    "s_memtime s[92:93]\n"
    "s_memrealtime s[94:95]\n"
    "v_mfma_f32_16x16x32_bf16 v[8:11], a[124:127], a[28:31], v[8:11]\n"
    "ds_read_b128 a[80:83], v24 offset:51200\n"
    "v_mfma_f32_16x16x32_bf16 v[12:15], a[124:127], a[60:63], v[12:15]\n"
    "v_exp_f32 v7, v7\n"
    "s_waitcnt lgkmcnt(3)\n"
    "s_nop 0\n"
    "s_lshl1_add_u32 s39, s36, s38\n"
    "PL3_%=:\n"
    "s_add_u32 s39, s39, 3\n"
    "s_cmpk_lt_u32 s39, 297\n"
    "s_cbranch_scc1 PL3_%=\n"
    "s_memrealtime s[36:37]\n"
    "v_mfma_f32_16x16x32_bf16 v[0:3], a[64:67], a[0:3], v[16:19]\n"
    "ds_read_b128 a[84:87], v25 offset:51200\n"
    "v_cvt_pk_bf16_f32 v109, v6, v7\n"
    "v_mfma_f32_16x16x32_bf16 v[4:7], a[64:67], a[32:35], v[20:23]\n"
    "v_exp_f32 v8, v8\n"
    "v_mfma_f32_16x16x32_bf16 v[0:3], a[68:71], a[4:7], v[0:3]\n"
    "ds_read_b128 a[88:91], v24 offset:52224\n"
    "buffer_load_dwordx4 v36, s[12:15], s40 offen offset:2048 lds\n"
    "v_mfma_f32_16x16x32_bf16 v[4:7], a[68:71], a[36:39], v[4:7]\n"
    "v_exp_f32 v9, v9\n"
    "s_waitcnt lgkmcnt(3)\n"
    "s_nop 0\n"
    "v_mfma_f32_16x16x32_bf16 v[0:3], a[72:75], a[8:11], v[0:3]\n"
    "ds_read_b128 a[92:95], v25 offset:52224\n"
    "v_cvt_pk_bf16_f32 v106, v8, v9\n"
    "v_mfma_f32_16x16x32_bf16 v[4:7], a[72:75], a[40:43], v[4:7]\n"
    "v_exp_f32 v10, v10\n"
    "s_nop 0\n"
    "v_mfma_f32_16x16x32_bf16 v[0:3], a[76:79], a[12:15], v[0:3]\n"
    "ds_read_b128 a[96:99], v24 offset:57344\n"
    "v_mfma_f32_16x16x32_bf16 v[4:7], a[76:79], a[44:47], v[4:7]\n"
    "v_exp_f32 v11, v11\n"
    "s_waitcnt lgkmcnt(3)\n"
    "s_nop 0\n"
    "v_mfma_f32_16x16x32_bf16 v[0:3], a[80:83], a[16:19], v[0:3]\n"
    "ds_read_b128 a[100:103], v25 offset:57344\n"
    "v_cvt_pk_bf16_f32 v107, v10, v11\n"
    "v_mfma_f32_16x16x32_bf16 v[4:7], a[80:83], a[48:51], v[4:7]\n"
    "v_exp_f32 v12, v12\n"
    "s_sub_u32 s92, s92, s96\n"
    "s_sub_u32 s94, s94, s98\n"
    "s_mulk_i32 s92, 10\n"
    "s_mulk_i32 s94, 170\n"
    "v_mfma_f32_16x16x32_bf16 v[0:3], a[84:87], a[20:23], v[0:3]\n"
    "ds_read_b128 a[104:107], v24 offset:58368\n"
    "buffer_load_dwordx4 v37, s[12:15], s40 offen offset:3072 lds\n"
    "v_mfma_f32_16x16x32_bf16 v[4:7], a[84:87], a[52:55], v[4:7]\n"
    "v_exp_f32 v13, v13\n"
    "s_waitcnt lgkmcnt(3)\n"
    "s_add_u32 m0, s28, 0x1c000\n"
    "s_cmp_lt_u32 s92, s94\n"
    "s_cbranch_scc1 FAST_%=\n"
    "v_mfma_f32_16x16x32_bf16 v[0:3], a[88:91], a[24:27], v[0:3]\n"
    "ds_read_b128 a[108:111], v25 offset:58368\n"
    "v_cvt_pk_bf16_f32 v110, v12, v13\n"
    "v_mfma_f32_16x16x32_bf16 v[4:7], a[88:91], a[56:59], v[4:7]\n"
    "v_exp_f32 v14, v14\n"
    "s_mov_b32 s40, 0x3400\n"
    "v_mfma_f32_16x16x32_bf16 v[0:3], a[92:95], a[28:31], v[0:3]\n"
    "ds_read_b128 a[112:115], v24 offset:59392\n"
    "v_mfma_f32_16x16x32_bf16 v[4:7], a[92:95], a[60:63], v[4:7]\n"
    "v_exp_f32 v15, v15\n"
    "s_waitcnt lgkmcnt(3)\n"
    "s_nop 0\n"
    "s_lshl1_add_u32 s39, s36, s38\n"
    "PL4_%=:\n"
    "s_add_u32 s39, s39, 3\n"
    "s_cmpk_lt_u32 s39, 344\n"
    "s_cbranch_scc1 PL4_%=\n"
    "s_memrealtime s[36:37]\n"
    "v_mfma_f32_16x16x32_bf16 v[8:11], a[96:99], a[0:3], v[16:19]\n"
    "ds_read_b128 a[116:119], v25 offset:59392\n"
    "v_cvt_pk_bf16_f32 v111, v14, v15\n"
    "v_mfma_f32_16x16x32_bf16 v[12:15], a[96:99], a[32:35], v[20:23]\n"
    "v_exp_f32 v0, v0\n"
    "v_mfma_f32_16x16x32_bf16 v[8:11], a[100:103], a[4:7], v[8:11]\n"
    "ds_read_b128 a[120:123], v24 offset:60416\n"
    "v_mfma_f32_16x16x32_bf16 v[12:15], a[100:103], a[36:39], v[12:15]\n"
    "v_exp_f32 v1, v1\n"
    "s_waitcnt lgkmcnt(3)\n"
    "s_nop 0\n"
    "v_mfma_f32_16x16x32_bf16 v[8:11], a[104:107], a[8:11], v[8:11]\n"
    "ds_read_b128 a[124:127], v25 offset:60416\n"
    "v_cvt_pk_bf16_f32 v120, v0, v1\n"
    "v_mfma_f32_16x16x32_bf16 v[12:15], a[104:107], a[40:43], v[12:15]\n"
    "v_exp_f32 v2, v2\n"
    "s_nop 0\n"
    "v_mfma_f32_16x16x32_bf16 a[204:207], a[208:211], v[60:63], a[204:207]\n"
    "s_waitcnt vmcnt(8)\n"
    "s_barrier\n"
    "v_mfma_f32_16x16x32_bf16 a[200:203], a[208:211], v[104:107], a[200:203]\n"
    "buffer_load_dwordx4 v34, s[12:15], s40 offen offset:0 lds\n"
    "v_mfma_f32_16x16x32_bf16 v[8:11], a[108:111], a[12:15], v[8:11]\n"
    "ds_read_b128 a[64:67], v26 offset:0\n"
    "v_mfma_f32_16x16x32_bf16 v[12:15], a[108:111], a[44:47], v[12:15]\n"
    "v_exp_f32 v3, v3\n"
    "s_waitcnt lgkmcnt(3)\n"
    "s_nop 0\n"
    "v_mfma_f32_16x16x32_bf16 v[8:11], a[112:115], a[16:19], v[8:11]\n"
    "ds_read_b128 a[68:71], v27 offset:0\n"
    "v_cvt_pk_bf16_f32 v121, v2, v3\n"
    "v_mfma_f32_16x16x32_bf16 v[12:15], a[112:115], a[48:51], v[12:15]\n"
    "v_exp_f32 v4, v4\n"
    "v_mfma_f32_16x16x32_bf16 v[8:11], a[116:119], a[20:23], v[8:11]\n"
    "ds_read_b128 a[72:75], v26 offset:1024\n"
    "buffer_load_dwordx4 v35, s[12:15], s40 offen offset:1024 lds\n"
    "v_mfma_f32_16x16x32_bf16 v[12:15], a[116:119], a[52:55], v[12:15]\n"
    "v_exp_f32 v5, v5\n"
    "s_waitcnt lgkmcnt(3)\n"
    "s_nop 0\n"
    "v_mfma_f32_16x16x32_bf16 v[8:11], a[120:123], a[24:27], v[8:11]\n"
    "ds_read_b128 a[76:79], v27 offset:1024\n"
    "v_cvt_pk_bf16_f32 v124, v4, v5\n"
    "v_mfma_f32_16x16x32_bf16 v[12:15], a[120:123], a[56:59], v[12:15]\n"
    "v_exp_f32 v6, v6\n"
    "s_nop 0\n"
    "v_mfma_f32_16x16x32_bf16 v[8:11], a[124:127], a[28:31], v[8:11]\n"
    "ds_read_b128 a[80:83], v26 offset:2048\n"
    "v_mfma_f32_16x16x32_bf16 v[12:15], a[124:127], a[60:63], v[12:15]\n"
    "v_exp_f32 v7, v7\n"
    "s_waitcnt vmcnt(6) lgkmcnt(3)\n"
    "s_nop 0\n"
    "s_lshl1_add_u32 s39, s36, s38\n"
    "PL5_%=:\n"
    "s_add_u32 s39, s39, 3\n"
    "s_cmpk_lt_u32 s39, 391\n"
    "s_cbranch_scc1 PL5_%=\n"
    "s_memrealtime s[36:37]\n"
    "v_mfma_f32_16x16x32_bf16 v[0:3], a[64:67], a[0:3], v[16:19]\n"
    "ds_read_b128 a[84:87], v27 offset:2048\n"
    "v_cvt_pk_bf16_f32 v125, v6, v7\n"
    "v_mfma_f32_16x16x32_bf16 v[4:7], a[64:67], a[32:35], v[20:23]\n"
    "v_exp_f32 v8, v8\n"
    "v_mfma_f32_16x16x32_bf16 v[0:3], a[68:71], a[4:7], v[0:3]\n"
    "ds_read_b128 a[88:91], v26 offset:3072\n"
    "buffer_load_dwordx4 v36, s[12:15], s40 offen offset:2048 lds\n"
    "v_mfma_f32_16x16x32_bf16 v[4:7], a[68:71], a[36:39], v[4:7]\n"
    "v_exp_f32 v9, v9\n"
    "s_waitcnt lgkmcnt(3)\n"
    "s_nop 0\n"
    "v_mfma_f32_16x16x32_bf16 v[0:3], a[72:75], a[8:11], v[0:3]\n"
    "ds_read_b128 a[92:95], v27 offset:3072\n"
    "v_cvt_pk_bf16_f32 v122, v8, v9\n"
    "v_mfma_f32_16x16x32_bf16 v[4:7], a[72:75], a[40:43], v[4:7]\n"
    "v_exp_f32 v10, v10\n"
    "s_nop 0\n"
    "v_mfma_f32_16x16x32_bf16 v[0:3], a[76:79], a[12:15], v[0:3]\n"
    "ds_read_b128 a[96:99], v26 offset:8192\n"
    "v_mfma_f32_16x16x32_bf16 v[4:7], a[76:79], a[44:47], v[4:7]\n"
    "v_exp_f32 v11, v11\n"
    "s_waitcnt lgkmcnt(3)\n"
    "s_nop 0\n"
    "v_mfma_f32_16x16x32_bf16 v[0:3], a[80:83], a[16:19], v[0:3]\n"
    "ds_read_b128 a[100:103], v27 offset:8192\n"
    "v_cvt_pk_bf16_f32 v123, v10, v11\n"
    "v_mfma_f32_16x16x32_bf16 v[4:7], a[80:83], a[48:51], v[4:7]\n"
    "v_exp_f32 v12, v12\n"
    "v_mfma_f32_16x16x32_bf16 v[0:3], a[84:87], a[20:23], v[0:3]\n"
    "ds_read_b128 a[104:107], v26 offset:9216\n"
    "buffer_load_dwordx4 v37, s[12:15], s40 offen offset:3072 lds\n"
    "v_mfma_f32_16x16x32_bf16 v[4:7], a[84:87], a[52:55], v[4:7]\n"
    "v_exp_f32 v13, v13\n"
    "s_waitcnt lgkmcnt(3)\n"
    "s_add_u32 m0, s28, 0x20000\n"
    "v_mfma_f32_16x16x32_bf16 v[0:3], a[88:91], a[24:27], v[0:3]\n"
    "ds_read_b128 a[108:111], v27 offset:9216\n"
    "v_cvt_pk_bf16_f32 v126, v12, v13\n"
    "v_mfma_f32_16x16x32_bf16 v[4:7], a[88:91], a[56:59], v[4:7]\n"
    "v_exp_f32 v14, v14\n"
    "s_mov_b32 s40, 0x20000\n"
    "v_mfma_f32_16x16x32_bf16 v[0:3], a[92:95], a[28:31], v[0:3]\n"
    "ds_read_b128 a[112:115], v26 offset:10240\n"
    "v_mfma_f32_16x16x32_bf16 v[4:7], a[92:95], a[60:63], v[4:7]\n"
    "v_exp_f32 v15, v15\n"
    "s_waitcnt lgkmcnt(3)\n"
    "s_nop 0\n"
    "s_lshl1_add_u32 s39, s36, s38\n"
    "PL6_%=:\n"
    "s_add_u32 s39, s39, 3\n"
    "s_cmpk_lt_u32 s39, 438\n"
    "s_cbranch_scc1 PL6_%=\n"
    "s_memrealtime s[36:37]\n"
    "v_mfma_f32_16x16x32_bf16 v[8:11], a[96:99], a[0:3], v[16:19]\n"
    "ds_read_b128 a[116:119], v27 offset:10240\n"
    "v_cvt_pk_bf16_f32 v127, v14, v15\n"
    "v_mfma_f32_16x16x32_bf16 v[12:15], a[96:99], a[32:35], v[20:23]\n"
    "v_exp_f32 v0, v0\n"
    "v_mfma_f32_16x16x32_bf16 v[8:11], a[100:103], a[4:7], v[8:11]\n"
    "ds_read_b128 a[120:123], v26 offset:11264\n"
    "buffer_load_dwordx4 v34, s[12:15], s40 offen offset:0 lds\n"
    "v_mfma_f32_16x16x32_bf16 v[12:15], a[100:103], a[36:39], v[12:15]\n"
    "v_exp_f32 v1, v1\n"
    "s_waitcnt lgkmcnt(3)\n"
    "s_nop 0\n"
    "v_mfma_f32_16x16x32_bf16 v[8:11], a[104:107], a[8:11], v[8:11]\n"
    "ds_read_b128 a[124:127], v27 offset:11264\n"
    "v_cvt_pk_bf16_f32 v48, v0, v1\n"
    "v_mfma_f32_16x16x32_bf16 v[12:15], a[104:107], a[40:43], v[12:15]\n"
    "v_exp_f32 v2, v2\n"
    "s_nop 0\n"
    "v_mfma_f32_16x16x32_bf16 a[204:207], a[208:211], v[108:111], a[204:207]\n"
    "s_barrier\n"
    "v_mfma_f32_16x16x32_bf16 a[200:203], a[208:211], v[120:123], a[200:203]\n"
    "buffer_load_dwordx4 v35, s[12:15], s40 offen offset:1024 lds\n"
    "v_mfma_f32_16x16x32_bf16 v[8:11], a[108:111], a[12:15], v[8:11]\n"
    "ds_read_b128 a[64:67], v26 offset:16384\n"
    "v_mfma_f32_16x16x32_bf16 v[12:15], a[108:111], a[44:47], v[12:15]\n"
    "v_exp_f32 v3, v3\n"
    "s_waitcnt lgkmcnt(3)\n"
    "s_nop 0\n"
    "v_mfma_f32_16x16x32_bf16 v[8:11], a[112:115], a[16:19], v[8:11]\n"
    "ds_read_b128 a[68:71], v27 offset:16384\n"
    "v_cvt_pk_bf16_f32 v49, v2, v3\n"
    "v_mfma_f32_16x16x32_bf16 v[12:15], a[112:115], a[48:51], v[12:15]\n"
    "v_exp_f32 v4, v4\n"
    "v_mfma_f32_16x16x32_bf16 v[8:11], a[116:119], a[20:23], v[8:11]\n"
    "ds_read_b128 a[72:75], v26 offset:17408\n"
    "buffer_load_dwordx4 v36, s[12:15], s40 offen offset:2048 lds\n"
    "v_mfma_f32_16x16x32_bf16 v[12:15], a[116:119], a[52:55], v[12:15]\n"
    "v_exp_f32 v5, v5\n"
    "s_waitcnt lgkmcnt(3)\n"
    "s_nop 0\n"
    "v_mfma_f32_16x16x32_bf16 v[8:11], a[120:123], a[24:27], v[8:11]\n"
    "ds_read_b128 a[76:79], v27 offset:17408\n"
    "v_cvt_pk_bf16_f32 v52, v4, v5\n"
    "v_mfma_f32_16x16x32_bf16 v[12:15], a[120:123], a[56:59], v[12:15]\n"
    "v_exp_f32 v6, v6\n"
    "s_nop 0\n"
    "v_mfma_f32_16x16x32_bf16 v[8:11], a[124:127], a[28:31], v[8:11]\n"
    "ds_read_b128 a[80:83], v26 offset:18432\n"
    "v_mfma_f32_16x16x32_bf16 v[12:15], a[124:127], a[60:63], v[12:15]\n"
    "v_exp_f32 v7, v7\n"
    "s_waitcnt vmcnt(7) lgkmcnt(3)\n"
    "s_nop 0\n"
    "s_lshl1_add_u32 s39, s36, s38\n"
    "PL7_%=:\n"
    "s_add_u32 s39, s39, 3\n"
    "s_cmpk_lt_u32 s39, 485\n"
    "s_cbranch_scc1 PL7_%=\n"
    "s_memrealtime s[36:37]\n"
    "v_mfma_f32_16x16x32_bf16 v[0:3], a[64:67], a[0:3], v[16:19]\n"
    "ds_read_b128 a[84:87], v27 offset:18432\n"
    "v_cvt_pk_bf16_f32 v53, v6, v7\n"
    "v_mfma_f32_16x16x32_bf16 v[4:7], a[64:67], a[32:35], v[20:23]\n"
    "v_exp_f32 v8, v8\n"
    "v_mfma_f32_16x16x32_bf16 v[0:3], a[68:71], a[4:7], v[0:3]\n"
    "ds_read_b128 a[88:91], v26 offset:19456\n"
    "buffer_load_dwordx4 v37, s[12:15], s40 offen offset:3072 lds\n"
    "v_mfma_f32_16x16x32_bf16 v[4:7], a[68:71], a[36:39], v[4:7]\n"
    "v_exp_f32 v9, v9\n"
    "s_waitcnt lgkmcnt(3)\n"
    "s_add_u32 m0, s28, 0x24000\n"
    "v_mfma_f32_16x16x32_bf16 v[0:3], a[72:75], a[8:11], v[0:3]\n"
    "ds_read_b128 a[92:95], v27 offset:19456\n"
    "v_cvt_pk_bf16_f32 v50, v8, v9\n"
    "v_mfma_f32_16x16x32_bf16 v[4:7], a[72:75], a[40:43], v[4:7]\n"
    "v_exp_f32 v10, v10\n"
    "s_mov_b32 s40, 0x21000\n"
    "v_mfma_f32_16x16x32_bf16 v[0:3], a[76:79], a[12:15], v[0:3]\n"
    "ds_read_b128 a[96:99], v26 offset:24576\n"
    "v_mfma_f32_16x16x32_bf16 v[4:7], a[76:79], a[44:47], v[4:7]\n"
    "v_exp_f32 v11, v11\n"
    "s_waitcnt lgkmcnt(3)\n"
    "s_nop 0\n"
    "v_mfma_f32_16x16x32_bf16 v[0:3], a[80:83], a[16:19], v[0:3]\n"
    "ds_read_b128 a[100:103], v27 offset:24576\n"
    "v_cvt_pk_bf16_f32 v51, v10, v11\n"
    "v_mfma_f32_16x16x32_bf16 v[4:7], a[80:83], a[48:51], v[4:7]\n"
    "v_exp_f32 v12, v12\n"
    "v_mfma_f32_16x16x32_bf16 v[0:3], a[84:87], a[20:23], v[0:3]\n"
    "ds_read_b128 a[104:107], v26 offset:25600\n"
    "ds_read_b128 a[108:111], v27 offset:25600\n"
    "buffer_load_dwordx4 v34, s[12:15], s40 offen offset:0 lds\n"
    "v_mfma_f32_16x16x32_bf16 v[4:7], a[84:87], a[52:55], v[4:7]\n"
    "v_exp_f32 v13, v13\n"
    "s_waitcnt lgkmcnt(4)\n"
    "s_nop 0\n"
    "v_mfma_f32_16x16x32_bf16 v[0:3], a[88:91], a[24:27], v[0:3]\n"
    "ds_read_b128 a[112:115], v26 offset:26624\n"
    "ds_read_b128 a[116:119], v27 offset:26624\n"
    "v_cvt_pk_bf16_f32 v54, v12, v13\n"
    "v_mfma_f32_16x16x32_bf16 v[4:7], a[88:91], a[56:59], v[4:7]\n"
    "v_exp_f32 v14, v14\n"
    "s_nop 0\n"
    "v_mfma_f32_16x16x32_bf16 v[0:3], a[92:95], a[28:31], v[0:3]\n"
    "ds_read_b128 a[120:123], v26 offset:27648\n"
    "ds_read_b128 a[124:127], v27 offset:27648\n"
    "v_mfma_f32_16x16x32_bf16 v[4:7], a[92:95], a[60:63], v[4:7]\n"
    "v_exp_f32 v15, v15\n"
    "s_waitcnt lgkmcnt(6)\n"
    "s_nop 0\n"
    "s_lshl1_add_u32 s39, s36, s38\n"
    "PL8_%=:\n"
    "s_add_u32 s39, s39, 3\n"
    "s_cmpk_lt_u32 s39, 532\n"
    "s_cbranch_scc1 PL8_%=\n"
    "s_memrealtime s[36:37]\n"
    "v_mfma_f32_16x16x32_bf16 a[204:207], a[208:211], v[124:127], a[204:207]\n"
    "s_barrier\n"
    "v_mfma_f32_16x16x32_bf16 v[8:11], a[96:99], a[0:3], v[16:19]\n"
    "ds_read_b128 a[64:67], v26 offset:32768\n"
    "ds_read_b128 a[68:71], v27 offset:32768\n"
    "v_cvt_pk_bf16_f32 v55, v14, v15\n"
    "v_mfma_f32_16x16x32_bf16 v[12:15], a[96:99], a[32:35], v[20:23]\n"
    "v_exp_f32 v0, v0\n"
    "v_mfma_f32_16x16x32_bf16 v[8:11], a[100:103], a[4:7], v[8:11]\n"
    "ds_read_b128 a[72:75], v26 offset:33792\n"
    "ds_read_b128 a[76:79], v27 offset:33792\n"
    "v_mfma_f32_16x16x32_bf16 v[12:15], a[100:103], a[36:39], v[12:15]\n"
    "v_exp_f32 v1, v1\n"
    "s_waitcnt lgkmcnt(8)\n"
    "s_nop 0\n"
    "v_mfma_f32_16x16x32_bf16 v[8:11], a[104:107], a[8:11], v[8:11]\n"
    "ds_read_b128 a[80:83], v26 offset:34816\n"
    "ds_read_b128 a[84:87], v27 offset:34816\n"
    "v_cvt_pk_bf16_f32 v64, v0, v1\n"
    "v_mfma_f32_16x16x32_bf16 v[12:15], a[104:107], a[40:43], v[12:15]\n"
    "v_exp_f32 v2, v2\n"
    "s_nop 0\n"
    "v_mfma_f32_16x16x32_bf16 a[200:203], a[208:211], v[48:51], a[200:203]\n"
    "buffer_load_dwordx4 v35, s[12:15], s40 offen offset:1024 lds\n"
    "v_mfma_f32_16x16x32_bf16 v[8:11], a[108:111], a[12:15], v[8:11]\n"
    "ds_read_b128 a[88:91], v26 offset:35840\n"
    "ds_read_b128 a[92:95], v27 offset:35840\n"
    "v_mfma_f32_16x16x32_bf16 v[12:15], a[108:111], a[44:47], v[12:15]\n"
    "v_exp_f32 v3, v3\n"
    "s_waitcnt lgkmcnt(10)\n"
    "s_nop 0\n"
    "v_mfma_f32_16x16x32_bf16 v[8:11], a[112:115], a[16:19], v[8:11]\n"
    "ds_read_b128 a[96:99], v26 offset:40960\n"
    "ds_read_b128 a[100:103], v27 offset:40960\n"
    "v_cvt_pk_bf16_f32 v65, v2, v3\n"
    "v_mfma_f32_16x16x32_bf16 v[12:15], a[112:115], a[48:51], v[12:15]\n"
    "v_exp_f32 v4, v4\n"
    "v_mfma_f32_16x16x32_bf16 v[8:11], a[116:119], a[20:23], v[8:11]\n"
    "ds_read_b128 a[104:107], v26 offset:41984\n"
    "buffer_load_dwordx4 v36, s[12:15], s40 offen offset:2048 lds\n"
    "v_mfma_f32_16x16x32_bf16 v[12:15], a[116:119], a[52:55], v[12:15]\n"
    "v_exp_f32 v5, v5\n"
    "s_waitcnt lgkmcnt(11)\n"
    "s_nop 0\n"
    "v_mfma_f32_16x16x32_bf16 v[8:11], a[120:123], a[24:27], v[8:11]\n"
    "ds_read_b128 a[108:111], v27 offset:41984\n"
    "v_cvt_pk_bf16_f32 v68, v4, v5\n"
    "v_mfma_f32_16x16x32_bf16 v[12:15], a[120:123], a[56:59], v[12:15]\n"
    "v_exp_f32 v6, v6\n"
    "s_nop 0\n"
    "v_mfma_f32_16x16x32_bf16 v[8:11], a[124:127], a[28:31], v[8:11]\n"
    "ds_read_b128 a[112:115], v26 offset:43008\n"
    "v_mfma_f32_16x16x32_bf16 v[12:15], a[124:127], a[60:63], v[12:15]\n"
    "v_exp_f32 v7, v7\n"
    "s_waitcnt vmcnt(7) lgkmcnt(11)\n"
    "s_nop 0\n"
    "s_lshl1_add_u32 s39, s36, s38\n"
    "PL9_%=:\n"
    "s_add_u32 s39, s39, 3\n"
    "s_cmpk_lt_u32 s39, 579\n"
    "s_cbranch_scc1 PL9_%=\n"
    "s_memrealtime s[36:37]\n"
    "v_mfma_f32_16x16x32_bf16 v[0:3], a[64:67], a[0:3], v[16:19]\n"
    "ds_read_b128 a[116:119], v27 offset:43008\n"
    "v_cvt_pk_bf16_f32 v69, v6, v7\n"
    "v_mfma_f32_16x16x32_bf16 v[4:7], a[64:67], a[32:35], v[20:23]\n"
    "v_exp_f32 v8, v8\n"
    "v_mfma_f32_16x16x32_bf16 v[0:3], a[68:71], a[4:7], v[0:3]\n"
    "ds_read_b128 a[120:123], v26 offset:44032\n"
    "buffer_load_dwordx4 v37, s[12:15], s40 offen offset:3072 lds\n"
    "v_mfma_f32_16x16x32_bf16 v[4:7], a[68:71], a[36:39], v[4:7]\n"
    "v_exp_f32 v9, v9\n"
    "s_waitcnt lgkmcnt(11)\n"
    "s_add_u32 m0, s28, 0x0\n"
    "v_mfma_f32_16x16x32_bf16 v[0:3], a[72:75], a[8:11], v[0:3]\n"
    "ds_read_b128 a[124:127], v27 offset:44032\n"
    "v_cvt_pk_bf16_f32 v66, v8, v9\n"
    "v_mfma_f32_16x16x32_bf16 v[4:7], a[72:75], a[40:43], v[4:7]\n"
    "v_exp_f32 v10, v10\n"
    "s_mov_b32 s40, 0x22000\n"
    "v_mfma_f32_16x16x32_bf16 a[204:207], a[208:211], v[52:55], a[204:207]\n"
    "s_barrier\n"
    "v_mfma_f32_16x16x32_bf16 v[0:3], a[76:79], a[12:15], v[0:3]\n"
    "ds_read_b128 a[64:67], v26 offset:49152\n"
    "v_mfma_f32_16x16x32_bf16 v[4:7], a[76:79], a[44:47], v[4:7]\n"
    "v_exp_f32 v11, v11\n"
    "s_waitcnt lgkmcnt(11)\n"
    "s_nop 0\n"
    "v_mfma_f32_16x16x32_bf16 v[0:3], a[80:83], a[16:19], v[0:3]\n"
    "ds_read_b128 a[68:71], v27 offset:49152\n"
    "v_cvt_pk_bf16_f32 v67, v10, v11\n"
    "v_mfma_f32_16x16x32_bf16 v[4:7], a[80:83], a[48:51], v[4:7]\n"
    "v_exp_f32 v12, v12\n"
    "v_mfma_f32_16x16x32_bf16 v[0:3], a[84:87], a[20:23], v[0:3]\n"
    "ds_read_b128 a[72:75], v26 offset:50176\n"
    "buffer_load_dwordx4 v34, s[12:15], s40 offen offset:0 lds\n"
    "v_mfma_f32_16x16x32_bf16 v[4:7], a[84:87], a[52:55], v[4:7]\n"
    "v_exp_f32 v13, v13\n"
    "s_waitcnt lgkmcnt(11)\n"
    "s_nop 0\n"
    "v_mfma_f32_16x16x32_bf16 v[0:3], a[88:91], a[24:27], v[0:3]\n"
    "ds_read_b128 a[76:79], v27 offset:50176\n"
    "v_cvt_pk_bf16_f32 v70, v12, v13\n"
    "v_mfma_f32_16x16x32_bf16 v[4:7], a[88:91], a[56:59], v[4:7]\n"
    "v_exp_f32 v14, v14\n"
    "s_nop 0\n"
    "v_mfma_f32_16x16x32_bf16 v[0:3], a[92:95], a[28:31], v[0:3]\n"
    "ds_read_b128 a[80:83], v26 offset:51200\n"
    "s_waitcnt lgkmcnt(5)\n"
    "v_mfma_f32_16x16x32_bf16 v[4:7], a[92:95], a[60:63], v[4:7]\n"
    "v_exp_f32 v15, v15\n"
    "s_nop 0\n"
    "s_lshl1_add_u32 s39, s36, s38\n"
    "PL10_%=:\n"
    "s_add_u32 s39, s39, 3\n"
    "s_cmpk_lt_u32 s39, 626\n"
    "s_cbranch_scc1 PL10_%=\n"
    "s_memrealtime s[36:37]\n"
    "v_mfma_f32_16x16x32_bf16 v[8:11], a[96:99], a[0:3], v[16:19]\n"
    "ds_read_b128 a[84:87], v27 offset:51200\n"
    "v_cvt_pk_bf16_f32 v71, v14, v15\n"
    "v_mfma_f32_16x16x32_bf16 v[12:15], a[96:99], a[32:35], v[20:23]\n"
    "v_exp_f32 v0, v0\n"
    "v_mfma_f32_16x16x32_bf16 v[8:11], a[100:103], a[4:7], v[8:11]\n"
    "ds_read_b128 a[88:91], v26 offset:52224\n"
    "buffer_load_dwordx4 v35, s[12:15], s40 offen offset:1024 lds\n"
    "v_mfma_f32_16x16x32_bf16 v[12:15], a[100:103], a[36:39], v[12:15]\n"
    "v_exp_f32 v1, v1\n"
    "s_nop 0\n"
    "v_mfma_f32_16x16x32_bf16 v[8:11], a[104:107], a[8:11], v[8:11]\n"
    "ds_read_b128 a[92:95], v27 offset:52224\n"
    "v_cvt_pk_bf16_f32 v112, v0, v1\n"
    "v_mfma_f32_16x16x32_bf16 v[12:15], a[104:107], a[40:43], v[12:15]\n"
    "v_exp_f32 v2, v2\n"
    "s_nop 0\n"
    "v_mfma_f32_16x16x32_bf16 a[200:203], a[208:211], v[64:67], a[200:203]\n"
    "buffer_load_dwordx4 v36, s[12:15], s40 offen offset:2048 lds\n"
    "v_mfma_f32_16x16x32_bf16 v[8:11], a[108:111], a[12:15], v[8:11]\n"
    "ds_read_b128 a[96:99], v26 offset:57344\n"
    "STUB1_%=:\n"
    "s_cbranch_execz STUB5_%=\n"
    "v_mfma_f32_16x16x32_bf16 v[12:15], a[108:111], a[44:47], v[12:15]\n"
    "v_exp_f32 v3, v3\n"
    "s_nop 0\n"
    "v_mfma_f32_16x16x32_bf16 v[8:11], a[112:115], a[16:19], v[8:11]\n"
    "ds_read_b128 a[100:103], v27 offset:57344\n"
    "v_cvt_pk_bf16_f32 v113, v2, v3\n"
    "v_mfma_f32_16x16x32_bf16 v[12:15], a[112:115], a[48:51], v[12:15]\n"
    "v_exp_f32 v4, v4\n"
    "v_mfma_f32_16x16x32_bf16 v[8:11], a[116:119], a[20:23], v[8:11]\n"
    "ds_read_b128 a[104:107], v26 offset:58368\n"
    "buffer_load_dwordx4 v37, s[12:15], s40 offen offset:3072 lds\n"
    "v_mfma_f32_16x16x32_bf16 v[12:15], a[116:119], a[52:55], v[12:15]\n"
    "v_exp_f32 v5, v5\n"
    "s_add_u32 m0, s28, 0x4000\n"
    "v_mfma_f32_16x16x32_bf16 v[8:11], a[120:123], a[24:27], v[8:11]\n"
    "ds_read_b128 a[108:111], v27 offset:58368\n"
    "v_cvt_pk_bf16_f32 v116, v4, v5\n"
    "v_mfma_f32_16x16x32_bf16 v[12:15], a[120:123], a[56:59], v[12:15]\n"
    "v_exp_f32 v6, v6\n"
    "s_mov_b32 s40, 0x23000\n"
    "v_mfma_f32_16x16x32_bf16 v[8:11], a[124:127], a[28:31], v[8:11]\n"
    "ds_read_b128 a[112:115], v26 offset:59392\n"
    "s_waitcnt vmcnt(8) lgkmcnt(5)\n"
    "v_mfma_f32_16x16x32_bf16 v[12:15], a[124:127], a[60:63], v[12:15]\n"
    "v_exp_f32 v7, v7\n"
    "s_nop 0\n"
    "s_lshl1_add_u32 s39, s36, s38\n"
    "PL11_%=:\n"
    "s_add_u32 s39, s39, 3\n"
    "s_cmpk_lt_u32 s39, 673\n"
    "s_cbranch_scc1 PL11_%=\n"
    "s_memrealtime s[36:37]\n"
    "v_mfma_f32_16x16x32_bf16 v[0:3], a[64:67], a[0:3], v[16:19]\n"
    "ds_read_b128 a[116:119], v27 offset:59392\n"
    "v_cvt_pk_bf16_f32 v117, v6, v7\n"
    "v_mfma_f32_16x16x32_bf16 v[4:7], a[64:67], a[32:35], v[20:23]\n"
    "v_exp_f32 v8, v8\n"
    "v_mfma_f32_16x16x32_bf16 v[0:3], a[68:71], a[4:7], v[0:3]\n"
    "ds_read_b128 a[120:123], v26 offset:60416\n"
    "buffer_load_dwordx4 v34, s[12:15], s40 offen offset:0 lds\n"
    "v_mfma_f32_16x16x32_bf16 v[4:7], a[68:71], a[36:39], v[4:7]\n"
    "v_exp_f32 v9, v9\n"
    "v_mfma_f32_16x16x32_bf16 v[0:3], a[72:75], a[8:11], v[0:3]\n"
    "ds_read_b128 a[124:127], v27 offset:60416\n"
    "v_cvt_pk_bf16_f32 v114, v8, v9\n"
    "v_mfma_f32_16x16x32_bf16 v[4:7], a[72:75], a[40:43], v[4:7]\n"
    "v_exp_f32 v10, v10\n"
    "v_mfma_f32_16x16x32_bf16 a[204:207], a[208:211], v[68:71], a[204:207]\n"
    "s_barrier\n"
    "v_mfma_f32_16x16x32_bf16 v[0:3], a[76:79], a[12:15], v[0:3]\n"
    "ds_read_b128 a[64:67], v28 offset:0\n"
    "v_mfma_f32_16x16x32_bf16 v[4:7], a[76:79], a[44:47], v[4:7]\n"
    "v_exp_f32 v11, v11\n"
    "v_mfma_f32_16x16x32_bf16 v[0:3], a[80:83], a[16:19], v[0:3]\n"
    "ds_read_b128 a[68:71], v29 offset:0\n"
    "v_cvt_pk_bf16_f32 v115, v10, v11\n"
    "v_mfma_f32_16x16x32_bf16 v[4:7], a[80:83], a[48:51], v[4:7]\n"
    "v_exp_f32 v12, v12\n"
    "v_mfma_f32_16x16x32_bf16 v[0:3], a[84:87], a[20:23], v[0:3]\n"
    "ds_read_b128 a[72:75], v28 offset:1024\n"
    "buffer_load_dwordx4 v35, s[12:15], s40 offen offset:1024 lds\n"
    "v_mfma_f32_16x16x32_bf16 v[4:7], a[84:87], a[52:55], v[4:7]\n"
    "v_exp_f32 v13, v13\n"
    "v_mfma_f32_16x16x32_bf16 v[0:3], a[88:91], a[24:27], v[0:3]\n"
    "ds_read_b128 a[76:79], v29 offset:1024\n"
    "v_cvt_pk_bf16_f32 v118, v12, v13\n"
    "v_mfma_f32_16x16x32_bf16 v[4:7], a[88:91], a[56:59], v[4:7]\n"
    "v_exp_f32 v14, v14\n"
    "v_mfma_f32_16x16x32_bf16 v[0:3], a[92:95], a[28:31], v[0:3]\n"
    "ds_read_b128 a[80:83], v28 offset:2048\n"
    "s_waitcnt lgkmcnt(5)\n"
    "v_mfma_f32_16x16x32_bf16 v[4:7], a[92:95], a[60:63], v[4:7]\n"
    "v_exp_f32 v15, v15\n"
    "v_mfma_f32_16x16x32_bf16 v[8:11], a[96:99], a[0:3], v[16:19]\n"
    "ds_read_b128 a[84:87], v29 offset:2048\n"
    "v_cvt_pk_bf16_f32 v119, v14, v15\n"
    "v_mfma_f32_16x16x32_bf16 v[12:15], a[96:99], a[32:35], v[20:23]\n"
    "v_exp_f32 v0, v0\n"
    "v_mfma_f32_16x16x32_bf16 v[8:11], a[100:103], a[4:7], v[8:11]\n"
    "ds_read_b128 a[88:91], v28 offset:3072\n"
    "v_mfma_f32_16x16x32_bf16 v[12:15], a[100:103], a[36:39], v[12:15]\n"
    "v_exp_f32 v1, v1\n"
    "v_mfma_f32_16x16x32_bf16 v[8:11], a[104:107], a[8:11], v[8:11]\n"
    "ds_read_b128 a[92:95], v29 offset:3072\n"
    "v_cvt_pk_bf16_f32 v128, v0, v1\n"
    "v_mfma_f32_16x16x32_bf16 v[12:15], a[104:107], a[40:43], v[12:15]\n"
    "v_exp_f32 v2, v2\n"
    "v_mfma_f32_16x16x32_bf16 a[200:203], a[208:211], v[112:115], a[200:203]\n"
    "buffer_load_dwordx4 v36, s[12:15], s40 offen offset:2048 lds\n"
    "v_mfma_f32_16x16x32_bf16 v[8:11], a[108:111], a[12:15], v[8:11]\n"
    "ds_read_b128 a[96:99], v28 offset:8192\n"
    "v_mfma_f32_16x16x32_bf16 v[12:15], a[108:111], a[44:47], v[12:15]\n"
    "v_exp_f32 v3, v3\n"
    "v_mfma_f32_16x16x32_bf16 v[8:11], a[112:115], a[16:19], v[8:11]\n"
    "ds_read_b128 a[100:103], v29 offset:8192\n"
    "v_cvt_pk_bf16_f32 v129, v2, v3\n"
    "v_mfma_f32_16x16x32_bf16 v[12:15], a[112:115], a[48:51], v[12:15]\n"
    "v_exp_f32 v4, v4\n"
    "v_mfma_f32_16x16x32_bf16 v[8:11], a[116:119], a[20:23], v[8:11]\n"
    "ds_read_b128 a[104:107], v28 offset:9216\n"
    "buffer_load_dwordx4 v37, s[12:15], s40 offen offset:3072 lds\n"
    "v_mfma_f32_16x16x32_bf16 v[12:15], a[116:119], a[52:55], v[12:15]\n"
    "v_exp_f32 v5, v5\n"
    "v_mfma_f32_16x16x32_bf16 v[8:11], a[120:123], a[24:27], v[8:11]\n"
    "ds_read_b128 a[108:111], v29 offset:9216\n"
    "v_cvt_pk_bf16_f32 v132, v4, v5\n"
    "v_mfma_f32_16x16x32_bf16 v[12:15], a[120:123], a[56:59], v[12:15]\n"
    "v_exp_f32 v6, v6\n"
    "v_mfma_f32_16x16x32_bf16 v[8:11], a[124:127], a[28:31], v[8:11]\n"
    "ds_read_b128 a[112:115], v28 offset:10240\n"
    "s_waitcnt vmcnt(8) lgkmcnt(5)\n"
    "v_mfma_f32_16x16x32_bf16 v[12:15], a[124:127], a[60:63], v[12:15]\n"
    "v_exp_f32 v7, v7\n"
    "s_lshl1_add_u32 s39, s36, s38\n"
    "PL12_%=:\n"
    "s_add_u32 s39, s39, 3\n"
    "s_cmpk_lt_u32 s39, 726\n"
    "s_cbranch_scc1 PL12_%=\n"
    "s_memrealtime s[36:37]\n"
    "v_mfma_f32_16x16x32_bf16 v[0:3], a[64:67], a[0:3], v[16:19]\n"
    "ds_read_b128 a[116:119], v29 offset:10240\n"
    "v_cvt_pk_bf16_f32 v133, v6, v7\n"
    "v_mfma_f32_16x16x32_bf16 v[4:7], a[64:67], a[32:35], v[20:23]\n"
    "v_exp_f32 v8, v8\n"
    "v_mfma_f32_16x16x32_bf16 v[0:3], a[68:71], a[4:7], v[0:3]\n"
    "ds_read_b128 a[120:123], v28 offset:11264\n"
    "s_add_u32 m0, s28, 0x8000\n"
    "s_mov_b32 s40, 0x20400\n"
    "buffer_load_dwordx4 v34, s[12:15], s40 offen offset:0 lds\n"
    "v_mfma_f32_16x16x32_bf16 v[4:7], a[68:71], a[36:39], v[4:7]\n"
    "v_exp_f32 v9, v9\n"
    "v_mfma_f32_16x16x32_bf16 v[0:3], a[72:75], a[8:11], v[0:3]\n"
    "ds_read_b128 a[124:127], v29 offset:11264\n"
    "v_cvt_pk_bf16_f32 v130, v8, v9\n"
    "v_mfma_f32_16x16x32_bf16 v[4:7], a[72:75], a[40:43], v[4:7]\n"
    "v_exp_f32 v10, v10\n"
    "v_mfma_f32_16x16x32_bf16 a[204:207], a[208:211], v[116:119], a[204:207]\n"
    "s_barrier\n"
    "v_mfma_f32_16x16x32_bf16 v[0:3], a[76:79], a[12:15], v[0:3]\n"
    "ds_read_b128 a[64:67], v28 offset:16384\n"
    "v_mfma_f32_16x16x32_bf16 v[4:7], a[76:79], a[44:47], v[4:7]\n"
    "v_exp_f32 v11, v11\n"
    "v_mfma_f32_16x16x32_bf16 v[0:3], a[80:83], a[16:19], v[0:3]\n"
    "ds_read_b128 a[68:71], v29 offset:16384\n"
    "v_cvt_pk_bf16_f32 v131, v10, v11\n"
    "v_mfma_f32_16x16x32_bf16 v[4:7], a[80:83], a[48:51], v[4:7]\n"
    "v_exp_f32 v12, v12\n"
    "v_mfma_f32_16x16x32_bf16 v[0:3], a[84:87], a[20:23], v[0:3]\n"
    "ds_read_b128 a[72:75], v28 offset:17408\n"
    "buffer_load_dwordx4 v35, s[12:15], s40 offen offset:1024 lds\n"
    "v_mfma_f32_16x16x32_bf16 v[4:7], a[84:87], a[52:55], v[4:7]\n"
    "v_exp_f32 v13, v13\n"
    "v_mfma_f32_16x16x32_bf16 v[0:3], a[88:91], a[24:27], v[0:3]\n"
    "ds_read_b128 a[76:79], v29 offset:17408\n"
    "v_cvt_pk_bf16_f32 v134, v12, v13\n"
    "v_mfma_f32_16x16x32_bf16 v[4:7], a[88:91], a[56:59], v[4:7]\n"
    "v_exp_f32 v14, v14\n"
    "v_mfma_f32_16x16x32_bf16 v[0:3], a[92:95], a[28:31], v[0:3]\n"
    "ds_read_b128 a[80:83], v28 offset:18432\n"
    "s_waitcnt lgkmcnt(5)\n"
    "v_mfma_f32_16x16x32_bf16 v[4:7], a[92:95], a[60:63], v[4:7]\n"
    "v_exp_f32 v15, v15\n"
    "v_mfma_f32_16x16x32_bf16 v[8:11], a[96:99], a[0:3], v[16:19]\n"
    "ds_read_b128 a[84:87], v29 offset:18432\n"
    "v_cvt_pk_bf16_f32 v135, v14, v15\n"
    "v_mfma_f32_16x16x32_bf16 v[12:15], a[96:99], a[32:35], v[20:23]\n"
    "v_exp_f32 v0, v0\n"
    "v_mfma_f32_16x16x32_bf16 v[8:11], a[100:103], a[4:7], v[8:11]\n"
    "buffer_load_dwordx4 v36, s[12:15], s40 offen offset:2048 lds\n"
    "v_mfma_f32_16x16x32_bf16 v[12:15], a[100:103], a[36:39], v[12:15]\n"
    "v_exp_f32 v1, v1\n"
    "v_mfma_f32_16x16x32_bf16 v[8:11], a[104:107], a[8:11], v[8:11]\n"
    "ds_read_b128 a[88:91], v28 offset:19456\n"
    "v_cvt_pk_bf16_f32 v72, v0, v1\n"
    "v_mfma_f32_16x16x32_bf16 v[12:15], a[104:107], a[40:43], v[12:15]\n"
    "v_exp_f32 v2, v2\n"
    "v_mfma_f32_16x16x32_bf16 a[200:203], a[208:211], v[128:131], a[200:203]\n"
    "buffer_load_dwordx4 v37, s[12:15], s40 offen offset:3072 lds\n"
    "v_mfma_f32_16x16x32_bf16 v[8:11], a[108:111], a[12:15], v[8:11]\n"
    "ds_read_b128 a[92:95], v29 offset:19456\n"
    "ds_read_b128 a[96:99], v28 offset:24576\n"
    "v_mfma_f32_16x16x32_bf16 v[12:15], a[108:111], a[44:47], v[12:15]\n"
    "v_exp_f32 v3, v3\n"
    "v_mfma_f32_16x16x32_bf16 v[8:11], a[112:115], a[16:19], v[8:11]\n"
    "ds_read_b128 a[100:103], v29 offset:24576\n"
    "v_cvt_pk_bf16_f32 v73, v2, v3\n"
    "v_mfma_f32_16x16x32_bf16 v[12:15], a[112:115], a[48:51], v[12:15]\n"
    "v_exp_f32 v4, v4\n"
    "v_mfma_f32_16x16x32_bf16 v[8:11], a[116:119], a[20:23], v[8:11]\n"
    "ds_read_b128 a[104:107], v28 offset:25600\n"
    "s_add_u32 m0, s28, 0xc000\n"
    "s_mov_b32 s40, 0x21400\n"
    "buffer_load_dwordx4 v34, s[12:15], s40 offen offset:0 lds\n"
    "v_mfma_f32_16x16x32_bf16 v[12:15], a[116:119], a[52:55], v[12:15]\n"
    "v_exp_f32 v5, v5\n"
    "v_mfma_f32_16x16x32_bf16 v[8:11], a[120:123], a[24:27], v[8:11]\n"
    "ds_read_b128 a[108:111], v29 offset:25600\n"
    "v_cvt_pk_bf16_f32 v76, v4, v5\n"
    "v_mfma_f32_16x16x32_bf16 v[12:15], a[120:123], a[56:59], v[12:15]\n"
    "v_exp_f32 v6, v6\n"
    "v_mfma_f32_16x16x32_bf16 v[8:11], a[124:127], a[28:31], v[8:11]\n"
    "ds_read_b128 a[112:115], v28 offset:26624\n"
    "s_waitcnt vmcnt(9) lgkmcnt(5)\n"
    "v_mfma_f32_16x16x32_bf16 v[12:15], a[124:127], a[60:63], v[12:15]\n"
    "v_exp_f32 v7, v7\n"
    "s_lshl1_add_u32 s39, s36, s38\n"
    "PL13_%=:\n"
    "s_add_u32 s39, s39, 3\n"
    "s_cmpk_lt_u32 s39, 809\n"
    "s_cbranch_scc1 PL13_%=\n"
    "s_memrealtime s[36:37]\n"
    "v_mfma_f32_16x16x32_bf16 v[0:3], a[64:67], a[0:3], v[16:19]\n"
    "ds_read_b128 a[116:119], v29 offset:26624\n"
    "v_cvt_pk_bf16_f32 v77, v6, v7\n"
    "v_mfma_f32_16x16x32_bf16 v[4:7], a[64:67], a[32:35], v[20:23]\n"
    "v_exp_f32 v8, v8\n"
    "v_mfma_f32_16x16x32_bf16 v[0:3], a[68:71], a[4:7], v[0:3]\n"
    "buffer_load_dwordx4 v35, s[12:15], s40 offen offset:1024 lds\n"
    "v_mfma_f32_16x16x32_bf16 v[4:7], a[68:71], a[36:39], v[4:7]\n"
    "v_exp_f32 v9, v9\n"
    "v_mfma_f32_16x16x32_bf16 v[0:3], a[72:75], a[8:11], v[0:3]\n"
    "ds_read_b128 a[120:123], v28 offset:27648\n"
    "v_cvt_pk_bf16_f32 v74, v8, v9\n"
    "v_mfma_f32_16x16x32_bf16 v[4:7], a[72:75], a[40:43], v[4:7]\n"
    "v_exp_f32 v10, v10\n"
    "v_mfma_f32_16x16x32_bf16 a[204:207], a[208:211], v[132:135], a[204:207]\n"
    "s_barrier\n"
    "v_mfma_f32_16x16x32_bf16 v[0:3], a[76:79], a[12:15], v[0:3]\n"
    "ds_read_b128 a[124:127], v29 offset:27648\n"
    "ds_read_b128 a[64:67], v24 offset:0\n"
    "v_mfma_f32_16x16x32_bf16 v[4:7], a[76:79], a[44:47], v[4:7]\n"
    "v_exp_f32 v11, v11\n"
    "v_mfma_f32_16x16x32_bf16 v[0:3], a[80:83], a[16:19], v[0:3]\n"
    "ds_read_b128 a[68:71], v25 offset:0\n"
    "v_cvt_pk_bf16_f32 v75, v10, v11\n"
    "v_mfma_f32_16x16x32_bf16 v[4:7], a[80:83], a[48:51], v[4:7]\n"
    "v_exp_f32 v12, v12\n"
    "v_mfma_f32_16x16x32_bf16 v[0:3], a[84:87], a[20:23], v[0:3]\n"
    "ds_read_b128 a[72:75], v24 offset:1024\n"
    "buffer_load_dwordx4 v36, s[12:15], s40 offen offset:2048 lds\n"
    "v_mfma_f32_16x16x32_bf16 v[4:7], a[84:87], a[52:55], v[4:7]\n"
    "v_exp_f32 v13, v13\n"
    "v_mfma_f32_16x16x32_bf16 v[0:3], a[88:91], a[24:27], v[0:3]\n"
    "ds_read_b128 a[76:79], v25 offset:1024\n"
    "v_cvt_pk_bf16_f32 v78, v12, v13\n"
    "v_mfma_f32_16x16x32_bf16 v[4:7], a[88:91], a[56:59], v[4:7]\n"
    "v_exp_f32 v14, v14\n"
    "v_mfma_f32_16x16x32_bf16 v[0:3], a[92:95], a[28:31], v[0:3]\n"
    "ds_read_b128 a[80:83], v24 offset:2048\n"
    "s_waitcnt lgkmcnt(5)\n"
    "v_mfma_f32_16x16x32_bf16 v[4:7], a[92:95], a[60:63], v[4:7]\n"
    "v_exp_f32 v15, v15\n"
    "v_mfma_f32_16x16x32_bf16 v[8:11], a[96:99], a[0:3], v[16:19]\n"
    "ds_read_b128 a[84:87], v25 offset:2048\n"
    "v_cvt_pk_bf16_f32 v79, v14, v15\n"
    "v_mfma_f32_16x16x32_bf16 v[12:15], a[96:99], a[32:35], v[20:23]\n"
    "v_exp_f32 v0, v0\n"
    "v_mfma_f32_16x16x32_bf16 v[8:11], a[100:103], a[4:7], v[8:11]\n"
    "ds_read_b128 a[88:91], v24 offset:3072\n"
    "v_mfma_f32_16x16x32_bf16 v[12:15], a[100:103], a[36:39], v[12:15]\n"
    "v_exp_f32 v1, v1\n"
    "v_mfma_f32_16x16x32_bf16 v[8:11], a[104:107], a[8:11], v[8:11]\n"
    "ds_read_b128 a[92:95], v25 offset:3072\n"
    "v_cvt_pk_bf16_f32 v88, v0, v1\n"
    "v_mfma_f32_16x16x32_bf16 v[12:15], a[104:107], a[40:43], v[12:15]\n"
    "v_exp_f32 v2, v2\n"
    "v_mfma_f32_16x16x32_bf16 a[200:203], a[208:211], v[72:75], a[200:203]\n"
    "buffer_load_dwordx4 v37, s[12:15], s40 offen offset:3072 lds\n"
    "v_mfma_f32_16x16x32_bf16 v[8:11], a[108:111], a[12:15], v[8:11]\n"
    "ds_read_b128 a[96:99], v24 offset:8192\n"
    "s_add_u32 m0, s28, 0x10000\n"
    "v_mfma_f32_16x16x32_bf16 v[12:15], a[108:111], a[44:47], v[12:15]\n"
    "v_exp_f32 v3, v3\n"
    "v_mfma_f32_16x16x32_bf16 v[8:11], a[112:115], a[16:19], v[8:11]\n"
    "ds_read_b128 a[100:103], v25 offset:8192\n"
    "v_cvt_pk_bf16_f32 v89, v2, v3\n"
    "v_mfma_f32_16x16x32_bf16 v[12:15], a[112:115], a[48:51], v[12:15]\n"
    "v_exp_f32 v4, v4\n"
    "v_mfma_f32_16x16x32_bf16 v[8:11], a[116:119], a[20:23], v[8:11]\n"
    "ds_read_b128 a[104:107], v24 offset:9216\n"
    "s_mov_b32 s40, 0x22400\n"
    "buffer_load_dwordx4 v34, s[12:15], s40 offen offset:0 lds\n"
    "v_mfma_f32_16x16x32_bf16 v[12:15], a[116:119], a[52:55], v[12:15]\n"
    "v_exp_f32 v5, v5\n"
    "v_mfma_f32_16x16x32_bf16 v[8:11], a[120:123], a[24:27], v[8:11]\n"
    "ds_read_b128 a[108:111], v25 offset:9216\n"
    "v_cvt_pk_bf16_f32 v92, v4, v5\n"
    "v_mfma_f32_16x16x32_bf16 v[12:15], a[120:123], a[56:59], v[12:15]\n"
    "v_exp_f32 v6, v6\n"
    "v_mfma_f32_16x16x32_bf16 v[8:11], a[124:127], a[28:31], v[8:11]\n"
    "ds_read_b128 a[112:115], v24 offset:10240\n"
    "s_waitcnt vmcnt(9) lgkmcnt(5)\n"
    "v_mfma_f32_16x16x32_bf16 v[12:15], a[124:127], a[60:63], v[12:15]\n"
    "v_exp_f32 v7, v7\n"
    "s_lshl1_add_u32 s39, s36, s38\n"
    "PL14_%=:\n"
    "s_add_u32 s39, s39, 3\n"
    "s_cmpk_lt_u32 s39, 892\n"
    "s_cbranch_scc1 PL14_%=\n"
    "s_memrealtime s[36:37]\n"
    "v_mfma_f32_16x16x32_bf16 v[0:3], a[64:67], a[0:3], v[16:19]\n"
    "ds_read_b128 a[116:119], v25 offset:10240\n"
    "v_cvt_pk_bf16_f32 v93, v6, v7\n"
    "v_mfma_f32_16x16x32_bf16 v[4:7], a[64:67], a[32:35], v[20:23]\n"
    "v_exp_f32 v8, v8\n"
    "v_mfma_f32_16x16x32_bf16 v[0:3], a[68:71], a[4:7], v[0:3]\n"
    "buffer_load_dwordx4 v35, s[12:15], s40 offen offset:1024 lds\n"
    "v_mfma_f32_16x16x32_bf16 v[4:7], a[68:71], a[36:39], v[4:7]\n"
    "v_exp_f32 v9, v9\n"
    "v_mfma_f32_16x16x32_bf16 v[0:3], a[72:75], a[8:11], v[0:3]\n"
    "ds_read_b128 a[120:123], v24 offset:11264\n"
    "v_cvt_pk_bf16_f32 v90, v8, v9\n"
    "v_mfma_f32_16x16x32_bf16 v[4:7], a[72:75], a[40:43], v[4:7]\n"
    "v_exp_f32 v10, v10\n"
    "v_mfma_f32_16x16x32_bf16 a[204:207], a[208:211], v[76:79], a[204:207]\n"
    "s_barrier\n"
    "v_mfma_f32_16x16x32_bf16 v[0:3], a[76:79], a[12:15], v[0:3]\n"
    "ds_read_b128 a[124:127], v25 offset:11264\n"
    "ds_read_b128 a[64:67], v24 offset:16384\n"
    "v_mfma_f32_16x16x32_bf16 v[4:7], a[76:79], a[44:47], v[4:7]\n"
    "v_exp_f32 v11, v11\n"
    "v_mfma_f32_16x16x32_bf16 v[0:3], a[80:83], a[16:19], v[0:3]\n"
    "ds_read_b128 a[68:71], v25 offset:16384\n"
    "v_cvt_pk_bf16_f32 v91, v10, v11\n"
    "v_mfma_f32_16x16x32_bf16 v[4:7], a[80:83], a[48:51], v[4:7]\n"
    "v_exp_f32 v12, v12\n"
    "v_mfma_f32_16x16x32_bf16 v[0:3], a[84:87], a[20:23], v[0:3]\n"
    "ds_read_b128 a[72:75], v24 offset:17408\n"
    "buffer_load_dwordx4 v36, s[12:15], s40 offen offset:2048 lds\n"
    "v_mfma_f32_16x16x32_bf16 v[4:7], a[84:87], a[52:55], v[4:7]\n"
    "v_exp_f32 v13, v13\n"
    "v_mfma_f32_16x16x32_bf16 v[0:3], a[88:91], a[24:27], v[0:3]\n"
    "ds_read_b128 a[76:79], v25 offset:17408\n"
    "v_cvt_pk_bf16_f32 v94, v12, v13\n"
    "v_mfma_f32_16x16x32_bf16 v[4:7], a[88:91], a[56:59], v[4:7]\n"
    "v_exp_f32 v14, v14\n"
    "v_mfma_f32_16x16x32_bf16 v[0:3], a[92:95], a[28:31], v[0:3]\n"
    "ds_read_b128 a[80:83], v24 offset:18432\n"
    "s_waitcnt lgkmcnt(5)\n"
    "v_mfma_f32_16x16x32_bf16 v[4:7], a[92:95], a[60:63], v[4:7]\n"
    "v_exp_f32 v15, v15\n"
    "v_mfma_f32_16x16x32_bf16 v[8:11], a[96:99], a[0:3], v[16:19]\n"
    "ds_read_b128 a[84:87], v25 offset:18432\n"
    "v_cvt_pk_bf16_f32 v95, v14, v15\n"
    "v_mfma_f32_16x16x32_bf16 v[12:15], a[96:99], a[32:35], v[20:23]\n"
    "v_exp_f32 v0, v0\n"
    "v_mfma_f32_16x16x32_bf16 v[8:11], a[100:103], a[4:7], v[8:11]\n"
    "buffer_load_dwordx4 v37, s[12:15], s40 offen offset:3072 lds\n"
    "v_mfma_f32_16x16x32_bf16 v[12:15], a[100:103], a[36:39], v[12:15]\n"
    "v_exp_f32 v1, v1\n"
    "v_mfma_f32_16x16x32_bf16 v[8:11], a[104:107], a[8:11], v[8:11]\n"
    "ds_read_b128 a[88:91], v24 offset:19456\n"
    "v_cvt_pk_bf16_f32 v136, v0, v1\n"
    "v_mfma_f32_16x16x32_bf16 v[12:15], a[104:107], a[40:43], v[12:15]\n"
    "v_exp_f32 v2, v2\n"
    "v_mfma_f32_16x16x32_bf16 a[200:203], a[208:211], v[88:91], a[200:203]\n"
    "s_add_u32 m0, s28, 0x14000\n"
    "s_mov_b32 s40, 0x23400\n"
    "buffer_load_dwordx4 v34, s[12:15], s40 offen offset:0 lds\n"
    "v_mfma_f32_16x16x32_bf16 v[8:11], a[108:111], a[12:15], v[8:11]\n"
    "ds_read_b128 a[92:95], v25 offset:19456\n"
    "ds_read_b128 a[96:99], v24 offset:24576\n"
    "v_mfma_f32_16x16x32_bf16 v[12:15], a[108:111], a[44:47], v[12:15]\n"
    "v_exp_f32 v3, v3\n"
    "v_mfma_f32_16x16x32_bf16 v[8:11], a[112:115], a[16:19], v[8:11]\n"
    "ds_read_b128 a[100:103], v25 offset:24576\n"
    "v_cvt_pk_bf16_f32 v137, v2, v3\n"
    "v_mfma_f32_16x16x32_bf16 v[12:15], a[112:115], a[48:51], v[12:15]\n"
    "v_exp_f32 v4, v4\n"
    "v_mfma_f32_16x16x32_bf16 v[8:11], a[116:119], a[20:23], v[8:11]\n"
    "ds_read_b128 a[104:107], v24 offset:25600\n"
    "buffer_load_dwordx4 v35, s[12:15], s40 offen offset:1024 lds\n"
    "v_mfma_f32_16x16x32_bf16 v[12:15], a[116:119], a[52:55], v[12:15]\n"
    "v_exp_f32 v5, v5\n"
    "v_mfma_f32_16x16x32_bf16 v[8:11], a[120:123], a[24:27], v[8:11]\n"
    "ds_read_b128 a[108:111], v25 offset:25600\n"
    "v_cvt_pk_bf16_f32 v140, v4, v5\n"
    "v_mfma_f32_16x16x32_bf16 v[12:15], a[120:123], a[56:59], v[12:15]\n"
    "v_exp_f32 v6, v6\n"
    "v_mfma_f32_16x16x32_bf16 v[8:11], a[124:127], a[28:31], v[8:11]\n"
    "ds_read_b128 a[112:115], v24 offset:26624\n"
    "s_waitcnt vmcnt(10) lgkmcnt(5)\n"
    "v_mfma_f32_16x16x32_bf16 v[12:15], a[124:127], a[60:63], v[12:15]\n"
    "v_exp_f32 v7, v7\n"
    "s_lshl1_add_u32 s39, s36, s38\n"
    "PL15_%=:\n"
    "s_add_u32 s39, s39, 3\n"
    "s_cmpk_lt_u32 s39, 975\n"
    "s_cbranch_scc1 PL15_%=\n"
    "s_memrealtime s[36:37]\n"
    "v_mfma_f32_16x16x32_bf16 v[0:3], a[64:67], a[0:3], v[16:19]\n"
    "ds_read_b128 a[116:119], v25 offset:26624\n"
    "v_cvt_pk_bf16_f32 v141, v6, v7\n"
    "v_mfma_f32_16x16x32_bf16 v[4:7], a[64:67], a[32:35], v[20:23]\n"
    "v_exp_f32 v8, v8\n"
    "v_mfma_f32_16x16x32_bf16 v[0:3], a[68:71], a[4:7], v[0:3]\n"
    "buffer_load_dwordx4 v36, s[12:15], s40 offen offset:2048 lds\n"
    "v_mfma_f32_16x16x32_bf16 v[4:7], a[68:71], a[36:39], v[4:7]\n"
    "v_exp_f32 v9, v9\n"
    "v_mfma_f32_16x16x32_bf16 v[0:3], a[72:75], a[8:11], v[0:3]\n"
    "ds_read_b128 a[120:123], v24 offset:27648\n"
    "v_cvt_pk_bf16_f32 v138, v8, v9\n"
    "v_mfma_f32_16x16x32_bf16 v[4:7], a[72:75], a[40:43], v[4:7]\n"
    "v_exp_f32 v10, v10\n"
    "v_mfma_f32_16x16x32_bf16 a[204:207], a[208:211], v[92:95], a[204:207]\n"
    "s_barrier\n"
    "v_mfma_f32_16x16x32_bf16 v[0:3], a[76:79], a[12:15], v[0:3]\n"
    "ds_read_b128 a[124:127], v25 offset:27648\n"
    "ds_read_b128 a[64:67], v24 offset:32768\n"
    "v_mfma_f32_16x16x32_bf16 v[4:7], a[76:79], a[44:47], v[4:7]\n"
    "v_exp_f32 v11, v11\n"
    "v_mfma_f32_16x16x32_bf16 v[0:3], a[80:83], a[16:19], v[0:3]\n"
    "ds_read_b128 a[68:71], v25 offset:32768\n"
    "v_cvt_pk_bf16_f32 v139, v10, v11\n"
    "v_mfma_f32_16x16x32_bf16 v[4:7], a[80:83], a[48:51], v[4:7]\n"
    "v_exp_f32 v12, v12\n"
    "v_mfma_f32_16x16x32_bf16 v[0:3], a[84:87], a[20:23], v[0:3]\n"
    "ds_read_b128 a[72:75], v24 offset:33792\n"
    "buffer_load_dwordx4 v37, s[12:15], s40 offen offset:3072 lds\n"
    "v_mfma_f32_16x16x32_bf16 v[4:7], a[84:87], a[52:55], v[4:7]\n"
    "v_exp_f32 v13, v13\n"
    "v_mfma_f32_16x16x32_bf16 v[0:3], a[88:91], a[24:27], v[0:3]\n"
    "ds_read_b128 a[76:79], v25 offset:33792\n"
    "v_cvt_pk_bf16_f32 v142, v12, v13\n"
    "v_mfma_f32_16x16x32_bf16 v[4:7], a[88:91], a[56:59], v[4:7]\n"
    "v_exp_f32 v14, v14\n"
    "v_mfma_f32_16x16x32_bf16 v[0:3], a[92:95], a[28:31], v[0:3]\n"
    "ds_read_b128 a[80:83], v24 offset:34816\n"
    "s_waitcnt lgkmcnt(5)\n"
    "v_mfma_f32_16x16x32_bf16 v[4:7], a[92:95], a[60:63], v[4:7]\n"
    "v_exp_f32 v15, v15\n"
    "v_mfma_f32_16x16x32_bf16 v[8:11], a[96:99], a[0:3], v[16:19]\n"
    "ds_read_b128 a[84:87], v25 offset:34816\n"
    "v_cvt_pk_bf16_f32 v143, v14, v15\n"
    "v_mfma_f32_16x16x32_bf16 v[12:15], a[96:99], a[32:35], v[20:23]\n"
    "v_exp_f32 v0, v0\n"
    "v_mfma_f32_16x16x32_bf16 v[8:11], a[100:103], a[4:7], v[8:11]\n"
    "ds_read_b128 a[88:91], v24 offset:35840\n"
    "s_add_u32 m0, s28, 0x18000\n"
    "v_mfma_f32_16x16x32_bf16 v[12:15], a[100:103], a[36:39], v[12:15]\n"
    "v_exp_f32 v1, v1\n"
    "v_mfma_f32_16x16x32_bf16 v[8:11], a[104:107], a[8:11], v[8:11]\n"
    "ds_read_b128 a[92:95], v25 offset:35840\n"
    "v_cvt_pk_bf16_f32 v152, v0, v1\n"
    "v_mfma_f32_16x16x32_bf16 v[12:15], a[104:107], a[40:43], v[12:15]\n"
    "v_exp_f32 v2, v2\n"
    "v_mfma_f32_16x16x32_bf16 a[200:203], a[208:211], v[136:139], a[200:203]\n"
    "s_mov_b32 s40, 0x40000\n"
    "buffer_load_dwordx4 v34, s[12:15], s40 offen offset:0 lds\n"
    "v_mfma_f32_16x16x32_bf16 v[8:11], a[108:111], a[12:15], v[8:11]\n"
    "ds_read_b128 a[96:99], v24 offset:40960\n"
    "v_mfma_f32_16x16x32_bf16 v[12:15], a[108:111], a[44:47], v[12:15]\n"
    "v_exp_f32 v3, v3\n"
    "v_mfma_f32_16x16x32_bf16 v[8:11], a[112:115], a[16:19], v[8:11]\n"
    "ds_read_b128 a[100:103], v25 offset:40960\n"
    "v_cvt_pk_bf16_f32 v153, v2, v3\n"
    "v_mfma_f32_16x16x32_bf16 v[12:15], a[112:115], a[48:51], v[12:15]\n"
    "v_exp_f32 v4, v4\n"
    "v_mfma_f32_16x16x32_bf16 v[8:11], a[116:119], a[20:23], v[8:11]\n"
    "ds_read_b128 a[104:107], v24 offset:41984\n"
    "buffer_load_dwordx4 v35, s[12:15], s40 offen offset:1024 lds\n"
    "v_mfma_f32_16x16x32_bf16 v[12:15], a[116:119], a[52:55], v[12:15]\n"
    "v_exp_f32 v5, v5\n"
    "v_mfma_f32_16x16x32_bf16 v[8:11], a[120:123], a[24:27], v[8:11]\n"
    "ds_read_b128 a[108:111], v25 offset:41984\n"
    "v_cvt_pk_bf16_f32 v156, v4, v5\n"
    "v_mfma_f32_16x16x32_bf16 v[12:15], a[120:123], a[56:59], v[12:15]\n"
    "v_exp_f32 v6, v6\n"
    "v_mfma_f32_16x16x32_bf16 v[8:11], a[124:127], a[28:31], v[8:11]\n"
    "ds_read_b128 a[112:115], v24 offset:43008\n"
    "s_waitcnt vmcnt(10) lgkmcnt(5)\n"
    "v_mfma_f32_16x16x32_bf16 v[12:15], a[124:127], a[60:63], v[12:15]\n"
    "v_exp_f32 v7, v7\n"
    "s_lshl1_add_u32 s39, s36, s38\n"
    "PL16_%=:\n"
    "s_add_u32 s39, s39, 3\n"
    "s_cmpk_lt_u32 s39, 1058\n"
    "s_cbranch_scc1 PL16_%=\n"
    "s_memrealtime s[36:37]\n"
    "v_mfma_f32_16x16x32_bf16 v[0:3], a[64:67], a[0:3], v[16:19]\n"
    "ds_read_b128 a[116:119], v25 offset:43008\n"
    "v_cvt_pk_bf16_f32 v157, v6, v7\n"
    "v_mfma_f32_16x16x32_bf16 v[4:7], a[64:67], a[32:35], v[20:23]\n"
    "v_exp_f32 v8, v8\n"
    "v_mfma_f32_16x16x32_bf16 v[0:3], a[68:71], a[4:7], v[0:3]\n"
    "buffer_load_dwordx4 v36, s[12:15], s40 offen offset:2048 lds\n"
    "v_mfma_f32_16x16x32_bf16 v[4:7], a[68:71], a[36:39], v[4:7]\n"
    "v_exp_f32 v9, v9\n"
    "v_mfma_f32_16x16x32_bf16 v[0:3], a[72:75], a[8:11], v[0:3]\n"
    "ds_read_b128 a[120:123], v24 offset:44032\n"
    "v_cvt_pk_bf16_f32 v154, v8, v9\n"
    "v_mfma_f32_16x16x32_bf16 v[4:7], a[72:75], a[40:43], v[4:7]\n"
    "v_exp_f32 v10, v10\n"
    "v_mfma_f32_16x16x32_bf16 a[204:207], a[208:211], v[140:143], a[204:207]\n"
    "s_barrier\n"
    "v_mfma_f32_16x16x32_bf16 v[0:3], a[76:79], a[12:15], v[0:3]\n"
    "ds_read_b128 a[124:127], v25 offset:44032\n"
    "ds_read_b128 a[64:67], v24 offset:49152\n"
    "v_mfma_f32_16x16x32_bf16 v[4:7], a[76:79], a[44:47], v[4:7]\n"
    "v_exp_f32 v11, v11\n"
    "v_mfma_f32_16x16x32_bf16 v[0:3], a[80:83], a[16:19], v[0:3]\n"
    "ds_read_b128 a[68:71], v25 offset:49152\n"
    "v_cvt_pk_bf16_f32 v155, v10, v11\n"
    "v_mfma_f32_16x16x32_bf16 v[4:7], a[80:83], a[48:51], v[4:7]\n"
    "v_exp_f32 v12, v12\n"
    "v_mfma_f32_16x16x32_bf16 v[0:3], a[84:87], a[20:23], v[0:3]\n"
    "ds_read_b128 a[72:75], v24 offset:50176\n"
    "buffer_load_dwordx4 v37, s[12:15], s40 offen offset:3072 lds\n"
    "v_mfma_f32_16x16x32_bf16 v[4:7], a[84:87], a[52:55], v[4:7]\n"
    "v_exp_f32 v13, v13\n"
    "v_mfma_f32_16x16x32_bf16 v[0:3], a[88:91], a[24:27], v[0:3]\n"
    "ds_read_b128 a[76:79], v25 offset:50176\n"
    "v_cvt_pk_bf16_f32 v158, v12, v13\n"
    "v_mfma_f32_16x16x32_bf16 v[4:7], a[88:91], a[56:59], v[4:7]\n"
    "v_exp_f32 v14, v14\n"
    "v_mfma_f32_16x16x32_bf16 v[0:3], a[92:95], a[28:31], v[0:3]\n"
    "ds_read_b128 a[80:83], v24 offset:51200\n"
    "s_waitcnt lgkmcnt(5)\n"
    "v_mfma_f32_16x16x32_bf16 v[4:7], a[92:95], a[60:63], v[4:7]\n"
    "v_exp_f32 v15, v15\n"
    "v_mfma_f32_16x16x32_bf16 v[8:11], a[96:99], a[0:3], v[16:19]\n"
    "ds_read_b128 a[84:87], v25 offset:51200\n"
    "v_cvt_pk_bf16_f32 v159, v14, v15\n"
    "v_mfma_f32_16x16x32_bf16 v[12:15], a[96:99], a[32:35], v[20:23]\n"
    "v_exp_f32 v0, v0\n"
    "v_mfma_f32_16x16x32_bf16 v[8:11], a[100:103], a[4:7], v[8:11]\n"
    "ds_read_b128 a[88:91], v24 offset:52224\n"
    "s_add_u32 m0, s28, 0x1c000\n"
    "s_mov_b32 s40, 0x41000\n"
    "buffer_load_dwordx4 v34, s[12:15], s40 offen offset:0 lds\n"
    "v_mfma_f32_16x16x32_bf16 v[12:15], a[100:103], a[36:39], v[12:15]\n"
    "v_exp_f32 v1, v1\n"
    "v_mfma_f32_16x16x32_bf16 v[8:11], a[104:107], a[8:11], v[8:11]\n"
    "ds_read_b128 a[92:95], v25 offset:52224\n"
    "v_cvt_pk_bf16_f32 v80, v0, v1\n"
    "v_mfma_f32_16x16x32_bf16 v[12:15], a[104:107], a[40:43], v[12:15]\n"
    "v_exp_f32 v2, v2\n"
    "v_mfma_f32_16x16x32_bf16 a[200:203], a[208:211], v[152:155], a[200:203]\n"
    "buffer_load_dwordx4 v35, s[12:15], s40 offen offset:1024 lds\n"
    "v_mfma_f32_16x16x32_bf16 v[8:11], a[108:111], a[12:15], v[8:11]\n"
    "ds_read_b128 a[96:99], v24 offset:57344\n"
    "v_mfma_f32_16x16x32_bf16 v[12:15], a[108:111], a[44:47], v[12:15]\n"
    "v_exp_f32 v3, v3\n"
    "v_mfma_f32_16x16x32_bf16 v[8:11], a[112:115], a[16:19], v[8:11]\n"
    "ds_read_b128 a[100:103], v25 offset:57344\n"
    "v_cvt_pk_bf16_f32 v81, v2, v3\n"
    "v_mfma_f32_16x16x32_bf16 v[12:15], a[112:115], a[48:51], v[12:15]\n"
    "v_exp_f32 v4, v4\n"
    "v_mfma_f32_16x16x32_bf16 v[8:11], a[116:119], a[20:23], v[8:11]\n"
    "ds_read_b128 a[104:107], v24 offset:58368\n"
    "buffer_load_dwordx4 v36, s[12:15], s40 offen offset:2048 lds\n"
    "v_mfma_f32_16x16x32_bf16 v[12:15], a[116:119], a[52:55], v[12:15]\n"
    "v_exp_f32 v5, v5\n"
    "v_mfma_f32_16x16x32_bf16 v[8:11], a[120:123], a[24:27], v[8:11]\n"
    "ds_read_b128 a[108:111], v25 offset:58368\n"
    "v_cvt_pk_bf16_f32 v84, v4, v5\n"
    "v_mfma_f32_16x16x32_bf16 v[12:15], a[120:123], a[56:59], v[12:15]\n"
    "v_exp_f32 v6, v6\n"
    "v_mfma_f32_16x16x32_bf16 v[8:11], a[124:127], a[28:31], v[8:11]\n"
    "ds_read_b128 a[112:115], v24 offset:59392\n"
    "s_waitcnt vmcnt(11) lgkmcnt(5)\n"
    "v_mfma_f32_16x16x32_bf16 v[12:15], a[124:127], a[60:63], v[12:15]\n"
    "v_exp_f32 v7, v7\n"
    "s_lshl1_add_u32 s39, s36, s38\n"
    "PL17_%=:\n"
    "s_add_u32 s39, s39, 3\n"
    "s_cmpk_lt_u32 s39, 1141\n"
    "s_cbranch_scc1 PL17_%=\n"
    "s_memrealtime s[36:37]\n"
    "v_mfma_f32_16x16x32_bf16 v[0:3], a[64:67], a[0:3], v[16:19]\n"
    "ds_read_b128 a[116:119], v25 offset:59392\n"
    "v_cvt_pk_bf16_f32 v85, v6, v7\n"
    "v_mfma_f32_16x16x32_bf16 v[4:7], a[64:67], a[32:35], v[20:23]\n"
    "v_exp_f32 v8, v8\n"
    "v_mfma_f32_16x16x32_bf16 v[0:3], a[68:71], a[4:7], v[0:3]\n"
    "buffer_load_dwordx4 v37, s[12:15], s40 offen offset:3072 lds\n"
    "v_mfma_f32_16x16x32_bf16 v[4:7], a[68:71], a[36:39], v[4:7]\n"
    "v_exp_f32 v9, v9\n"
    "v_mfma_f32_16x16x32_bf16 v[0:3], a[72:75], a[8:11], v[0:3]\n"
    "ds_read_b128 a[120:123], v24 offset:60416\n"
    "v_cvt_pk_bf16_f32 v82, v8, v9\n"
    "v_mfma_f32_16x16x32_bf16 v[4:7], a[72:75], a[40:43], v[4:7]\n"
    "v_exp_f32 v10, v10\n"
    "v_mfma_f32_16x16x32_bf16 a[204:207], a[208:211], v[156:159], a[204:207]\n"
    "s_barrier\n"
    "v_mfma_f32_16x16x32_bf16 v[0:3], a[76:79], a[12:15], v[0:3]\n"
    "ds_read_b128 a[124:127], v25 offset:60416\n"
    "ds_read_b128 a[64:67], v26 offset:0\n"
    "v_mfma_f32_16x16x32_bf16 v[4:7], a[76:79], a[44:47], v[4:7]\n"
    "v_exp_f32 v11, v11\n"
    "v_mfma_f32_16x16x32_bf16 v[0:3], a[80:83], a[16:19], v[0:3]\n"
    "ds_read_b128 a[68:71], v27 offset:0\n"
    "v_cvt_pk_bf16_f32 v83, v10, v11\n"
    "v_mfma_f32_16x16x32_bf16 v[4:7], a[80:83], a[48:51], v[4:7]\n"
    "v_exp_f32 v12, v12\n"
    "v_mfma_f32_16x16x32_bf16 v[0:3], a[84:87], a[20:23], v[0:3]\n"
    "ds_read_b128 a[72:75], v26 offset:1024\n"
    "s_add_u32 m0, s28, 0x20000\n"
    "s_mov_b32 s40, 0x42000\n"
    "buffer_load_dwordx4 v34, s[12:15], s40 offen offset:0 lds\n"
    "v_mfma_f32_16x16x32_bf16 v[4:7], a[84:87], a[52:55], v[4:7]\n"
    "v_exp_f32 v13, v13\n"
    "STUB2_%=:\n"
    "s_cbranch_execz STUB6_%=\n"
    "v_mfma_f32_16x16x32_bf16 v[0:3], a[88:91], a[24:27], v[0:3]\n"
    "ds_read_b128 a[76:79], v27 offset:1024\n"
    "v_cvt_pk_bf16_f32 v86, v12, v13\n"
    "v_mfma_f32_16x16x32_bf16 v[4:7], a[88:91], a[56:59], v[4:7]\n"
    "v_exp_f32 v14, v14\n"
    "v_mfma_f32_16x16x32_bf16 v[0:3], a[92:95], a[28:31], v[0:3]\n"
    "ds_read_b128 a[80:83], v26 offset:2048\n"
    "s_waitcnt lgkmcnt(5)\n"
    "v_mfma_f32_16x16x32_bf16 v[4:7], a[92:95], a[60:63], v[4:7]\n"
    "v_exp_f32 v15, v15\n"
    "v_mfma_f32_16x16x32_bf16 v[8:11], a[96:99], a[0:3], v[16:19]\n"
    "ds_read_b128 a[84:87], v27 offset:2048\n"
    "v_cvt_pk_bf16_f32 v87, v14, v15\n"
    "v_mfma_f32_16x16x32_bf16 v[12:15], a[96:99], a[32:35], v[20:23]\n"
    "v_exp_f32 v0, v0\n"
    "v_mfma_f32_16x16x32_bf16 v[8:11], a[100:103], a[4:7], v[8:11]\n"
    "ds_read_b128 a[88:91], v26 offset:3072\n"
    "v_mfma_f32_16x16x32_bf16 v[12:15], a[100:103], a[36:39], v[12:15]\n"
    "v_exp_f32 v1, v1\n"
    "v_mfma_f32_16x16x32_bf16 v[8:11], a[104:107], a[8:11], v[8:11]\n"
    "ds_read_b128 a[92:95], v27 offset:3072\n"
    "v_cvt_pk_bf16_f32 v96, v0, v1\n"
    "v_mfma_f32_16x16x32_bf16 v[12:15], a[104:107], a[40:43], v[12:15]\n"
    "v_exp_f32 v2, v2\n"
    "v_mfma_f32_16x16x32_bf16 a[200:203], a[208:211], v[80:83], a[200:203]\n"
    "buffer_load_dwordx4 v35, s[12:15], s40 offen offset:1024 lds\n"
    "v_mfma_f32_16x16x32_bf16 v[8:11], a[108:111], a[12:15], v[8:11]\n"
    "ds_read_b128 a[96:99], v26 offset:8192\n"
    "v_mfma_f32_16x16x32_bf16 v[12:15], a[108:111], a[44:47], v[12:15]\n"
    "v_exp_f32 v3, v3\n"
    "v_mfma_f32_16x16x32_bf16 v[8:11], a[112:115], a[16:19], v[8:11]\n"
    "ds_read_b128 a[100:103], v27 offset:8192\n"
    "v_cvt_pk_bf16_f32 v97, v2, v3\n"
    "v_mfma_f32_16x16x32_bf16 v[12:15], a[112:115], a[48:51], v[12:15]\n"
    "v_exp_f32 v4, v4\n"
    "v_mfma_f32_16x16x32_bf16 v[8:11], a[116:119], a[20:23], v[8:11]\n"
    "ds_read_b128 a[104:107], v26 offset:9216\n"
    "buffer_load_dwordx4 v36, s[12:15], s40 offen offset:2048 lds\n"
    "v_mfma_f32_16x16x32_bf16 v[12:15], a[116:119], a[52:55], v[12:15]\n"
    "v_exp_f32 v5, v5\n"
    "v_mfma_f32_16x16x32_bf16 v[8:11], a[120:123], a[24:27], v[8:11]\n"
    "ds_read_b128 a[108:111], v27 offset:9216\n"
    "v_cvt_pk_bf16_f32 v100, v4, v5\n"
    "v_mfma_f32_16x16x32_bf16 v[12:15], a[120:123], a[56:59], v[12:15]\n"
    "v_exp_f32 v6, v6\n"
    "v_mfma_f32_16x16x32_bf16 v[8:11], a[124:127], a[28:31], v[8:11]\n"
    "ds_read_b128 a[112:115], v26 offset:10240\n"
    "s_waitcnt vmcnt(11) lgkmcnt(5)\n"
    "v_mfma_f32_16x16x32_bf16 v[12:15], a[124:127], a[60:63], v[12:15]\n"
    "v_exp_f32 v7, v7\n"
    "s_lshl1_add_u32 s39, s36, s38\n"
    "PL18_%=:\n"
    "s_add_u32 s39, s39, 3\n"
    "s_cmpk_lt_u32 s39, 1224\n"
    "s_cbranch_scc1 PL18_%=\n"
    "JOIN_%=:\n"
    "FST3_%=:\n"
    "s_cbranch_execz WRET2_%=\n"
    "v_mfma_f32_16x16x32_bf16 v[0:3], a[64:67], a[0:3], v[16:19]\n"
    "ds_read_b128 a[116:119], v27 offset:10240\n"
    "v_cvt_pk_bf16_f32 v101, v6, v7\n"
    "v_mfma_f32_16x16x32_bf16 v[4:7], a[64:67], a[32:35], v[20:23]\n"
    "v_exp_f32 v8, v8\n"
    "v_mfma_f32_16x16x32_bf16 v[0:3], a[68:71], a[4:7], v[0:3]\n"
    "buffer_load_dwordx4 v37, s[12:15], s40 offen offset:3072 lds\n"
    "v_mfma_f32_16x16x32_bf16 v[4:7], a[68:71], a[36:39], v[4:7]\n"
    "v_exp_f32 v9, v9\n"
    "v_mfma_f32_16x16x32_bf16 v[0:3], a[72:75], a[8:11], v[0:3]\n"
    "ds_read_b128 a[120:123], v26 offset:11264\n"
    "v_cvt_pk_bf16_f32 v98, v8, v9\n"
    "v_mfma_f32_16x16x32_bf16 v[4:7], a[72:75], a[40:43], v[4:7]\n"
    "v_exp_f32 v10, v10\n"
    "v_mfma_f32_16x16x32_bf16 a[204:207], a[208:211], v[84:87], a[204:207]\n"
    "s_barrier\n"
    "v_mfma_f32_16x16x32_bf16 v[0:3], a[76:79], a[12:15], v[0:3]\n"
    "ds_read_b128 a[124:127], v27 offset:11264\n"
    "ds_read_b128 a[64:67], v26 offset:16384\n"
    "v_mfma_f32_16x16x32_bf16 v[4:7], a[76:79], a[44:47], v[4:7]\n"
    "v_exp_f32 v11, v11\n"
    "v_mfma_f32_16x16x32_bf16 v[0:3], a[80:83], a[16:19], v[0:3]\n"
    "ds_read_b128 a[68:71], v27 offset:16384\n"
    "v_cvt_pk_bf16_f32 v99, v10, v11\n"
    "v_mfma_f32_16x16x32_bf16 v[4:7], a[80:83], a[48:51], v[4:7]\n"
    "v_exp_f32 v12, v12\n"
    "v_mfma_f32_16x16x32_bf16 v[0:3], a[84:87], a[20:23], v[0:3]\n"
    "ds_read_b128 a[72:75], v26 offset:17408\n"
    "s_add_u32 m0, s28, 0x24000\n"
    "s_mov_b32 s40, 0x43000\n"
    "buffer_load_dwordx4 v34, s[12:15], s40 offen offset:0 lds\n"
    "v_mfma_f32_16x16x32_bf16 v[4:7], a[84:87], a[52:55], v[4:7]\n"
    "v_exp_f32 v13, v13\n"
    "v_mfma_f32_16x16x32_bf16 v[0:3], a[88:91], a[24:27], v[0:3]\n"
    "ds_read_b128 a[76:79], v27 offset:17408\n"
    "v_cvt_pk_bf16_f32 v102, v12, v13\n"
    "v_mfma_f32_16x16x32_bf16 v[4:7], a[88:91], a[56:59], v[4:7]\n"
    "v_exp_f32 v14, v14\n"
    "v_mfma_f32_16x16x32_bf16 v[0:3], a[92:95], a[28:31], v[0:3]\n"
    "ds_read_b128 a[80:83], v26 offset:18432\n"
    "s_waitcnt lgkmcnt(5)\n"
    "v_mfma_f32_16x16x32_bf16 v[4:7], a[92:95], a[60:63], v[4:7]\n"
    "v_exp_f32 v15, v15\n"
    "v_mfma_f32_16x16x32_bf16 v[8:11], a[96:99], a[0:3], v[16:19]\n"
    "ds_read_b128 a[84:87], v27 offset:18432\n"
    "v_cvt_pk_bf16_f32 v103, v14, v15\n"
    "v_mfma_f32_16x16x32_bf16 v[12:15], a[96:99], a[32:35], v[20:23]\n"
    "v_exp_f32 v0, v0\n"
    "v_mfma_f32_16x16x32_bf16 v[8:11], a[100:103], a[4:7], v[8:11]\n"
    "buffer_load_dwordx4 v35, s[12:15], s40 offen offset:1024 lds\n"
    "v_mfma_f32_16x16x32_bf16 v[12:15], a[100:103], a[36:39], v[12:15]\n"
    "v_exp_f32 v1, v1\n"
    "v_mfma_f32_16x16x32_bf16 v[8:11], a[104:107], a[8:11], v[8:11]\n"
    "ds_read_b128 a[88:91], v26 offset:19456\n"
    "v_cvt_pk_bf16_f32 v144, v0, v1\n"
    "v_mfma_f32_16x16x32_bf16 v[12:15], a[104:107], a[40:43], v[12:15]\n"
    "v_exp_f32 v2, v2\n"
    "v_mfma_f32_16x16x32_bf16 a[200:203], a[208:211], v[96:99], a[200:203]\n"
    "buffer_load_dwordx4 v36, s[12:15], s40 offen offset:2048 lds\n"
    "v_mfma_f32_16x16x32_bf16 v[8:11], a[108:111], a[12:15], v[8:11]\n"
    "ds_read_b128 a[92:95], v27 offset:19456\n"
    "ds_read_b128 a[96:99], v26 offset:24576\n"
    "v_mfma_f32_16x16x32_bf16 v[12:15], a[108:111], a[44:47], v[12:15]\n"
    "v_exp_f32 v3, v3\n"
    "v_mfma_f32_16x16x32_bf16 v[8:11], a[112:115], a[16:19], v[8:11]\n"
    "ds_read_b128 a[100:103], v27 offset:24576\n"
    "v_cvt_pk_bf16_f32 v145, v2, v3\n"
    "v_mfma_f32_16x16x32_bf16 v[12:15], a[112:115], a[48:51], v[12:15]\n"
    "v_exp_f32 v4, v4\n"
    "v_mfma_f32_16x16x32_bf16 v[8:11], a[116:119], a[20:23], v[8:11]\n"
    "ds_read_b128 a[104:107], v26 offset:25600\n"
    "buffer_load_dwordx4 v37, s[12:15], s40 offen offset:3072 lds\n"
    "v_mfma_f32_16x16x32_bf16 v[12:15], a[116:119], a[52:55], v[12:15]\n"
    "v_exp_f32 v5, v5\n"
    "v_mfma_f32_16x16x32_bf16 v[8:11], a[120:123], a[24:27], v[8:11]\n"
    "ds_read_b128 a[108:111], v27 offset:25600\n"
    "v_cvt_pk_bf16_f32 v148, v4, v5\n"
    "v_mfma_f32_16x16x32_bf16 v[12:15], a[120:123], a[56:59], v[12:15]\n"
    "v_exp_f32 v6, v6\n"
    "v_mfma_f32_16x16x32_bf16 v[8:11], a[124:127], a[28:31], v[8:11]\n"
    "ds_read_b128 a[112:115], v26 offset:26624\n"
    "s_waitcnt vmcnt(12) lgkmcnt(5)\n"
    "v_mfma_f32_16x16x32_bf16 v[12:15], a[124:127], a[60:63], v[12:15]\n"
    "v_exp_f32 v7, v7\n"
    "v_mfma_f32_16x16x32_bf16 v[0:3], a[64:67], a[0:3], v[16:19]\n"
    "ds_read_b128 a[116:119], v27 offset:26624\n"
    "v_cvt_pk_bf16_f32 v149, v6, v7\n"
    "v_mfma_f32_16x16x32_bf16 v[4:7], a[64:67], a[32:35], v[20:23]\n"
    "v_exp_f32 v8, v8\n"
    "v_mfma_f32_16x16x32_bf16 v[0:3], a[68:71], a[4:7], v[0:3]\n"
    "ds_read_b128 a[120:123], v26 offset:27648\n"
    "s_add_u32 m0, s28, 0x0\n"
    "s_mov_b32 s40, 0x40400\n"
    "buffer_load_dwordx4 v34, s[12:15], s40 offen offset:0 nt lds\n"
    "v_mfma_f32_16x16x32_bf16 v[4:7], a[68:71], a[36:39], v[4:7]\n"
    "v_exp_f32 v9, v9\n"
    "v_mfma_f32_16x16x32_bf16 v[0:3], a[72:75], a[8:11], v[0:3]\n"
    "ds_read_b128 a[124:127], v27 offset:27648\n"
    "v_cvt_pk_bf16_f32 v146, v8, v9\n"
    "v_mfma_f32_16x16x32_bf16 v[4:7], a[72:75], a[40:43], v[4:7]\n"
    "v_exp_f32 v10, v10\n"
    "v_mfma_f32_16x16x32_bf16 a[204:207], a[208:211], v[100:103], a[204:207]\n"
    "s_barrier\n"
    "v_mfma_f32_16x16x32_bf16 v[0:3], a[76:79], a[12:15], v[0:3]\n"
    "ds_read_b128 a[64:67], v26 offset:32768\n"
    "v_mfma_f32_16x16x32_bf16 v[4:7], a[76:79], a[44:47], v[4:7]\n"
    "v_exp_f32 v11, v11\n"
    "v_mfma_f32_16x16x32_bf16 v[0:3], a[80:83], a[16:19], v[0:3]\n"
    "ds_read_b128 a[68:71], v27 offset:32768\n"
    "v_cvt_pk_bf16_f32 v147, v10, v11\n"
    "v_mfma_f32_16x16x32_bf16 v[4:7], a[80:83], a[48:51], v[4:7]\n"
    "v_exp_f32 v12, v12\n"
    "v_mfma_f32_16x16x32_bf16 v[0:3], a[84:87], a[20:23], v[0:3]\n"
    "ds_read_b128 a[72:75], v26 offset:33792\n"
    "buffer_load_dwordx4 v35, s[12:15], s40 offen offset:1024 nt lds\n"
    "v_mfma_f32_16x16x32_bf16 v[4:7], a[84:87], a[52:55], v[4:7]\n"
    "v_exp_f32 v13, v13\n"
    "v_mfma_f32_16x16x32_bf16 v[0:3], a[88:91], a[24:27], v[0:3]\n"
    "ds_read_b128 a[76:79], v27 offset:33792\n"
    "v_cvt_pk_bf16_f32 v150, v12, v13\n"
    "v_mfma_f32_16x16x32_bf16 v[4:7], a[88:91], a[56:59], v[4:7]\n"
    "v_exp_f32 v14, v14\n"
    "v_mfma_f32_16x16x32_bf16 v[0:3], a[92:95], a[28:31], v[0:3]\n"
    "ds_read_b128 a[80:83], v26 offset:34816\n"
    "s_waitcnt lgkmcnt(5)\n"
    "v_mfma_f32_16x16x32_bf16 v[4:7], a[92:95], a[60:63], v[4:7]\n"
    "v_exp_f32 v15, v15\n"
    "v_mfma_f32_16x16x32_bf16 v[8:11], a[96:99], a[0:3], v[16:19]\n"
    "ds_read_b128 a[84:87], v27 offset:34816\n"
    "v_cvt_pk_bf16_f32 v151, v14, v15\n"
    "v_mfma_f32_16x16x32_bf16 v[12:15], a[96:99], a[32:35], v[20:23]\n"
    "v_exp_f32 v0, v0\n"
    "v_mfma_f32_16x16x32_bf16 v[8:11], a[100:103], a[4:7], v[8:11]\n"
    "ds_read_b128 a[88:91], v26 offset:35840\n"
    "v_mfma_f32_16x16x32_bf16 v[12:15], a[100:103], a[36:39], v[12:15]\n"
    "v_exp_f32 v1, v1\n"
    "v_mfma_f32_16x16x32_bf16 v[8:11], a[104:107], a[8:11], v[8:11]\n"
    "ds_read_b128 a[92:95], v27 offset:35840\n"
    "v_cvt_pk_bf16_f32 v160, v0, v1\n"
    "v_mfma_f32_16x16x32_bf16 v[12:15], a[104:107], a[40:43], v[12:15]\n"
    "v_exp_f32 v2, v2\n"
    "v_mfma_f32_16x16x32_bf16 a[200:203], a[208:211], v[144:147], a[200:203]\n"
    "buffer_load_dwordx4 v36, s[12:15], s40 offen offset:2048 nt lds\n"
    "v_mfma_f32_16x16x32_bf16 v[8:11], a[108:111], a[12:15], v[8:11]\n"
    "ds_read_b128 a[96:99], v26 offset:40960\n"
    "v_mfma_f32_16x16x32_bf16 v[12:15], a[108:111], a[44:47], v[12:15]\n"
    "v_exp_f32 v3, v3\n"
    "v_mfma_f32_16x16x32_bf16 v[8:11], a[112:115], a[16:19], v[8:11]\n"
    "ds_read_b128 a[100:103], v27 offset:40960\n"
    "v_cvt_pk_bf16_f32 v161, v2, v3\n"
    "v_mfma_f32_16x16x32_bf16 v[12:15], a[112:115], a[48:51], v[12:15]\n"
    "v_exp_f32 v4, v4\n"
    "v_mfma_f32_16x16x32_bf16 v[8:11], a[116:119], a[20:23], v[8:11]\n"
    "ds_read_b128 a[104:107], v26 offset:41984\n"
    "buffer_load_dwordx4 v37, s[12:15], s40 offen offset:3072 nt lds\n"
    "v_mfma_f32_16x16x32_bf16 v[12:15], a[116:119], a[52:55], v[12:15]\n"
    "v_exp_f32 v5, v5\n"
    "v_mfma_f32_16x16x32_bf16 v[8:11], a[120:123], a[24:27], v[8:11]\n"
    "ds_read_b128 a[108:111], v27 offset:41984\n"
    "v_cvt_pk_bf16_f32 v164, v4, v5\n"
    "v_mfma_f32_16x16x32_bf16 v[12:15], a[120:123], a[56:59], v[12:15]\n"
    "v_exp_f32 v6, v6\n"
    "v_mfma_f32_16x16x32_bf16 v[8:11], a[124:127], a[28:31], v[8:11]\n"
    "ds_read_b128 a[112:115], v26 offset:43008\n"
    "s_waitcnt vmcnt(12) lgkmcnt(5)\n"
    "v_mfma_f32_16x16x32_bf16 v[12:15], a[124:127], a[60:63], v[12:15]\n"
    "v_exp_f32 v7, v7\n"
    "v_mfma_f32_16x16x32_bf16 v[0:3], a[64:67], a[0:3], v[16:19]\n"
    "ds_read_b128 a[116:119], v27 offset:43008\n"
    "v_cvt_pk_bf16_f32 v165, v6, v7\n"
    "v_mfma_f32_16x16x32_bf16 v[4:7], a[64:67], a[32:35], v[20:23]\n"
    "v_exp_f32 v8, v8\n"
    "v_mfma_f32_16x16x32_bf16 v[0:3], a[68:71], a[4:7], v[0:3]\n"
    "ds_read_b128 a[120:123], v26 offset:44032\n"
    "s_add_u32 m0, s28, 0x4000\n"
    "s_mov_b32 s40, 0x41400\n"
    "buffer_load_dwordx4 v34, s[12:15], s40 offen offset:0 nt lds\n"
    "v_mfma_f32_16x16x32_bf16 v[4:7], a[68:71], a[36:39], v[4:7]\n"
    "v_exp_f32 v9, v9\n"
    "v_mfma_f32_16x16x32_bf16 v[0:3], a[72:75], a[8:11], v[0:3]\n"
    "ds_read_b128 a[124:127], v27 offset:44032\n"
    "v_cvt_pk_bf16_f32 v162, v8, v9\n"
    "v_mfma_f32_16x16x32_bf16 v[4:7], a[72:75], a[40:43], v[4:7]\n"
    "v_exp_f32 v10, v10\n"
    "v_mfma_f32_16x16x32_bf16 a[204:207], a[208:211], v[148:151], a[204:207]\n"
    "s_barrier\n"
    "v_mfma_f32_16x16x32_bf16 v[0:3], a[76:79], a[12:15], v[0:3]\n"
    "ds_read_b128 a[64:67], v26 offset:49152\n"
    "v_mfma_f32_16x16x32_bf16 v[4:7], a[76:79], a[44:47], v[4:7]\n"
    "v_exp_f32 v11, v11\n"
    "v_mfma_f32_16x16x32_bf16 v[0:3], a[80:83], a[16:19], v[0:3]\n"
    "ds_read_b128 a[68:71], v27 offset:49152\n"
    "v_cvt_pk_bf16_f32 v163, v10, v11\n"
    "v_mfma_f32_16x16x32_bf16 v[4:7], a[80:83], a[48:51], v[4:7]\n"
    "v_exp_f32 v12, v12\n"
    "v_mfma_f32_16x16x32_bf16 v[0:3], a[84:87], a[20:23], v[0:3]\n"
    "ds_read_b128 a[72:75], v26 offset:50176\n"
    "buffer_load_dwordx4 v35, s[12:15], s40 offen offset:1024 nt lds\n"
    "v_mfma_f32_16x16x32_bf16 v[4:7], a[84:87], a[52:55], v[4:7]\n"
    "v_exp_f32 v13, v13\n"
    "v_mfma_f32_16x16x32_bf16 v[0:3], a[88:91], a[24:27], v[0:3]\n"
    "ds_read_b128 a[76:79], v27 offset:50176\n"
    "v_cvt_pk_bf16_f32 v166, v12, v13\n"
    "v_mfma_f32_16x16x32_bf16 v[4:7], a[88:91], a[56:59], v[4:7]\n"
    "v_exp_f32 v14, v14\n"
    "v_mfma_f32_16x16x32_bf16 v[0:3], a[92:95], a[28:31], v[0:3]\n"
    "ds_read_b128 a[80:83], v26 offset:51200\n"
    "s_waitcnt lgkmcnt(5)\n"
    "v_mfma_f32_16x16x32_bf16 v[4:7], a[92:95], a[60:63], v[4:7]\n"
    "v_exp_f32 v15, v15\n"
    "v_mfma_f32_16x16x32_bf16 v[8:11], a[96:99], a[0:3], v[16:19]\n"
    "ds_read_b128 a[84:87], v27 offset:51200\n"
    "v_cvt_pk_bf16_f32 v167, v14, v15\n"
    "v_mfma_f32_16x16x32_bf16 v[12:15], a[96:99], a[32:35], v[20:23]\n"
    "v_exp_f32 v0, v0\n"
    "v_mfma_f32_16x16x32_bf16 v[8:11], a[100:103], a[4:7], v[8:11]\n"
    "buffer_load_dwordx4 v36, s[12:15], s40 offen offset:2048 nt lds\n"
    "v_mfma_f32_16x16x32_bf16 v[12:15], a[100:103], a[36:39], v[12:15]\n"
    "v_exp_f32 v1, v1\n"
    "v_mfma_f32_16x16x32_bf16 v[8:11], a[104:107], a[8:11], v[8:11]\n"
    "ds_read_b128 a[88:91], v26 offset:52224\n"
    "v_cvt_pk_bf16_f32 v168, v0, v1\n"
    "v_mfma_f32_16x16x32_bf16 v[12:15], a[104:107], a[40:43], v[12:15]\n"
    "v_exp_f32 v2, v2\n"
    "v_mfma_f32_16x16x32_bf16 a[200:203], a[208:211], v[160:163], a[200:203]\n"
    "buffer_load_dwordx4 v37, s[12:15], s40 offen offset:3072 nt lds\n"
    "v_mfma_f32_16x16x32_bf16 v[8:11], a[108:111], a[12:15], v[8:11]\n"
    "ds_read_b128 a[92:95], v27 offset:52224\n"
    "ds_read_b128 a[96:99], v26 offset:57344\n"
    "v_mfma_f32_16x16x32_bf16 v[12:15], a[108:111], a[44:47], v[12:15]\n"
    "v_exp_f32 v3, v3\n"
    "v_mfma_f32_16x16x32_bf16 v[8:11], a[112:115], a[16:19], v[8:11]\n"
    "ds_read_b128 a[100:103], v27 offset:57344\n"
    "v_cvt_pk_bf16_f32 v169, v2, v3\n"
    "v_mfma_f32_16x16x32_bf16 v[12:15], a[112:115], a[48:51], v[12:15]\n"
    "v_exp_f32 v4, v4\n"
    "v_mfma_f32_16x16x32_bf16 v[8:11], a[116:119], a[20:23], v[8:11]\n"
    "ds_read_b128 a[104:107], v26 offset:58368\n"
    "s_add_u32 m0, s28, 0x8000\n"
    "s_mov_b32 s40, 0x42400\n"
    "buffer_load_dwordx4 v34, s[12:15], s40 offen offset:0 nt lds\n"
    "v_mfma_f32_16x16x32_bf16 v[12:15], a[116:119], a[52:55], v[12:15]\n"
    "v_exp_f32 v5, v5\n"
    "v_mfma_f32_16x16x32_bf16 v[8:11], a[120:123], a[24:27], v[8:11]\n"
    "ds_read_b128 a[108:111], v27 offset:58368\n"
    "v_cvt_pk_bf16_f32 v172, v4, v5\n"
    "v_mfma_f32_16x16x32_bf16 v[12:15], a[120:123], a[56:59], v[12:15]\n"
    "v_exp_f32 v6, v6\n"
    "v_mfma_f32_16x16x32_bf16 v[8:11], a[124:127], a[28:31], v[8:11]\n"
    "ds_read_b128 a[112:115], v26 offset:59392\n"
    "s_waitcnt vmcnt(13) lgkmcnt(5)\n"
    "v_mfma_f32_16x16x32_bf16 v[12:15], a[124:127], a[60:63], v[12:15]\n"
    "v_exp_f32 v7, v7\n"
    "v_mfma_f32_16x16x32_bf16 v[0:3], a[64:67], a[0:3], v[16:19]\n"
    "ds_read_b128 a[116:119], v27 offset:59392\n"
    "v_cvt_pk_bf16_f32 v173, v6, v7\n"
    "v_mfma_f32_16x16x32_bf16 v[4:7], a[64:67], a[32:35], v[20:23]\n"
    "v_exp_f32 v8, v8\n"
    "v_mfma_f32_16x16x32_bf16 v[0:3], a[68:71], a[4:7], v[0:3]\n"
    "buffer_load_dwordx4 v35, s[12:15], s40 offen offset:1024 nt lds\n"
    "v_mfma_f32_16x16x32_bf16 v[4:7], a[68:71], a[36:39], v[4:7]\n"
    "v_exp_f32 v9, v9\n"
    "v_mfma_f32_16x16x32_bf16 v[0:3], a[72:75], a[8:11], v[0:3]\n"
    "ds_read_b128 a[120:123], v26 offset:60416\n"
    "v_cvt_pk_bf16_f32 v170, v8, v9\n"
    "v_mfma_f32_16x16x32_bf16 v[4:7], a[72:75], a[40:43], v[4:7]\n"
    "v_exp_f32 v10, v10\n"
    "v_mfma_f32_16x16x32_bf16 a[204:207], a[208:211], v[164:167], a[204:207]\n"
    "s_barrier\n"
    "v_mfma_f32_16x16x32_bf16 v[0:3], a[76:79], a[12:15], v[0:3]\n"
    "ds_read_b128 a[124:127], v27 offset:60416\n"
    "ds_read_b128 a[64:67], v28 offset:0\n"
    "v_mfma_f32_16x16x32_bf16 v[4:7], a[76:79], a[44:47], v[4:7]\n"
    "v_exp_f32 v11, v11\n"
    "v_mfma_f32_16x16x32_bf16 v[0:3], a[80:83], a[16:19], v[0:3]\n"
    "ds_read_b128 a[68:71], v29 offset:0\n"
    "v_cvt_pk_bf16_f32 v171, v10, v11\n"
    "v_mfma_f32_16x16x32_bf16 v[4:7], a[80:83], a[48:51], v[4:7]\n"
    "v_exp_f32 v12, v12\n"
    "v_mfma_f32_16x16x32_bf16 v[0:3], a[84:87], a[20:23], v[0:3]\n"
    "ds_read_b128 a[72:75], v28 offset:1024\n"
    "buffer_load_dwordx4 v36, s[12:15], s40 offen offset:2048 nt lds\n"
    "v_mfma_f32_16x16x32_bf16 v[4:7], a[84:87], a[52:55], v[4:7]\n"
    "v_exp_f32 v13, v13\n"
    "v_mfma_f32_16x16x32_bf16 v[0:3], a[88:91], a[24:27], v[0:3]\n"
    "ds_read_b128 a[76:79], v29 offset:1024\n"
    "v_cvt_pk_bf16_f32 v174, v12, v13\n"
    "v_mfma_f32_16x16x32_bf16 v[4:7], a[88:91], a[56:59], v[4:7]\n"
    "v_exp_f32 v14, v14\n"
    "v_mfma_f32_16x16x32_bf16 v[0:3], a[92:95], a[28:31], v[0:3]\n"
    "ds_read_b128 a[80:83], v28 offset:2048\n"
    "s_waitcnt lgkmcnt(5)\n"
    "v_mfma_f32_16x16x32_bf16 v[4:7], a[92:95], a[60:63], v[4:7]\n"
    "v_exp_f32 v15, v15\n"
    "v_mfma_f32_16x16x32_bf16 v[8:11], a[96:99], a[0:3], v[16:19]\n"
    "ds_read_b128 a[84:87], v29 offset:2048\n"
    "v_cvt_pk_bf16_f32 v175, v14, v15\n"
    "v_mfma_f32_16x16x32_bf16 v[12:15], a[96:99], a[32:35], v[20:23]\n"
    "v_exp_f32 v0, v0\n"
    "v_mfma_f32_16x16x32_bf16 v[8:11], a[100:103], a[4:7], v[8:11]\n"
    "buffer_load_dwordx4 v37, s[12:15], s40 offen offset:3072 nt lds\n"
    "v_mfma_f32_16x16x32_bf16 v[12:15], a[100:103], a[36:39], v[12:15]\n"
    "v_exp_f32 v1, v1\n"
    "v_mfma_f32_16x16x32_bf16 v[8:11], a[104:107], a[8:11], v[8:11]\n"
    "ds_read_b128 a[88:91], v28 offset:3072\n"
    "v_cvt_pk_bf16_f32 v184, v0, v1\n"
    "v_mfma_f32_16x16x32_bf16 v[12:15], a[104:107], a[40:43], v[12:15]\n"
    "v_exp_f32 v2, v2\n"
    "v_mfma_f32_16x16x32_bf16 a[200:203], a[208:211], v[168:171], a[200:203]\n"
    "s_add_u32 m0, s28, 0xc000\n"
    "s_mov_b32 s40, 0x43400\n"
    "buffer_load_dwordx4 v34, s[12:15], s40 offen offset:0 nt lds\n"
    "v_mfma_f32_16x16x32_bf16 v[8:11], a[108:111], a[12:15], v[8:11]\n"
    "ds_read_b128 a[92:95], v29 offset:3072\n"
    "ds_read_b128 a[96:99], v28 offset:8192\n"
    "v_mfma_f32_16x16x32_bf16 v[12:15], a[108:111], a[44:47], v[12:15]\n"
    "v_exp_f32 v3, v3\n"
    "v_mfma_f32_16x16x32_bf16 v[8:11], a[112:115], a[16:19], v[8:11]\n"
    "ds_read_b128 a[100:103], v29 offset:8192\n"
    "v_cvt_pk_bf16_f32 v185, v2, v3\n"
    "v_mfma_f32_16x16x32_bf16 v[12:15], a[112:115], a[48:51], v[12:15]\n"
    "v_exp_f32 v4, v4\n"
    "v_mfma_f32_16x16x32_bf16 v[8:11], a[116:119], a[20:23], v[8:11]\n"
    "ds_read_b128 a[104:107], v28 offset:9216\n"
    "buffer_load_dwordx4 v35, s[12:15], s40 offen offset:1024 nt lds\n"
    "v_mfma_f32_16x16x32_bf16 v[12:15], a[116:119], a[52:55], v[12:15]\n"
    "v_exp_f32 v5, v5\n"
    "v_mfma_f32_16x16x32_bf16 v[8:11], a[120:123], a[24:27], v[8:11]\n"
    "ds_read_b128 a[108:111], v29 offset:9216\n"
    "v_cvt_pk_bf16_f32 v188, v4, v5\n"
    "v_mfma_f32_16x16x32_bf16 v[12:15], a[120:123], a[56:59], v[12:15]\n"
    "v_exp_f32 v6, v6\n"
    "v_mfma_f32_16x16x32_bf16 v[8:11], a[124:127], a[28:31], v[8:11]\n"
    "ds_read_b128 a[112:115], v28 offset:10240\n"
    "s_waitcnt vmcnt(14) lgkmcnt(5)\n"
    "v_mfma_f32_16x16x32_bf16 v[12:15], a[124:127], a[60:63], v[12:15]\n"
    "v_exp_f32 v7, v7\n"
    "v_mfma_f32_16x16x32_bf16 v[0:3], a[64:67], a[0:3], v[16:19]\n"
    "ds_read_b128 a[116:119], v29 offset:10240\n"
    "v_cvt_pk_bf16_f32 v189, v6, v7\n"
    "v_mfma_f32_16x16x32_bf16 v[4:7], a[64:67], a[32:35], v[20:23]\n"
    "v_exp_f32 v8, v8\n"
    "v_mfma_f32_16x16x32_bf16 v[0:3], a[68:71], a[4:7], v[0:3]\n"
    "buffer_load_dwordx4 v36, s[12:15], s40 offen offset:2048 nt lds\n"
    "v_mfma_f32_16x16x32_bf16 v[4:7], a[68:71], a[36:39], v[4:7]\n"
    "v_exp_f32 v9, v9\n"
    "v_mfma_f32_16x16x32_bf16 v[0:3], a[72:75], a[8:11], v[0:3]\n"
    "ds_read_b128 a[120:123], v28 offset:11264\n"
    "v_cvt_pk_bf16_f32 v186, v8, v9\n"
    "v_mfma_f32_16x16x32_bf16 v[4:7], a[72:75], a[40:43], v[4:7]\n"
    "v_exp_f32 v10, v10\n"
    "v_mfma_f32_16x16x32_bf16 a[204:207], a[208:211], v[172:175], a[204:207]\n"
    "s_barrier\n"
    "v_mfma_f32_16x16x32_bf16 v[0:3], a[76:79], a[12:15], v[0:3]\n"
    "ds_read_b128 a[124:127], v29 offset:11264\n"
    "ds_read_b128 a[64:67], v28 offset:16384\n"
    "v_mfma_f32_16x16x32_bf16 v[4:7], a[76:79], a[44:47], v[4:7]\n"
    "v_exp_f32 v11, v11\n"
    "v_mfma_f32_16x16x32_bf16 v[0:3], a[80:83], a[16:19], v[0:3]\n"
    "ds_read_b128 a[68:71], v29 offset:16384\n"
    "v_cvt_pk_bf16_f32 v187, v10, v11\n"
    "v_mfma_f32_16x16x32_bf16 v[4:7], a[80:83], a[48:51], v[4:7]\n"
    "v_exp_f32 v12, v12\n"
    "v_mfma_f32_16x16x32_bf16 v[0:3], a[84:87], a[20:23], v[0:3]\n"
    "ds_read_b128 a[72:75], v28 offset:17408\n"
    "buffer_load_dwordx4 v37, s[12:15], s40 offen offset:3072 nt lds\n"
    "v_mfma_f32_16x16x32_bf16 v[4:7], a[84:87], a[52:55], v[4:7]\n"
    "v_exp_f32 v13, v13\n"
    "v_mfma_f32_16x16x32_bf16 v[0:3], a[88:91], a[24:27], v[0:3]\n"
    "ds_read_b128 a[76:79], v29 offset:17408\n"
    "v_cvt_pk_bf16_f32 v190, v12, v13\n"
    "v_mfma_f32_16x16x32_bf16 v[4:7], a[88:91], a[56:59], v[4:7]\n"
    "v_exp_f32 v14, v14\n"
    "v_mfma_f32_16x16x32_bf16 v[0:3], a[92:95], a[28:31], v[0:3]\n"
    "ds_read_b128 a[80:83], v28 offset:18432\n"
    "s_waitcnt lgkmcnt(5)\n"
    "v_mfma_f32_16x16x32_bf16 v[4:7], a[92:95], a[60:63], v[4:7]\n"
    "v_exp_f32 v15, v15\n"
    "v_mfma_f32_16x16x32_bf16 v[8:11], a[96:99], a[0:3], v[16:19]\n"
    "ds_read_b128 a[84:87], v29 offset:18432\n"
    "v_cvt_pk_bf16_f32 v191, v14, v15\n"
    "v_mfma_f32_16x16x32_bf16 v[12:15], a[96:99], a[32:35], v[20:23]\n"
    "v_exp_f32 v0, v0\n"
    "v_mfma_f32_16x16x32_bf16 v[8:11], a[100:103], a[4:7], v[8:11]\n"
    "ds_read_b128 a[88:91], v28 offset:19456\n"
    "s_add_u32 m0, s28, 0x10000\n"
    "s_mov_b32 s40, 0x60000\n"
    "buffer_load_dwordx4 v34, s[12:15], s40 offen offset:0 nt lds\n"
    "v_mfma_f32_16x16x32_bf16 v[12:15], a[100:103], a[36:39], v[12:15]\n"
    "v_exp_f32 v1, v1\n"
    "v_mfma_f32_16x16x32_bf16 v[8:11], a[104:107], a[8:11], v[8:11]\n"
    "ds_read_b128 a[92:95], v29 offset:19456\n"
    "v_cvt_pk_bf16_f32 v232, v0, v1\n"
    "v_mfma_f32_16x16x32_bf16 v[12:15], a[104:107], a[40:43], v[12:15]\n"
    "v_exp_f32 v2, v2\n"
    "v_mfma_f32_16x16x32_bf16 a[200:203], a[208:211], v[184:187], a[200:203]\n"
    "buffer_load_dwordx4 v35, s[12:15], s40 offen offset:1024 nt lds\n"
    "v_mfma_f32_16x16x32_bf16 v[8:11], a[108:111], a[12:15], v[8:11]\n"
    "ds_read_b128 a[96:99], v28 offset:24576\n"
    "v_mfma_f32_16x16x32_bf16 v[12:15], a[108:111], a[44:47], v[12:15]\n"
    "v_exp_f32 v3, v3\n"
    "v_mfma_f32_16x16x32_bf16 v[8:11], a[112:115], a[16:19], v[8:11]\n"
    "ds_read_b128 a[100:103], v29 offset:24576\n"
    "v_cvt_pk_bf16_f32 v233, v2, v3\n"
    "v_mfma_f32_16x16x32_bf16 v[12:15], a[112:115], a[48:51], v[12:15]\n"
    "v_exp_f32 v4, v4\n"
    "v_mfma_f32_16x16x32_bf16 v[8:11], a[116:119], a[20:23], v[8:11]\n"
    "ds_read_b128 a[104:107], v28 offset:25600\n"
    "buffer_load_dwordx4 v36, s[12:15], s40 offen offset:2048 nt lds\n"
    "v_mfma_f32_16x16x32_bf16 v[12:15], a[116:119], a[52:55], v[12:15]\n"
    "v_exp_f32 v5, v5\n"
    "v_mfma_f32_16x16x32_bf16 v[8:11], a[120:123], a[24:27], v[8:11]\n"
    "ds_read_b128 a[108:111], v29 offset:25600\n"
    "v_cvt_pk_bf16_f32 v236, v4, v5\n"
    "v_mfma_f32_16x16x32_bf16 v[12:15], a[120:123], a[56:59], v[12:15]\n"
    "v_exp_f32 v6, v6\n"
    "v_mfma_f32_16x16x32_bf16 v[8:11], a[124:127], a[28:31], v[8:11]\n"
    "ds_read_b128 a[112:115], v28 offset:26624\n"
    "s_waitcnt vmcnt(15) lgkmcnt(5)\n"
    "v_mfma_f32_16x16x32_bf16 v[12:15], a[124:127], a[60:63], v[12:15]\n"
    "v_exp_f32 v7, v7\n"
    "v_mfma_f32_16x16x32_bf16 v[0:3], a[64:67], a[0:3], v[16:19]\n"
    "ds_read_b128 a[116:119], v29 offset:26624\n"
    "v_cvt_pk_bf16_f32 v237, v6, v7\n"
    "v_mfma_f32_16x16x32_bf16 v[4:7], a[64:67], a[32:35], v[20:23]\n"
    "v_exp_f32 v8, v8\n"
    "v_mfma_f32_16x16x32_bf16 v[0:3], a[68:71], a[4:7], v[0:3]\n"
    "buffer_load_dwordx4 v37, s[12:15], s40 offen offset:3072 nt lds\n"
    "v_mfma_f32_16x16x32_bf16 v[4:7], a[68:71], a[36:39], v[4:7]\n"
    "v_exp_f32 v9, v9\n"
    "v_mfma_f32_16x16x32_bf16 v[0:3], a[72:75], a[8:11], v[0:3]\n"
    "ds_read_b128 a[120:123], v28 offset:27648\n"
    "v_cvt_pk_bf16_f32 v234, v8, v9\n"
    "v_mfma_f32_16x16x32_bf16 v[4:7], a[72:75], a[40:43], v[4:7]\n"
    "v_exp_f32 v10, v10\n"
    "v_mfma_f32_16x16x32_bf16 a[204:207], a[208:211], v[188:191], a[204:207]\n"
    "s_barrier\n"
    "v_mfma_f32_16x16x32_bf16 v[0:3], a[76:79], a[12:15], v[0:3]\n"
    "ds_read_b128 a[124:127], v29 offset:27648\n"
    "ds_read_b128 a[64:67], v24 offset:0\n"
    "v_mfma_f32_16x16x32_bf16 v[4:7], a[76:79], a[44:47], v[4:7]\n"
    "v_exp_f32 v11, v11\n"
    "v_mfma_f32_16x16x32_bf16 v[0:3], a[80:83], a[16:19], v[0:3]\n"
    "ds_read_b128 a[68:71], v25 offset:0\n"
    "v_cvt_pk_bf16_f32 v235, v10, v11\n"
    "v_mfma_f32_16x16x32_bf16 v[4:7], a[80:83], a[48:51], v[4:7]\n"
    "v_exp_f32 v12, v12\n"
    "v_mfma_f32_16x16x32_bf16 v[0:3], a[84:87], a[20:23], v[0:3]\n"
    "ds_read_b128 a[72:75], v24 offset:1024\n"
    "s_add_u32 m0, s28, 0x14000\n"
    "s_mov_b32 s40, 0x61000\n"
    "buffer_load_dwordx4 v34, s[12:15], s40 offen offset:0 nt lds\n"
    "v_mfma_f32_16x16x32_bf16 v[4:7], a[84:87], a[52:55], v[4:7]\n"
    "v_exp_f32 v13, v13\n"
    "v_mfma_f32_16x16x32_bf16 v[0:3], a[88:91], a[24:27], v[0:3]\n"
    "ds_read_b128 a[76:79], v25 offset:1024\n"
    "v_cvt_pk_bf16_f32 v238, v12, v13\n"
    "v_mfma_f32_16x16x32_bf16 v[4:7], a[88:91], a[56:59], v[4:7]\n"
    "v_exp_f32 v14, v14\n"
    "v_mfma_f32_16x16x32_bf16 v[0:3], a[92:95], a[28:31], v[0:3]\n"
    "ds_read_b128 a[80:83], v24 offset:2048\n"
    "s_waitcnt lgkmcnt(5)\n"
    "v_mfma_f32_16x16x32_bf16 v[4:7], a[92:95], a[60:63], v[4:7]\n"
    "v_exp_f32 v15, v15\n"
    "v_mfma_f32_16x16x32_bf16 v[8:11], a[96:99], a[0:3], v[16:19]\n"
    "ds_read_b128 a[84:87], v25 offset:2048\n"
    "v_cvt_pk_bf16_f32 v239, v14, v15\n"
    "v_mfma_f32_16x16x32_bf16 v[12:15], a[96:99], a[32:35], v[20:23]\n"
    "v_exp_f32 v0, v0\n"
    "v_mfma_f32_16x16x32_bf16 v[8:11], a[100:103], a[4:7], v[8:11]\n"
    "buffer_load_dwordx4 v35, s[12:15], s40 offen offset:1024 nt lds\n"
    "v_mfma_f32_16x16x32_bf16 v[12:15], a[100:103], a[36:39], v[12:15]\n"
    "v_exp_f32 v1, v1\n"
    "v_mfma_f32_16x16x32_bf16 v[8:11], a[104:107], a[8:11], v[8:11]\n"
    "ds_read_b128 a[88:91], v24 offset:3072\n"
    "v_cvt_pk_bf16_f32 v248, v0, v1\n"
    "v_mfma_f32_16x16x32_bf16 v[12:15], a[104:107], a[40:43], v[12:15]\n"
    "v_exp_f32 v2, v2\n"
    "v_mfma_f32_16x16x32_bf16 a[200:203], a[208:211], v[232:235], a[200:203]\n"
    "buffer_load_dwordx4 v36, s[12:15], s40 offen offset:2048 nt lds\n"
    "v_mfma_f32_16x16x32_bf16 v[8:11], a[108:111], a[12:15], v[8:11]\n"
    "ds_read_b128 a[92:95], v25 offset:3072\n"
    "ds_read_b128 a[96:99], v24 offset:8192\n"
    "v_mfma_f32_16x16x32_bf16 v[12:15], a[108:111], a[44:47], v[12:15]\n"
    "v_exp_f32 v3, v3\n"
    "v_mfma_f32_16x16x32_bf16 v[8:11], a[112:115], a[16:19], v[8:11]\n"
    "ds_read_b128 a[100:103], v25 offset:8192\n"
    "v_cvt_pk_bf16_f32 v249, v2, v3\n"
    "v_mfma_f32_16x16x32_bf16 v[12:15], a[112:115], a[48:51], v[12:15]\n"
    "v_exp_f32 v4, v4\n"
    "v_mfma_f32_16x16x32_bf16 v[8:11], a[116:119], a[20:23], v[8:11]\n"
    "ds_read_b128 a[104:107], v24 offset:9216\n"
    "buffer_load_dwordx4 v37, s[12:15], s40 offen offset:3072 nt lds\n"
    "v_mfma_f32_16x16x32_bf16 v[12:15], a[116:119], a[52:55], v[12:15]\n"
    "v_exp_f32 v5, v5\n"
    "v_mfma_f32_16x16x32_bf16 v[8:11], a[120:123], a[24:27], v[8:11]\n"
    "ds_read_b128 a[108:111], v25 offset:9216\n"
    "v_cvt_pk_bf16_f32 v252, v4, v5\n"
    "v_mfma_f32_16x16x32_bf16 v[12:15], a[120:123], a[56:59], v[12:15]\n"
    "v_exp_f32 v6, v6\n"
    "v_mfma_f32_16x16x32_bf16 v[8:11], a[124:127], a[28:31], v[8:11]\n"
    "ds_read_b128 a[112:115], v24 offset:10240\n"
    "s_waitcnt vmcnt(16) lgkmcnt(5)\n"
    "v_mfma_f32_16x16x32_bf16 v[12:15], a[124:127], a[60:63], v[12:15]\n"
    "v_exp_f32 v7, v7\n"
    "v_mfma_f32_16x16x32_bf16 v[0:3], a[64:67], a[0:3], v[16:19]\n"
    "ds_read_b128 a[116:119], v25 offset:10240\n"
    "v_cvt_pk_bf16_f32 v253, v6, v7\n"
    "v_mfma_f32_16x16x32_bf16 v[4:7], a[64:67], a[32:35], v[20:23]\n"
    "v_exp_f32 v8, v8\n"
    "v_mfma_f32_16x16x32_bf16 v[0:3], a[68:71], a[4:7], v[0:3]\n"
    "ds_read_b128 a[120:123], v24 offset:11264\n"
    "s_add_u32 m0, s28, 0x18000\n"
    "s_mov_b32 s40, 0x62000\n"
    "buffer_load_dwordx4 v34, s[12:15], s40 offen offset:0 nt lds\n"
    "v_mfma_f32_16x16x32_bf16 v[4:7], a[68:71], a[36:39], v[4:7]\n"
    "v_exp_f32 v9, v9\n"
    "v_mfma_f32_16x16x32_bf16 v[0:3], a[72:75], a[8:11], v[0:3]\n"
    "ds_read_b128 a[124:127], v25 offset:11264\n"
    "v_cvt_pk_bf16_f32 v250, v8, v9\n"
    "v_mfma_f32_16x16x32_bf16 v[4:7], a[72:75], a[40:43], v[4:7]\n"
    "v_exp_f32 v10, v10\n"
    "v_mfma_f32_16x16x32_bf16 a[204:207], a[208:211], v[236:239], a[204:207]\n"
    "s_barrier\n"
    "v_mfma_f32_16x16x32_bf16 v[0:3], a[76:79], a[12:15], v[0:3]\n"
    "ds_read_b128 a[64:67], v24 offset:16384\n"
    "STUB3_%=:\n"
    "s_cbranch_execz STUB7_%=\n"
    "v_mfma_f32_16x16x32_bf16 v[4:7], a[76:79], a[44:47], v[4:7]\n"
    "v_exp_f32 v11, v11\n"
    "v_mfma_f32_16x16x32_bf16 v[0:3], a[80:83], a[16:19], v[0:3]\n"
    "ds_read_b128 a[68:71], v25 offset:16384\n"
    "v_cvt_pk_bf16_f32 v251, v10, v11\n"
    "v_mfma_f32_16x16x32_bf16 v[4:7], a[80:83], a[48:51], v[4:7]\n"
    "v_exp_f32 v12, v12\n"
    "v_mfma_f32_16x16x32_bf16 v[0:3], a[84:87], a[20:23], v[0:3]\n"
    "ds_read_b128 a[72:75], v24 offset:17408\n"
    "buffer_load_dwordx4 v35, s[12:15], s40 offen offset:1024 nt lds\n"
    "v_mfma_f32_16x16x32_bf16 v[4:7], a[84:87], a[52:55], v[4:7]\n"
    "v_exp_f32 v13, v13\n"
    "v_mfma_f32_16x16x32_bf16 v[0:3], a[88:91], a[24:27], v[0:3]\n"
    "ds_read_b128 a[76:79], v25 offset:17408\n"
    "v_cvt_pk_bf16_f32 v254, v12, v13\n"
    "v_mfma_f32_16x16x32_bf16 v[4:7], a[88:91], a[56:59], v[4:7]\n"
    "v_exp_f32 v14, v14\n"
    "v_mfma_f32_16x16x32_bf16 v[0:3], a[92:95], a[28:31], v[0:3]\n"
    "ds_read_b128 a[80:83], v24 offset:18432\n"
    "s_waitcnt lgkmcnt(5)\n"
    "v_mfma_f32_16x16x32_bf16 v[4:7], a[92:95], a[60:63], v[4:7]\n"
    "v_exp_f32 v15, v15\n"
    "v_mfma_f32_16x16x32_bf16 v[8:11], a[96:99], a[0:3], v[16:19]\n"
    "ds_read_b128 a[84:87], v25 offset:18432\n"
    "v_cvt_pk_bf16_f32 v255, v14, v15\n"
    "v_mfma_f32_16x16x32_bf16 v[12:15], a[96:99], a[32:35], v[20:23]\n"
    "v_exp_f32 v0, v0\n"
    "v_mfma_f32_16x16x32_bf16 v[8:11], a[100:103], a[4:7], v[8:11]\n"
    "buffer_load_dwordx4 v36, s[12:15], s40 offen offset:2048 nt lds\n"
    "v_mfma_f32_16x16x32_bf16 v[12:15], a[100:103], a[36:39], v[12:15]\n"
    "v_exp_f32 v1, v1\n"
    "v_mfma_f32_16x16x32_bf16 v[8:11], a[104:107], a[8:11], v[8:11]\n"
    "ds_read_b128 a[88:91], v24 offset:19456\n"
    "v_cvt_pk_bf16_f32 v176, v0, v1\n"
    "v_mfma_f32_16x16x32_bf16 v[12:15], a[104:107], a[40:43], v[12:15]\n"
    "v_exp_f32 v2, v2\n"
    "v_mfma_f32_16x16x32_bf16 a[200:203], a[208:211], v[248:251], a[200:203]\n"
    "buffer_load_dwordx4 v37, s[12:15], s40 offen offset:3072 nt lds\n"
    "v_mfma_f32_16x16x32_bf16 v[8:11], a[108:111], a[12:15], v[8:11]\n"
    "ds_read_b128 a[92:95], v25 offset:19456\n"
    "ds_read_b128 a[96:99], v24 offset:24576\n"
    "v_mfma_f32_16x16x32_bf16 v[12:15], a[108:111], a[44:47], v[12:15]\n"
    "v_exp_f32 v3, v3\n"
    "v_mfma_f32_16x16x32_bf16 v[8:11], a[112:115], a[16:19], v[8:11]\n"
    "ds_read_b128 a[100:103], v25 offset:24576\n"
    "v_cvt_pk_bf16_f32 v177, v2, v3\n"
    "v_mfma_f32_16x16x32_bf16 v[12:15], a[112:115], a[48:51], v[12:15]\n"
    "v_exp_f32 v4, v4\n"
    "v_mfma_f32_16x16x32_bf16 v[8:11], a[116:119], a[20:23], v[8:11]\n"
    "ds_read_b128 a[104:107], v24 offset:25600\n"
    "s_add_u32 m0, s28, 0x1c000\n"
    "s_mov_b32 s40, 0x63000\n"
    "buffer_load_dwordx4 v34, s[12:15], s40 offen offset:0 nt lds\n"
    "v_mfma_f32_16x16x32_bf16 v[12:15], a[116:119], a[52:55], v[12:15]\n"
    "v_exp_f32 v5, v5\n"
    "v_mfma_f32_16x16x32_bf16 v[8:11], a[120:123], a[24:27], v[8:11]\n"
    "ds_read_b128 a[108:111], v25 offset:25600\n"
    "v_cvt_pk_bf16_f32 v180, v4, v5\n"
    "v_mfma_f32_16x16x32_bf16 v[12:15], a[120:123], a[56:59], v[12:15]\n"
    "v_exp_f32 v6, v6\n"
    "v_mfma_f32_16x16x32_bf16 v[8:11], a[124:127], a[28:31], v[8:11]\n"
    "ds_read_b128 a[112:115], v24 offset:26624\n"
    "s_waitcnt vmcnt(17) lgkmcnt(5)\n"
    "v_mfma_f32_16x16x32_bf16 v[12:15], a[124:127], a[60:63], v[12:15]\n"
    "v_exp_f32 v7, v7\n"
    "v_mfma_f32_16x16x32_bf16 v[0:3], a[64:67], a[0:3], v[16:19]\n"
    "ds_read_b128 a[116:119], v25 offset:26624\n"
    "v_cvt_pk_bf16_f32 v181, v6, v7\n"
    "v_mfma_f32_16x16x32_bf16 v[4:7], a[64:67], a[32:35], v[20:23]\n"
    "v_exp_f32 v8, v8\n"
    "v_mfma_f32_16x16x32_bf16 v[0:3], a[68:71], a[4:7], v[0:3]\n"
    "buffer_load_dwordx4 v35, s[12:15], s40 offen offset:1024 nt lds\n"
    "v_mfma_f32_16x16x32_bf16 v[4:7], a[68:71], a[36:39], v[4:7]\n"
    "v_exp_f32 v9, v9\n"
    "v_mfma_f32_16x16x32_bf16 v[0:3], a[72:75], a[8:11], v[0:3]\n"
    "ds_read_b128 a[120:123], v24 offset:27648\n"
    "v_cvt_pk_bf16_f32 v178, v8, v9\n"
    "v_mfma_f32_16x16x32_bf16 v[4:7], a[72:75], a[40:43], v[4:7]\n"
    "v_exp_f32 v10, v10\n"
    "v_mfma_f32_16x16x32_bf16 a[204:207], a[208:211], v[252:255], a[204:207]\n"
    "s_barrier\n"
    "v_mfma_f32_16x16x32_bf16 v[0:3], a[76:79], a[12:15], v[0:3]\n"
    "ds_read_b128 a[124:127], v25 offset:27648\n"
    "ds_read_b128 a[64:67], v24 offset:32768\n"
    "v_mfma_f32_16x16x32_bf16 v[4:7], a[76:79], a[44:47], v[4:7]\n"
    "v_exp_f32 v11, v11\n"
    "v_mfma_f32_16x16x32_bf16 v[0:3], a[80:83], a[16:19], v[0:3]\n"
    "ds_read_b128 a[68:71], v25 offset:32768\n"
    "v_cvt_pk_bf16_f32 v179, v10, v11\n"
    "v_mfma_f32_16x16x32_bf16 v[4:7], a[80:83], a[48:51], v[4:7]\n"
    "v_exp_f32 v12, v12\n"
    "v_mfma_f32_16x16x32_bf16 v[0:3], a[84:87], a[20:23], v[0:3]\n"
    "ds_read_b128 a[72:75], v24 offset:33792\n"
    "buffer_load_dwordx4 v36, s[12:15], s40 offen offset:2048 nt lds\n"
    "v_mfma_f32_16x16x32_bf16 v[4:7], a[84:87], a[52:55], v[4:7]\n"
    "v_exp_f32 v13, v13\n"
    "v_mfma_f32_16x16x32_bf16 v[0:3], a[88:91], a[24:27], v[0:3]\n"
    "ds_read_b128 a[76:79], v25 offset:33792\n"
    "v_cvt_pk_bf16_f32 v182, v12, v13\n"
    "v_mfma_f32_16x16x32_bf16 v[4:7], a[88:91], a[56:59], v[4:7]\n"
    "v_exp_f32 v14, v14\n"
    "v_mfma_f32_16x16x32_bf16 v[0:3], a[92:95], a[28:31], v[0:3]\n"
    "ds_read_b128 a[80:83], v24 offset:34816\n"
    "s_waitcnt lgkmcnt(5)\n"
    "v_mfma_f32_16x16x32_bf16 v[4:7], a[92:95], a[60:63], v[4:7]\n"
    "v_exp_f32 v15, v15\n"
    "v_mfma_f32_16x16x32_bf16 v[8:11], a[96:99], a[0:3], v[16:19]\n"
    "ds_read_b128 a[84:87], v25 offset:34816\n"
    "v_cvt_pk_bf16_f32 v183, v14, v15\n"
    "v_mfma_f32_16x16x32_bf16 v[12:15], a[96:99], a[32:35], v[20:23]\n"
    "v_exp_f32 v0, v0\n"
    "v_mfma_f32_16x16x32_bf16 v[8:11], a[100:103], a[4:7], v[8:11]\n"
    "buffer_load_dwordx4 v37, s[12:15], s40 offen offset:3072 nt lds\n"
    "v_mfma_f32_16x16x32_bf16 v[12:15], a[100:103], a[36:39], v[12:15]\n"
    "v_exp_f32 v1, v1\n"
    "v_mfma_f32_16x16x32_bf16 v[8:11], a[104:107], a[8:11], v[8:11]\n"
    "ds_read_b128 a[88:91], v24 offset:35840\n"
    "v_cvt_pk_bf16_f32 v192, v0, v1\n"
    "v_mfma_f32_16x16x32_bf16 v[12:15], a[104:107], a[40:43], v[12:15]\n"
    "v_exp_f32 v2, v2\n"
    "v_mfma_f32_16x16x32_bf16 a[200:203], a[208:211], v[176:179], a[200:203]\n"
    "s_add_u32 m0, s28, 0x20000\n"
    "s_mov_b32 s40, 0x60400\n"
    "buffer_load_dwordx4 v34, s[12:15], s40 offen offset:0 nt lds\n"
    "v_mfma_f32_16x16x32_bf16 v[8:11], a[108:111], a[12:15], v[8:11]\n"
    "ds_read_b128 a[92:95], v25 offset:35840\n"
    "ds_read_b128 a[96:99], v24 offset:40960\n"
    "v_mfma_f32_16x16x32_bf16 v[12:15], a[108:111], a[44:47], v[12:15]\n"
    "v_exp_f32 v3, v3\n"
    "v_mfma_f32_16x16x32_bf16 v[8:11], a[112:115], a[16:19], v[8:11]\n"
    "ds_read_b128 a[100:103], v25 offset:40960\n"
    "v_cvt_pk_bf16_f32 v193, v2, v3\n"
    "v_mfma_f32_16x16x32_bf16 v[12:15], a[112:115], a[48:51], v[12:15]\n"
    "v_exp_f32 v4, v4\n"
    "v_mfma_f32_16x16x32_bf16 v[8:11], a[116:119], a[20:23], v[8:11]\n"
    "ds_read_b128 a[104:107], v24 offset:41984\n"
    "buffer_load_dwordx4 v35, s[12:15], s40 offen offset:1024 nt lds\n"
    "v_mfma_f32_16x16x32_bf16 v[12:15], a[116:119], a[52:55], v[12:15]\n"
    "v_exp_f32 v5, v5\n"
    "v_mfma_f32_16x16x32_bf16 v[8:11], a[120:123], a[24:27], v[8:11]\n"
    "ds_read_b128 a[108:111], v25 offset:41984\n"
    "v_cvt_pk_bf16_f32 v196, v4, v5\n"
    "v_mfma_f32_16x16x32_bf16 v[12:15], a[120:123], a[56:59], v[12:15]\n"
    "v_exp_f32 v6, v6\n"
    "v_mfma_f32_16x16x32_bf16 v[8:11], a[124:127], a[28:31], v[8:11]\n"
    "ds_read_b128 a[112:115], v24 offset:43008\n"
    "s_waitcnt vmcnt(18) lgkmcnt(5)\n"
    "v_mfma_f32_16x16x32_bf16 v[12:15], a[124:127], a[60:63], v[12:15]\n"
    "v_exp_f32 v7, v7\n"
    "v_mfma_f32_16x16x32_bf16 v[0:3], a[64:67], a[0:3], v[16:19]\n"
    "ds_read_b128 a[116:119], v25 offset:43008\n"
    "v_cvt_pk_bf16_f32 v197, v6, v7\n"
    "v_mfma_f32_16x16x32_bf16 v[4:7], a[64:67], a[32:35], v[20:23]\n"
    "v_exp_f32 v8, v8\n"
    "v_mfma_f32_16x16x32_bf16 v[0:3], a[68:71], a[4:7], v[0:3]\n"
    "buffer_load_dwordx4 v36, s[12:15], s40 offen offset:2048 nt lds\n"
    "v_mfma_f32_16x16x32_bf16 v[4:7], a[68:71], a[36:39], v[4:7]\n"
    "v_exp_f32 v9, v9\n"
    "v_mfma_f32_16x16x32_bf16 v[0:3], a[72:75], a[8:11], v[0:3]\n"
    "ds_read_b128 a[120:123], v24 offset:44032\n"
    "v_cvt_pk_bf16_f32 v194, v8, v9\n"
    "v_mfma_f32_16x16x32_bf16 v[4:7], a[72:75], a[40:43], v[4:7]\n"
    "v_exp_f32 v10, v10\n"
    "v_mfma_f32_16x16x32_bf16 a[204:207], a[208:211], v[180:183], a[204:207]\n"
    "s_barrier\n"
    "v_mfma_f32_16x16x32_bf16 v[0:3], a[76:79], a[12:15], v[0:3]\n"
    "ds_read_b128 a[124:127], v25 offset:44032\n"
    "ds_read_b128 a[64:67], v24 offset:49152\n"
    "v_mfma_f32_16x16x32_bf16 v[4:7], a[76:79], a[44:47], v[4:7]\n"
    "v_exp_f32 v11, v11\n"
    "v_mfma_f32_16x16x32_bf16 v[0:3], a[80:83], a[16:19], v[0:3]\n"
    "ds_read_b128 a[68:71], v25 offset:49152\n"
    "v_cvt_pk_bf16_f32 v195, v10, v11\n"
    "v_mfma_f32_16x16x32_bf16 v[4:7], a[80:83], a[48:51], v[4:7]\n"
    "v_exp_f32 v12, v12\n"
    "v_mfma_f32_16x16x32_bf16 v[0:3], a[84:87], a[20:23], v[0:3]\n"
    "ds_read_b128 a[72:75], v24 offset:50176\n"
    "buffer_load_dwordx4 v37, s[12:15], s40 offen offset:3072 nt lds\n"
    "v_mfma_f32_16x16x32_bf16 v[4:7], a[84:87], a[52:55], v[4:7]\n"
    "v_exp_f32 v13, v13\n"
    "v_mfma_f32_16x16x32_bf16 v[0:3], a[88:91], a[24:27], v[0:3]\n"
    "ds_read_b128 a[76:79], v25 offset:50176\n"
    "v_cvt_pk_bf16_f32 v198, v12, v13\n"
    "v_mfma_f32_16x16x32_bf16 v[4:7], a[88:91], a[56:59], v[4:7]\n"
    "v_exp_f32 v14, v14\n"
    "v_mfma_f32_16x16x32_bf16 v[0:3], a[92:95], a[28:31], v[0:3]\n"
    "ds_read_b128 a[80:83], v24 offset:51200\n"
    "s_waitcnt lgkmcnt(5)\n"
    "v_mfma_f32_16x16x32_bf16 v[4:7], a[92:95], a[60:63], v[4:7]\n"
    "v_exp_f32 v15, v15\n"
    "v_mfma_f32_16x16x32_bf16 v[8:11], a[96:99], a[0:3], v[16:19]\n"
    "ds_read_b128 a[84:87], v25 offset:51200\n"
    "v_cvt_pk_bf16_f32 v199, v14, v15\n"
    "v_mfma_f32_16x16x32_bf16 v[12:15], a[96:99], a[32:35], v[20:23]\n"
    "v_exp_f32 v0, v0\n"
    "v_mfma_f32_16x16x32_bf16 v[8:11], a[100:103], a[4:7], v[8:11]\n"
    "ds_read_b128 a[88:91], v24 offset:52224\n"
    "s_add_u32 m0, s28, 0x24000\n"
    "s_mov_b32 s40, 0x61400\n"
    "buffer_load_dwordx4 v34, s[12:15], s40 offen offset:0 nt lds\n"
    "v_mfma_f32_16x16x32_bf16 v[12:15], a[100:103], a[36:39], v[12:15]\n"
    "v_exp_f32 v1, v1\n"
    "v_mfma_f32_16x16x32_bf16 v[8:11], a[104:107], a[8:11], v[8:11]\n"
    "ds_read_b128 a[92:95], v25 offset:52224\n"
    "v_cvt_pk_bf16_f32 v240, v0, v1\n"
    "v_mfma_f32_16x16x32_bf16 v[12:15], a[104:107], a[40:43], v[12:15]\n"
    "v_exp_f32 v2, v2\n"
    "v_mfma_f32_16x16x32_bf16 a[200:203], a[208:211], v[192:195], a[200:203]\n"
    "buffer_load_dwordx4 v35, s[12:15], s40 offen offset:1024 nt lds\n"
    "v_mfma_f32_16x16x32_bf16 v[8:11], a[108:111], a[12:15], v[8:11]\n"
    "ds_read_b128 a[96:99], v24 offset:57344\n"
    "v_mfma_f32_16x16x32_bf16 v[12:15], a[108:111], a[44:47], v[12:15]\n"
    "v_exp_f32 v3, v3\n"
    "v_mfma_f32_16x16x32_bf16 v[8:11], a[112:115], a[16:19], v[8:11]\n"
    "ds_read_b128 a[100:103], v25 offset:57344\n"
    "v_cvt_pk_bf16_f32 v241, v2, v3\n"
    "v_mfma_f32_16x16x32_bf16 v[12:15], a[112:115], a[48:51], v[12:15]\n"
    "v_exp_f32 v4, v4\n"
    "v_mfma_f32_16x16x32_bf16 v[8:11], a[116:119], a[20:23], v[8:11]\n"
    "ds_read_b128 a[104:107], v24 offset:58368\n"
    "buffer_load_dwordx4 v36, s[12:15], s40 offen offset:2048 nt lds\n"
    "v_mfma_f32_16x16x32_bf16 v[12:15], a[116:119], a[52:55], v[12:15]\n"
    "v_exp_f32 v5, v5\n"
    "v_mfma_f32_16x16x32_bf16 v[8:11], a[120:123], a[24:27], v[8:11]\n"
    "ds_read_b128 a[108:111], v25 offset:58368\n"
    "v_cvt_pk_bf16_f32 v244, v4, v5\n"
    "v_mfma_f32_16x16x32_bf16 v[12:15], a[120:123], a[56:59], v[12:15]\n"
    "v_exp_f32 v6, v6\n"
    "v_mfma_f32_16x16x32_bf16 v[8:11], a[124:127], a[28:31], v[8:11]\n"
    "ds_read_b128 a[112:115], v24 offset:59392\n"
    "s_waitcnt vmcnt(19) lgkmcnt(5)\n"
    "v_mfma_f32_16x16x32_bf16 v[12:15], a[124:127], a[60:63], v[12:15]\n"
    "v_exp_f32 v7, v7\n"
    "v_mfma_f32_16x16x32_bf16 v[0:3], a[64:67], a[0:3], v[16:19]\n"
    "ds_read_b128 a[116:119], v25 offset:59392\n"
    "v_cvt_pk_bf16_f32 v245, v6, v7\n"
    "v_mfma_f32_16x16x32_bf16 v[4:7], a[64:67], a[32:35], v[20:23]\n"
    "v_exp_f32 v8, v8\n"
    "v_mfma_f32_16x16x32_bf16 v[0:3], a[68:71], a[4:7], v[0:3]\n"
    "buffer_load_dwordx4 v37, s[12:15], s40 offen offset:3072 nt lds\n"
    "v_mfma_f32_16x16x32_bf16 v[4:7], a[68:71], a[36:39], v[4:7]\n"
    "v_exp_f32 v9, v9\n"
    "v_mfma_f32_16x16x32_bf16 v[0:3], a[72:75], a[8:11], v[0:3]\n"
    "ds_read_b128 a[120:123], v24 offset:60416\n"
    "v_cvt_pk_bf16_f32 v242, v8, v9\n"
    "v_mfma_f32_16x16x32_bf16 v[4:7], a[72:75], a[40:43], v[4:7]\n"
    "v_exp_f32 v10, v10\n"
    "v_mfma_f32_16x16x32_bf16 a[204:207], a[208:211], v[196:199], a[204:207]\n"
    "s_barrier\n"
    "v_mfma_f32_16x16x32_bf16 v[0:3], a[76:79], a[12:15], v[0:3]\n"
    "ds_read_b128 a[124:127], v25 offset:60416\n"
    "ds_read_b128 a[64:67], v26 offset:0\n"
    "v_mfma_f32_16x16x32_bf16 v[4:7], a[76:79], a[44:47], v[4:7]\n"
    "v_exp_f32 v11, v11\n"
    "v_mfma_f32_16x16x32_bf16 v[0:3], a[80:83], a[16:19], v[0:3]\n"
    "ds_read_b128 a[68:71], v27 offset:0\n"
    "v_cvt_pk_bf16_f32 v243, v10, v11\n"
    "v_mfma_f32_16x16x32_bf16 v[4:7], a[80:83], a[48:51], v[4:7]\n"
    "v_exp_f32 v12, v12\n"
    "v_mfma_f32_16x16x32_bf16 v[0:3], a[84:87], a[20:23], v[0:3]\n"
    "ds_read_b128 a[72:75], v26 offset:1024\n"
    "s_add_u32 m0, s28, 0x0\n"
    "s_mov_b32 s40, 0x62400\n"
    "buffer_load_dwordx4 v34, s[12:15], s40 offen offset:0 nt lds\n"
    "v_mfma_f32_16x16x32_bf16 v[4:7], a[84:87], a[52:55], v[4:7]\n"
    "v_exp_f32 v13, v13\n"
    "v_mfma_f32_16x16x32_bf16 v[0:3], a[88:91], a[24:27], v[0:3]\n"
    "ds_read_b128 a[76:79], v27 offset:1024\n"
    "v_cvt_pk_bf16_f32 v246, v12, v13\n"
    "v_mfma_f32_16x16x32_bf16 v[4:7], a[88:91], a[56:59], v[4:7]\n"
    "v_exp_f32 v14, v14\n"
    "v_mfma_f32_16x16x32_bf16 v[0:3], a[92:95], a[28:31], v[0:3]\n"
    "ds_read_b128 a[80:83], v26 offset:2048\n"
    "s_waitcnt lgkmcnt(5)\n"
    "v_mfma_f32_16x16x32_bf16 v[4:7], a[92:95], a[60:63], v[4:7]\n"
    "v_exp_f32 v15, v15\n"
    "v_mfma_f32_16x16x32_bf16 v[8:11], a[96:99], a[0:3], v[16:19]\n"
    "ds_read_b128 a[84:87], v27 offset:2048\n"
    "v_cvt_pk_bf16_f32 v247, v14, v15\n"
    "v_mfma_f32_16x16x32_bf16 v[12:15], a[96:99], a[32:35], v[20:23]\n"
    "v_exp_f32 v0, v0\n"
    "v_mfma_f32_16x16x32_bf16 v[8:11], a[100:103], a[4:7], v[8:11]\n"
    "buffer_load_dwordx4 v35, s[12:15], s40 offen offset:1024 nt lds\n"
    "v_mfma_f32_16x16x32_bf16 v[12:15], a[100:103], a[36:39], v[12:15]\n"
    "v_exp_f32 v1, v1\n"
    "v_mfma_f32_16x16x32_bf16 v[8:11], a[104:107], a[8:11], v[8:11]\n"
    "ds_read_b128 a[88:91], v26 offset:3072\n"
    "v_cvt_pk_bf16_f32 v0, v0, v1\n"
    "v_accvgpr_write_b32 a160, v0\n"
    "v_mfma_f32_16x16x32_bf16 v[12:15], a[104:107], a[40:43], v[12:15]\n"
    "v_exp_f32 v2, v2\n"
    "v_mfma_f32_16x16x32_bf16 a[200:203], a[208:211], v[240:243], a[200:203]\n"
    "buffer_load_dwordx4 v36, s[12:15], s40 offen offset:2048 nt lds\n"
    "v_mfma_f32_16x16x32_bf16 v[8:11], a[108:111], a[12:15], v[8:11]\n"
    "ds_read_b128 a[92:95], v27 offset:3072\n"
    "ds_read_b128 a[96:99], v26 offset:8192\n"
    "v_mfma_f32_16x16x32_bf16 v[12:15], a[108:111], a[44:47], v[12:15]\n"
    "v_exp_f32 v3, v3\n"
    "v_mfma_f32_16x16x32_bf16 v[8:11], a[112:115], a[16:19], v[8:11]\n"
    "ds_read_b128 a[100:103], v27 offset:8192\n"
    "v_cvt_pk_bf16_f32 v2, v2, v3\n"
    "v_accvgpr_write_b32 a161, v2\n"
    "v_mfma_f32_16x16x32_bf16 v[12:15], a[112:115], a[48:51], v[12:15]\n"
    "v_exp_f32 v4, v4\n"
    "v_mfma_f32_16x16x32_bf16 v[8:11], a[116:119], a[20:23], v[8:11]\n"
    "ds_read_b128 a[104:107], v26 offset:9216\n"
    "buffer_load_dwordx4 v37, s[12:15], s40 offen offset:3072 nt lds\n"
    "v_mfma_f32_16x16x32_bf16 v[12:15], a[116:119], a[52:55], v[12:15]\n"
    "v_exp_f32 v5, v5\n"
    "v_mfma_f32_16x16x32_bf16 v[8:11], a[120:123], a[24:27], v[8:11]\n"
    "ds_read_b128 a[108:111], v27 offset:9216\n"
    "v_cvt_pk_bf16_f32 v4, v4, v5\n"
    "v_accvgpr_write_b32 a164, v4\n"
    "v_mfma_f32_16x16x32_bf16 v[12:15], a[120:123], a[56:59], v[12:15]\n"
    "v_exp_f32 v6, v6\n"
    "v_mfma_f32_16x16x32_bf16 v[8:11], a[124:127], a[28:31], v[8:11]\n"
    "ds_read_b128 a[112:115], v26 offset:10240\n"
    "s_waitcnt vmcnt(20) lgkmcnt(5)\n"
    "v_mfma_f32_16x16x32_bf16 v[12:15], a[124:127], a[60:63], v[12:15]\n"
    "v_exp_f32 v7, v7\n"
    "v_mfma_f32_16x16x32_bf16 v[0:3], a[64:67], a[0:3], v[16:19]\n"
    "ds_read_b128 a[116:119], v27 offset:10240\n"
    "v_cvt_pk_bf16_f32 v6, v6, v7\n"
    "v_accvgpr_write_b32 a165, v6\n"
    "v_mfma_f32_16x16x32_bf16 v[4:7], a[64:67], a[32:35], v[20:23]\n"
    "v_exp_f32 v8, v8\n"
    "v_mfma_f32_16x16x32_bf16 v[0:3], a[68:71], a[4:7], v[0:3]\n"
    "ds_read_b128 a[120:123], v26 offset:11264\n"
    "s_add_u32 m0, s28, 0x4000\n"
    "s_mov_b32 s40, 0x63400\n"
    "buffer_load_dwordx4 v34, s[12:15], s40 offen offset:0 nt lds\n"
    "v_mfma_f32_16x16x32_bf16 v[4:7], a[68:71], a[36:39], v[4:7]\n"
    "v_exp_f32 v9, v9\n"
    "v_mfma_f32_16x16x32_bf16 v[0:3], a[72:75], a[8:11], v[0:3]\n"
    "ds_read_b128 a[124:127], v27 offset:11264\n"
    "v_cvt_pk_bf16_f32 v8, v8, v9\n"
    "v_accvgpr_write_b32 a162, v8\n"
    "v_mfma_f32_16x16x32_bf16 v[4:7], a[72:75], a[40:43], v[4:7]\n"
    "v_exp_f32 v10, v10\n"
    "v_mfma_f32_16x16x32_bf16 a[204:207], a[208:211], v[244:247], a[204:207]\n"
    "s_barrier\n"
    "v_mfma_f32_16x16x32_bf16 v[0:3], a[76:79], a[12:15], v[0:3]\n"
    "ds_read_b128 a[64:67], v26 offset:16384\n"
    "v_mfma_f32_16x16x32_bf16 v[4:7], a[76:79], a[44:47], v[4:7]\n"
    "v_exp_f32 v11, v11\n"
    "v_mfma_f32_16x16x32_bf16 v[0:3], a[80:83], a[16:19], v[0:3]\n"
    "ds_read_b128 a[68:71], v27 offset:16384\n"
    "v_cvt_pk_bf16_f32 v10, v10, v11\n"
    "v_accvgpr_write_b32 a163, v10\n"
    "v_mfma_f32_16x16x32_bf16 v[4:7], a[80:83], a[48:51], v[4:7]\n"
    "v_exp_f32 v12, v12\n"
    "v_mfma_f32_16x16x32_bf16 v[0:3], a[84:87], a[20:23], v[0:3]\n"
    "ds_read_b128 a[72:75], v26 offset:17408\n"
    "buffer_load_dwordx4 v35, s[12:15], s40 offen offset:1024 nt lds\n"
    "v_mfma_f32_16x16x32_bf16 v[4:7], a[84:87], a[52:55], v[4:7]\n"
    "v_exp_f32 v13, v13\n"
    "v_mfma_f32_16x16x32_bf16 v[0:3], a[88:91], a[24:27], v[0:3]\n"
    "ds_read_b128 a[76:79], v27 offset:17408\n"
    "v_cvt_pk_bf16_f32 v12, v12, v13\n"
    "v_accvgpr_write_b32 a166, v12\n"
    "v_mfma_f32_16x16x32_bf16 v[4:7], a[88:91], a[56:59], v[4:7]\n"
    "v_exp_f32 v14, v14\n"
    "v_mfma_f32_16x16x32_bf16 v[0:3], a[92:95], a[28:31], v[0:3]\n"
    "ds_read_b128 a[80:83], v26 offset:18432\n"
    "s_waitcnt lgkmcnt(5)\n"
    "v_mfma_f32_16x16x32_bf16 v[4:7], a[92:95], a[60:63], v[4:7]\n"
    "v_exp_f32 v15, v15\n"
    "v_mfma_f32_16x16x32_bf16 v[8:11], a[96:99], a[0:3], v[16:19]\n"
    "ds_read_b128 a[84:87], v27 offset:18432\n"
    "v_cvt_pk_bf16_f32 v14, v14, v15\n"
    "v_accvgpr_write_b32 a167, v14\n"
    "v_mfma_f32_16x16x32_bf16 v[12:15], a[96:99], a[32:35], v[20:23]\n"
    "v_exp_f32 v0, v0\n"
    "v_mfma_f32_16x16x32_bf16 v[8:11], a[100:103], a[4:7], v[8:11]\n"
    "ds_read_b128 a[88:91], v26 offset:19456\n"
    "v_mfma_f32_16x16x32_bf16 v[12:15], a[100:103], a[36:39], v[12:15]\n"
    "v_exp_f32 v1, v1\n"
    "v_mfma_f32_16x16x32_bf16 v[8:11], a[104:107], a[8:11], v[8:11]\n"
    "ds_read_b128 a[92:95], v27 offset:19456\n"
    "v_cvt_pk_bf16_f32 v200, v0, v1\n"
    "v_mfma_f32_16x16x32_bf16 v[12:15], a[104:107], a[40:43], v[12:15]\n"
    "v_exp_f32 v2, v2\n"
    "v_mfma_f32_16x16x32_bf16 a[200:203], a[208:211], a[160:163], a[200:203]\n"
    "buffer_load_dwordx4 v36, s[12:15], s40 offen offset:2048 nt lds\n"
    "v_mfma_f32_16x16x32_bf16 v[8:11], a[108:111], a[12:15], v[8:11]\n"
    "ds_read_b128 a[96:99], v26 offset:24576\n"
    "v_mfma_f32_16x16x32_bf16 v[12:15], a[108:111], a[44:47], v[12:15]\n"
    "v_exp_f32 v3, v3\n"
    "v_mfma_f32_16x16x32_bf16 v[8:11], a[112:115], a[16:19], v[8:11]\n"
    "ds_read_b128 a[100:103], v27 offset:24576\n"
    "v_cvt_pk_bf16_f32 v201, v2, v3\n"
    "v_mfma_f32_16x16x32_bf16 v[12:15], a[112:115], a[48:51], v[12:15]\n"
    "v_exp_f32 v4, v4\n"
    "v_mfma_f32_16x16x32_bf16 v[8:11], a[116:119], a[20:23], v[8:11]\n"
    "ds_read_b128 a[104:107], v26 offset:25600\n"
    "buffer_load_dwordx4 v37, s[12:15], s40 offen offset:3072 nt lds\n"
    "v_mfma_f32_16x16x32_bf16 v[12:15], a[116:119], a[52:55], v[12:15]\n"
    "v_exp_f32 v5, v5\n"
    "v_mfma_f32_16x16x32_bf16 v[8:11], a[120:123], a[24:27], v[8:11]\n"
    "ds_read_b128 a[108:111], v27 offset:25600\n"
    "v_cvt_pk_bf16_f32 v204, v4, v5\n"
    "v_mfma_f32_16x16x32_bf16 v[12:15], a[120:123], a[56:59], v[12:15]\n"
    "v_exp_f32 v6, v6\n"
    "v_mfma_f32_16x16x32_bf16 v[8:11], a[124:127], a[28:31], v[8:11]\n"
    "ds_read_b128 a[112:115], v26 offset:26624\n"
    "s_waitcnt vmcnt(20) lgkmcnt(5)\n"
    "v_mfma_f32_16x16x32_bf16 v[12:15], a[124:127], a[60:63], v[12:15]\n"
    "v_exp_f32 v7, v7\n"
    "v_mfma_f32_16x16x32_bf16 v[0:3], a[64:67], a[0:3], v[16:19]\n"
    "ds_read_b128 a[116:119], v27 offset:26624\n"
    "v_cvt_pk_bf16_f32 v205, v6, v7\n"
    "v_mfma_f32_16x16x32_bf16 v[4:7], a[64:67], a[32:35], v[20:23]\n"
    "v_exp_f32 v8, v8\n"
    "v_mfma_f32_16x16x32_bf16 v[0:3], a[68:71], a[4:7], v[0:3]\n"
    "ds_read_b128 a[120:123], v26 offset:27648\n"
    "v_mov_b32 v34, v38\n"
    "v_add_u32 v35, 0, v38\n"
    "v_add_u32 v36, 2048, v38\n"
    "v_add_u32 v37, 2048, v38\n"
    "s_add_u32 m0, s28, 0x8000\n"
    "s_add_u32 s40, s30, 0x0\n"
    "buffer_load_dwordx4 v34, s[16:19], s40 offen offset:0 nt lds\n"
    "v_mfma_f32_16x16x32_bf16 v[4:7], a[68:71], a[36:39], v[4:7]\n"
    "v_exp_f32 v9, v9\n"
    "v_mfma_f32_16x16x32_bf16 v[0:3], a[72:75], a[8:11], v[0:3]\n"
    "ds_read_b128 a[124:127], v27 offset:27648\n"
    "v_cvt_pk_bf16_f32 v202, v8, v9\n"
    "v_mfma_f32_16x16x32_bf16 v[4:7], a[72:75], a[40:43], v[4:7]\n"
    "v_exp_f32 v10, v10\n"
    "v_mfma_f32_16x16x32_bf16 a[204:207], a[208:211], a[164:167], a[204:207]\n"
    "s_barrier\n"
    "v_mfma_f32_16x16x32_bf16 v[0:3], a[76:79], a[12:15], v[0:3]\n"
    "ds_read_b128 a[64:67], v26 offset:32768\n"
    "v_mfma_f32_16x16x32_bf16 v[4:7], a[76:79], a[44:47], v[4:7]\n"
    "v_exp_f32 v11, v11\n"
    "v_mfma_f32_16x16x32_bf16 v[0:3], a[80:83], a[16:19], v[0:3]\n"
    "ds_read_b128 a[68:71], v27 offset:32768\n"
    "v_cvt_pk_bf16_f32 v203, v10, v11\n"
    "v_mfma_f32_16x16x32_bf16 v[4:7], a[80:83], a[48:51], v[4:7]\n"
    "v_exp_f32 v12, v12\n"
    "v_mfma_f32_16x16x32_bf16 v[0:3], a[84:87], a[20:23], v[0:3]\n"
    "ds_read_b128 a[72:75], v26 offset:33792\n"
    "buffer_load_dwordx4 v35, s[16:19], s40 offen offset:1024 nt lds\n"
    "v_mfma_f32_16x16x32_bf16 v[4:7], a[84:87], a[52:55], v[4:7]\n"
    "v_exp_f32 v13, v13\n"
    "v_mfma_f32_16x16x32_bf16 v[0:3], a[88:91], a[24:27], v[0:3]\n"
    "ds_read_b128 a[76:79], v27 offset:33792\n"
    "v_cvt_pk_bf16_f32 v206, v12, v13\n"
    "v_mfma_f32_16x16x32_bf16 v[4:7], a[88:91], a[56:59], v[4:7]\n"
    "v_exp_f32 v14, v14\n"
    "v_mfma_f32_16x16x32_bf16 v[0:3], a[92:95], a[28:31], v[0:3]\n"
    "ds_read_b128 a[80:83], v26 offset:34816\n"
    "s_waitcnt lgkmcnt(5)\n"
    "v_mfma_f32_16x16x32_bf16 v[4:7], a[92:95], a[60:63], v[4:7]\n"
    "v_exp_f32 v15, v15\n"
    "v_mfma_f32_16x16x32_bf16 v[8:11], a[96:99], a[0:3], v[16:19]\n"
    "ds_read_b128 a[84:87], v27 offset:34816\n"
    "v_cvt_pk_bf16_f32 v207, v14, v15\n"
    "v_mfma_f32_16x16x32_bf16 v[12:15], a[96:99], a[32:35], v[20:23]\n"
    "v_exp_f32 v0, v0\n"
    "v_mfma_f32_16x16x32_bf16 v[8:11], a[100:103], a[4:7], v[8:11]\n"
    "buffer_load_dwordx4 v36, s[16:19], s40 offen offset:2048 nt lds\n"
    "v_mfma_f32_16x16x32_bf16 v[12:15], a[100:103], a[36:39], v[12:15]\n"
    "v_exp_f32 v1, v1\n"
    "v_mfma_f32_16x16x32_bf16 v[8:11], a[104:107], a[8:11], v[8:11]\n"
    "ds_read_b128 a[88:91], v26 offset:35840\n"
    "v_cvt_pk_bf16_f32 v216, v0, v1\n"
    "v_mfma_f32_16x16x32_bf16 v[12:15], a[104:107], a[40:43], v[12:15]\n"
    "v_exp_f32 v2, v2\n"
    "v_mfma_f32_16x16x32_bf16 a[200:203], a[208:211], v[200:203], a[200:203]\n"
    "buffer_load_dwordx4 v37, s[16:19], s40 offen offset:3072 nt lds\n"
    "v_mfma_f32_16x16x32_bf16 v[8:11], a[108:111], a[12:15], v[8:11]\n"
    "ds_read_b128 a[92:95], v27 offset:35840\n"
    "ds_read_b128 a[96:99], v26 offset:40960\n"
    "v_mfma_f32_16x16x32_bf16 v[12:15], a[108:111], a[44:47], v[12:15]\n"
    "v_exp_f32 v3, v3\n"
    "v_mfma_f32_16x16x32_bf16 v[8:11], a[112:115], a[16:19], v[8:11]\n"
    "ds_read_b128 a[100:103], v27 offset:40960\n"
    "v_cvt_pk_bf16_f32 v217, v2, v3\n"
    "v_mfma_f32_16x16x32_bf16 v[12:15], a[112:115], a[48:51], v[12:15]\n"
    "v_exp_f32 v4, v4\n"
    "v_mfma_f32_16x16x32_bf16 v[8:11], a[116:119], a[20:23], v[8:11]\n"
    "ds_read_b128 a[104:107], v26 offset:41984\n"
    "s_add_u32 m0, s28, 0xc000\n"
    "s_add_u32 s40, s30, 0x20000\n"
    "buffer_load_dwordx4 v34, s[16:19], s40 offen offset:0 nt lds\n"
    "v_mfma_f32_16x16x32_bf16 v[12:15], a[116:119], a[52:55], v[12:15]\n"
    "v_exp_f32 v5, v5\n"
    "v_mfma_f32_16x16x32_bf16 v[8:11], a[120:123], a[24:27], v[8:11]\n"
    "ds_read_b128 a[108:111], v27 offset:41984\n"
    "v_cvt_pk_bf16_f32 v220, v4, v5\n"
    "v_mfma_f32_16x16x32_bf16 v[12:15], a[120:123], a[56:59], v[12:15]\n"
    "v_exp_f32 v6, v6\n"
    "v_mfma_f32_16x16x32_bf16 v[8:11], a[124:127], a[28:31], v[8:11]\n"
    "ds_read_b128 a[112:115], v26 offset:43008\n"
    "s_waitcnt vmcnt(21) lgkmcnt(5)\n"
    "v_mfma_f32_16x16x32_bf16 v[12:15], a[124:127], a[60:63], v[12:15]\n"
    "v_exp_f32 v7, v7\n"
    "v_mfma_f32_16x16x32_bf16 v[0:3], a[64:67], a[0:3], v[16:19]\n"
    "ds_read_b128 a[116:119], v27 offset:43008\n"
    "v_cvt_pk_bf16_f32 v221, v6, v7\n"
    "v_mfma_f32_16x16x32_bf16 v[4:7], a[64:67], a[32:35], v[20:23]\n"
    "v_exp_f32 v8, v8\n"
    "v_mfma_f32_16x16x32_bf16 v[0:3], a[68:71], a[4:7], v[0:3]\n"
    "buffer_load_dwordx4 v35, s[16:19], s40 offen offset:1024 nt lds\n"
    "v_mfma_f32_16x16x32_bf16 v[4:7], a[68:71], a[36:39], v[4:7]\n"
    "v_exp_f32 v9, v9\n"
    "v_mfma_f32_16x16x32_bf16 v[0:3], a[72:75], a[8:11], v[0:3]\n"
    "ds_read_b128 a[120:123], v26 offset:44032\n"
    "v_cvt_pk_bf16_f32 v218, v8, v9\n"
    "v_mfma_f32_16x16x32_bf16 v[4:7], a[72:75], a[40:43], v[4:7]\n"
    "v_exp_f32 v10, v10\n"
    "v_mfma_f32_16x16x32_bf16 a[204:207], a[208:211], v[204:207], a[204:207]\n"
    "s_barrier\n"
    "v_mfma_f32_16x16x32_bf16 v[0:3], a[76:79], a[12:15], v[0:3]\n"
    "ds_read_b128 a[124:127], v27 offset:44032\n"
    "ds_read_b128 a[64:67], v26 offset:49152\n"
    "v_mfma_f32_16x16x32_bf16 v[4:7], a[76:79], a[44:47], v[4:7]\n"
    "v_exp_f32 v11, v11\n"
    "v_mfma_f32_16x16x32_bf16 v[0:3], a[80:83], a[16:19], v[0:3]\n"
    "ds_read_b128 a[68:71], v27 offset:49152\n"
    "v_cvt_pk_bf16_f32 v219, v10, v11\n"
    "v_mfma_f32_16x16x32_bf16 v[4:7], a[80:83], a[48:51], v[4:7]\n"
    "v_exp_f32 v12, v12\n"
    "v_mfma_f32_16x16x32_bf16 v[0:3], a[84:87], a[20:23], v[0:3]\n"
    "ds_read_b128 a[72:75], v26 offset:50176\n"
    "buffer_load_dwordx4 v36, s[16:19], s40 offen offset:2048 nt lds\n"
    "v_mfma_f32_16x16x32_bf16 v[4:7], a[84:87], a[52:55], v[4:7]\n"
    "v_exp_f32 v13, v13\n"
    "v_mfma_f32_16x16x32_bf16 v[0:3], a[88:91], a[24:27], v[0:3]\n"
    "ds_read_b128 a[76:79], v27 offset:50176\n"
    "v_cvt_pk_bf16_f32 v222, v12, v13\n"
    "v_mfma_f32_16x16x32_bf16 v[4:7], a[88:91], a[56:59], v[4:7]\n"
    "v_exp_f32 v14, v14\n"
    "v_mfma_f32_16x16x32_bf16 v[0:3], a[92:95], a[28:31], v[0:3]\n"
    "ds_read_b128 a[80:83], v26 offset:51200\n"
    "s_waitcnt lgkmcnt(5)\n"
    "v_mfma_f32_16x16x32_bf16 v[4:7], a[92:95], a[60:63], v[4:7]\n"
    "v_exp_f32 v15, v15\n"
    "v_mfma_f32_16x16x32_bf16 v[8:11], a[96:99], a[0:3], v[16:19]\n"
    "ds_read_b128 a[84:87], v27 offset:51200\n"
    "v_cvt_pk_bf16_f32 v223, v14, v15\n"
    "v_mfma_f32_16x16x32_bf16 v[12:15], a[96:99], a[32:35], v[20:23]\n"
    "v_exp_f32 v0, v0\n"
    "v_mfma_f32_16x16x32_bf16 v[8:11], a[100:103], a[4:7], v[8:11]\n"
    "buffer_load_dwordx4 v37, s[16:19], s40 offen offset:3072 nt lds\n"
    "v_mfma_f32_16x16x32_bf16 v[12:15], a[100:103], a[36:39], v[12:15]\n"
    "v_exp_f32 v1, v1\n"
    "v_mfma_f32_16x16x32_bf16 v[8:11], a[104:107], a[8:11], v[8:11]\n"
    "ds_read_b128 a[88:91], v26 offset:52224\n"
    "v_cvt_pk_bf16_f32 v0, v0, v1\n"
    "v_accvgpr_write_b32 a168, v0\n"
    "v_mfma_f32_16x16x32_bf16 v[12:15], a[104:107], a[40:43], v[12:15]\n"
    "v_exp_f32 v2, v2\n"
    "v_mfma_f32_16x16x32_bf16 a[200:203], a[208:211], v[216:219], a[200:203]\n"
    "s_add_u32 m0, s28, 0x10000\n"
    "s_add_u32 s40, s30, 0x2000\n"
    "buffer_load_dwordx4 v34, s[16:19], s40 offen offset:0 nt lds\n"
    "v_mfma_f32_16x16x32_bf16 v[8:11], a[108:111], a[12:15], v[8:11]\n"
    "ds_read_b128 a[92:95], v27 offset:52224\n"
    "ds_read_b128 a[96:99], v26 offset:57344\n"
    "v_mfma_f32_16x16x32_bf16 v[12:15], a[108:111], a[44:47], v[12:15]\n"
    "v_exp_f32 v3, v3\n"
    "v_mfma_f32_16x16x32_bf16 v[8:11], a[112:115], a[16:19], v[8:11]\n"
    "ds_read_b128 a[100:103], v27 offset:57344\n"
    "v_cvt_pk_bf16_f32 v2, v2, v3\n"
    "v_accvgpr_write_b32 a169, v2\n"
    "v_mfma_f32_16x16x32_bf16 v[12:15], a[112:115], a[48:51], v[12:15]\n"
    "v_exp_f32 v4, v4\n"
    "STUB4_%=:\n"
    "s_cbranch_execz STUB8_%=\n"
    "v_mfma_f32_16x16x32_bf16 v[8:11], a[116:119], a[20:23], v[8:11]\n"
    "ds_read_b128 a[104:107], v26 offset:58368\n"
    "buffer_load_dwordx4 v35, s[16:19], s40 offen offset:1024 nt lds\n"
    "v_mfma_f32_16x16x32_bf16 v[12:15], a[116:119], a[52:55], v[12:15]\n"
    "v_exp_f32 v5, v5\n"
    "v_mfma_f32_16x16x32_bf16 v[8:11], a[120:123], a[24:27], v[8:11]\n"
    "ds_read_b128 a[108:111], v27 offset:58368\n"
    "v_cvt_pk_bf16_f32 v4, v4, v5\n"
    "v_accvgpr_write_b32 a172, v4\n"
    "v_mfma_f32_16x16x32_bf16 v[12:15], a[120:123], a[56:59], v[12:15]\n"
    "v_exp_f32 v6, v6\n"
    "v_mfma_f32_16x16x32_bf16 v[8:11], a[124:127], a[28:31], v[8:11]\n"
    "ds_read_b128 a[112:115], v26 offset:59392\n"
    "s_waitcnt vmcnt(22) lgkmcnt(5)\n"
    "v_mfma_f32_16x16x32_bf16 v[12:15], a[124:127], a[60:63], v[12:15]\n"
    "v_exp_f32 v7, v7\n"
    "v_mfma_f32_16x16x32_bf16 v[0:3], a[64:67], a[0:3], v[16:19]\n"
    "ds_read_b128 a[116:119], v27 offset:59392\n"
    "v_cvt_pk_bf16_f32 v6, v6, v7\n"
    "v_accvgpr_write_b32 a173, v6\n"
    "v_mfma_f32_16x16x32_bf16 v[4:7], a[64:67], a[32:35], v[20:23]\n"
    "v_exp_f32 v8, v8\n"
    "v_mfma_f32_16x16x32_bf16 v[0:3], a[68:71], a[4:7], v[0:3]\n"
    "buffer_load_dwordx4 v36, s[16:19], s40 offen offset:2048 nt lds\n"
    "v_mfma_f32_16x16x32_bf16 v[4:7], a[68:71], a[36:39], v[4:7]\n"
    "v_exp_f32 v9, v9\n"
    "v_mfma_f32_16x16x32_bf16 v[0:3], a[72:75], a[8:11], v[0:3]\n"
    "ds_read_b128 a[120:123], v26 offset:60416\n"
    "v_cvt_pk_bf16_f32 v8, v8, v9\n"
    "v_accvgpr_write_b32 a170, v8\n"
    "v_mfma_f32_16x16x32_bf16 v[4:7], a[72:75], a[40:43], v[4:7]\n"
    "v_exp_f32 v10, v10\n"
    "v_mfma_f32_16x16x32_bf16 a[204:207], a[208:211], v[220:223], a[204:207]\n"
    "s_barrier\n"
    "v_mfma_f32_16x16x32_bf16 v[0:3], a[76:79], a[12:15], v[0:3]\n"
    "ds_read_b128 a[124:127], v27 offset:60416\n"
    "ds_read_b128 a[64:67], v28 offset:0\n"
    "v_mfma_f32_16x16x32_bf16 v[4:7], a[76:79], a[44:47], v[4:7]\n"
    "v_exp_f32 v11, v11\n"
    "v_mfma_f32_16x16x32_bf16 v[0:3], a[80:83], a[16:19], v[0:3]\n"
    "ds_read_b128 a[68:71], v29 offset:0\n"
    "v_cvt_pk_bf16_f32 v10, v10, v11\n"
    "v_accvgpr_write_b32 a171, v10\n"
    "v_mfma_f32_16x16x32_bf16 v[4:7], a[80:83], a[48:51], v[4:7]\n"
    "v_exp_f32 v12, v12\n"
    "v_mfma_f32_16x16x32_bf16 v[0:3], a[84:87], a[20:23], v[0:3]\n"
    "ds_read_b128 a[72:75], v28 offset:1024\n"
    "buffer_load_dwordx4 v37, s[16:19], s40 offen offset:3072 nt lds\n"
    "v_mfma_f32_16x16x32_bf16 v[4:7], a[84:87], a[52:55], v[4:7]\n"
    "v_exp_f32 v13, v13\n"
    "v_mfma_f32_16x16x32_bf16 v[0:3], a[88:91], a[24:27], v[0:3]\n"
    "ds_read_b128 a[76:79], v29 offset:1024\n"
    "v_cvt_pk_bf16_f32 v12, v12, v13\n"
    "v_accvgpr_write_b32 a174, v12\n"
    "v_mfma_f32_16x16x32_bf16 v[4:7], a[88:91], a[56:59], v[4:7]\n"
    "v_exp_f32 v14, v14\n"
    "v_mfma_f32_16x16x32_bf16 v[0:3], a[92:95], a[28:31], v[0:3]\n"
    "ds_read_b128 a[80:83], v28 offset:2048\n"
    "s_waitcnt lgkmcnt(5)\n"
    "v_mfma_f32_16x16x32_bf16 v[4:7], a[92:95], a[60:63], v[4:7]\n"
    "v_exp_f32 v15, v15\n"
    "v_mfma_f32_16x16x32_bf16 v[8:11], a[96:99], a[0:3], v[16:19]\n"
    "ds_read_b128 a[84:87], v29 offset:2048\n"
    "v_cvt_pk_bf16_f32 v14, v14, v15\n"
    "v_accvgpr_write_b32 a175, v14\n"
    "v_mfma_f32_16x16x32_bf16 v[12:15], a[96:99], a[32:35], v[20:23]\n"
    "v_exp_f32 v0, v0\n"
    "v_mfma_f32_16x16x32_bf16 v[8:11], a[100:103], a[4:7], v[8:11]\n"
    "ds_read_b128 a[88:91], v28 offset:3072\n"
    "s_add_u32 m0, s28, 0x14000\n"
    "s_add_u32 s40, s30, 0x22000\n"
    "buffer_load_dwordx4 v34, s[16:19], s40 offen offset:0 nt lds\n"
    "v_mfma_f32_16x16x32_bf16 v[12:15], a[100:103], a[36:39], v[12:15]\n"
    "v_exp_f32 v1, v1\n"
    "v_mfma_f32_16x16x32_bf16 v[8:11], a[104:107], a[8:11], v[8:11]\n"
    "ds_read_b128 a[92:95], v29 offset:3072\n"
    "v_cvt_pk_bf16_f32 v0, v0, v1\n"
    "v_accvgpr_write_b32 a184, v0\n"
    "v_mfma_f32_16x16x32_bf16 v[12:15], a[104:107], a[40:43], v[12:15]\n"
    "v_exp_f32 v2, v2\n"
    "v_mfma_f32_16x16x32_bf16 a[200:203], a[208:211], a[168:171], a[200:203]\n"
    "buffer_load_dwordx4 v35, s[16:19], s40 offen offset:1024 nt lds\n"
    "v_mfma_f32_16x16x32_bf16 v[8:11], a[108:111], a[12:15], v[8:11]\n"
    "ds_read_b128 a[96:99], v28 offset:8192\n"
    "v_mfma_f32_16x16x32_bf16 v[12:15], a[108:111], a[44:47], v[12:15]\n"
    "v_exp_f32 v3, v3\n"
    "v_mfma_f32_16x16x32_bf16 v[8:11], a[112:115], a[16:19], v[8:11]\n"
    "ds_read_b128 a[100:103], v29 offset:8192\n"
    "v_cvt_pk_bf16_f32 v2, v2, v3\n"
    "v_accvgpr_write_b32 a185, v2\n"
    "v_mfma_f32_16x16x32_bf16 v[12:15], a[112:115], a[48:51], v[12:15]\n"
    "v_exp_f32 v4, v4\n"
    "v_mfma_f32_16x16x32_bf16 v[8:11], a[116:119], a[20:23], v[8:11]\n"
    "ds_read_b128 a[104:107], v28 offset:9216\n"
    "buffer_load_dwordx4 v36, s[16:19], s40 offen offset:2048 nt lds\n"
    "v_mfma_f32_16x16x32_bf16 v[12:15], a[116:119], a[52:55], v[12:15]\n"
    "v_exp_f32 v5, v5\n"
    "v_mfma_f32_16x16x32_bf16 v[8:11], a[120:123], a[24:27], v[8:11]\n"
    "ds_read_b128 a[108:111], v29 offset:9216\n"
    "v_cvt_pk_bf16_f32 v4, v4, v5\n"
    "v_accvgpr_write_b32 a188, v4\n"
    "v_mfma_f32_16x16x32_bf16 v[12:15], a[120:123], a[56:59], v[12:15]\n"
    "v_exp_f32 v6, v6\n"
    "v_mfma_f32_16x16x32_bf16 v[8:11], a[124:127], a[28:31], v[8:11]\n"
    "ds_read_b128 a[112:115], v28 offset:10240\n"
    "s_waitcnt vmcnt(23) lgkmcnt(5)\n"
    "v_mfma_f32_16x16x32_bf16 v[12:15], a[124:127], a[60:63], v[12:15]\n"
    "v_exp_f32 v7, v7\n"
    "v_mfma_f32_16x16x32_bf16 v[0:3], a[64:67], a[0:3], v[16:19]\n"
    "ds_read_b128 a[116:119], v29 offset:10240\n"
    "v_cvt_pk_bf16_f32 v6, v6, v7\n"
    "v_accvgpr_write_b32 a189, v6\n"
    "v_mfma_f32_16x16x32_bf16 v[4:7], a[64:67], a[32:35], v[20:23]\n"
    "v_exp_f32 v8, v8\n"
    "v_mfma_f32_16x16x32_bf16 v[0:3], a[68:71], a[4:7], v[0:3]\n"
    "buffer_load_dwordx4 v37, s[16:19], s40 offen offset:3072 nt lds\n"
    "v_mfma_f32_16x16x32_bf16 v[4:7], a[68:71], a[36:39], v[4:7]\n"
    "v_exp_f32 v9, v9\n"
    "v_mfma_f32_16x16x32_bf16 v[0:3], a[72:75], a[8:11], v[0:3]\n"
    "ds_read_b128 a[120:123], v28 offset:11264\n"
    "v_cvt_pk_bf16_f32 v8, v8, v9\n"
    "v_accvgpr_write_b32 a186, v8\n"
    "v_mfma_f32_16x16x32_bf16 v[4:7], a[72:75], a[40:43], v[4:7]\n"
    "v_exp_f32 v10, v10\n"
    "v_mfma_f32_16x16x32_bf16 a[204:207], a[208:211], a[172:175], a[204:207]\n"
    "s_barrier\n"
    "v_mfma_f32_16x16x32_bf16 v[0:3], a[76:79], a[12:15], v[0:3]\n"
    "ds_read_b128 a[124:127], v29 offset:11264\n"
    "ds_read_b128 a[64:67], v28 offset:16384\n"
    "v_mfma_f32_16x16x32_bf16 v[4:7], a[76:79], a[44:47], v[4:7]\n"
    "v_exp_f32 v11, v11\n"
    "v_mfma_f32_16x16x32_bf16 v[0:3], a[80:83], a[16:19], v[0:3]\n"
    "ds_read_b128 a[68:71], v29 offset:16384\n"
    "v_cvt_pk_bf16_f32 v10, v10, v11\n"
    "v_accvgpr_write_b32 a187, v10\n"
    "v_mfma_f32_16x16x32_bf16 v[4:7], a[80:83], a[48:51], v[4:7]\n"
    "v_exp_f32 v12, v12\n"
    "v_mfma_f32_16x16x32_bf16 v[0:3], a[84:87], a[20:23], v[0:3]\n"
    "ds_read_b128 a[72:75], v28 offset:17408\n"
    "s_add_u32 m0, s28, 0x18000\n"
    "s_add_u32 s40, s30, 0x40000\n"
    "buffer_load_dwordx4 v34, s[16:19], s40 offen offset:0 nt lds\n"
    "v_mfma_f32_16x16x32_bf16 v[4:7], a[84:87], a[52:55], v[4:7]\n"
    "v_exp_f32 v13, v13\n"
    "v_mfma_f32_16x16x32_bf16 v[0:3], a[88:91], a[24:27], v[0:3]\n"
    "ds_read_b128 a[76:79], v29 offset:17408\n"
    "v_cvt_pk_bf16_f32 v12, v12, v13\n"
    "v_accvgpr_write_b32 a190, v12\n"
    "v_mfma_f32_16x16x32_bf16 v[4:7], a[88:91], a[56:59], v[4:7]\n"
    "v_exp_f32 v14, v14\n"
    "v_mfma_f32_16x16x32_bf16 v[0:3], a[92:95], a[28:31], v[0:3]\n"
    "ds_read_b128 a[80:83], v28 offset:18432\n"
    "s_waitcnt lgkmcnt(5)\n"
    "v_mfma_f32_16x16x32_bf16 v[4:7], a[92:95], a[60:63], v[4:7]\n"
    "v_exp_f32 v15, v15\n"
    "v_mfma_f32_16x16x32_bf16 v[8:11], a[96:99], a[0:3], v[16:19]\n"
    "ds_read_b128 a[84:87], v29 offset:18432\n"
    "v_cvt_pk_bf16_f32 v14, v14, v15\n"
    "v_accvgpr_write_b32 a191, v14\n"
    "v_mfma_f32_16x16x32_bf16 v[12:15], a[96:99], a[32:35], v[20:23]\n"
    "v_exp_f32 v0, v0\n"
    "v_mfma_f32_16x16x32_bf16 v[8:11], a[100:103], a[4:7], v[8:11]\n"
    "buffer_load_dwordx4 v35, s[16:19], s40 offen offset:1024 nt lds\n"
    "v_mfma_f32_16x16x32_bf16 v[12:15], a[100:103], a[36:39], v[12:15]\n"
    "v_exp_f32 v1, v1\n"
    "v_mfma_f32_16x16x32_bf16 v[8:11], a[104:107], a[8:11], v[8:11]\n"
    "ds_read_b128 a[88:91], v28 offset:19456\n"
    "v_cvt_pk_bf16_f32 v208, v0, v1\n"
    "v_mfma_f32_16x16x32_bf16 v[12:15], a[104:107], a[40:43], v[12:15]\n"
    "v_exp_f32 v2, v2\n"
    "v_mfma_f32_16x16x32_bf16 a[200:203], a[208:211], a[184:187], a[200:203]\n"
    "buffer_load_dwordx4 v36, s[16:19], s40 offen offset:2048 nt lds\n"
    "v_mfma_f32_16x16x32_bf16 v[8:11], a[108:111], a[12:15], v[8:11]\n"
    "ds_read_b128 a[92:95], v29 offset:19456\n"
    "ds_read_b128 a[96:99], v28 offset:24576\n"
    "v_mfma_f32_16x16x32_bf16 v[12:15], a[108:111], a[44:47], v[12:15]\n"
    "v_exp_f32 v3, v3\n"
    "v_mfma_f32_16x16x32_bf16 v[8:11], a[112:115], a[16:19], v[8:11]\n"
    "ds_read_b128 a[100:103], v29 offset:24576\n"
    "v_cvt_pk_bf16_f32 v209, v2, v3\n"
    "v_mfma_f32_16x16x32_bf16 v[12:15], a[112:115], a[48:51], v[12:15]\n"
    "v_exp_f32 v4, v4\n"
    "v_mfma_f32_16x16x32_bf16 v[8:11], a[116:119], a[20:23], v[8:11]\n"
    "ds_read_b128 a[104:107], v28 offset:25600\n"
    "buffer_load_dwordx4 v37, s[16:19], s40 offen offset:3072 nt lds\n"
    "v_mfma_f32_16x16x32_bf16 v[12:15], a[116:119], a[52:55], v[12:15]\n"
    "v_exp_f32 v5, v5\n"
    "v_mfma_f32_16x16x32_bf16 v[8:11], a[120:123], a[24:27], v[8:11]\n"
    "ds_read_b128 a[108:111], v29 offset:25600\n"
    "v_cvt_pk_bf16_f32 v212, v4, v5\n"
    "v_mfma_f32_16x16x32_bf16 v[12:15], a[120:123], a[56:59], v[12:15]\n"
    "v_exp_f32 v6, v6\n"
    "v_mfma_f32_16x16x32_bf16 v[8:11], a[124:127], a[28:31], v[8:11]\n"
    "ds_read_b128 a[112:115], v28 offset:26624\n"
    "s_waitcnt vmcnt(24) lgkmcnt(5)\n"
    "v_mfma_f32_16x16x32_bf16 v[12:15], a[124:127], a[60:63], v[12:15]\n"
    "v_exp_f32 v7, v7\n"
    "v_mfma_f32_16x16x32_bf16 v[0:3], a[64:67], a[0:3], v[16:19]\n"
    "ds_read_b128 a[116:119], v29 offset:26624\n"
    "v_cvt_pk_bf16_f32 v213, v6, v7\n"
    "v_mfma_f32_16x16x32_bf16 v[4:7], a[64:67], a[32:35], v[20:23]\n"
    "v_exp_f32 v8, v8\n"
    "v_mfma_f32_16x16x32_bf16 v[0:3], a[68:71], a[4:7], v[0:3]\n"
    "ds_read_b128 a[120:123], v28 offset:27648\n"
    "s_add_u32 m0, s28, 0x1c000\n"
    "s_add_u32 s40, s30, 0x60000\n"
    "buffer_load_dwordx4 v34, s[16:19], s40 offen offset:0 nt lds\n"
    "v_mfma_f32_16x16x32_bf16 v[4:7], a[68:71], a[36:39], v[4:7]\n"
    "v_exp_f32 v9, v9\n"
    "v_mfma_f32_16x16x32_bf16 v[0:3], a[72:75], a[8:11], v[0:3]\n"
    "ds_read_b128 a[124:127], v29 offset:27648\n"
    "v_cvt_pk_bf16_f32 v210, v8, v9\n"
    "v_mfma_f32_16x16x32_bf16 v[4:7], a[72:75], a[40:43], v[4:7]\n"
    "v_exp_f32 v10, v10\n"
    "v_mfma_f32_16x16x32_bf16 a[204:207], a[208:211], a[188:191], a[204:207]\n"
    "s_barrier\n"
    "v_mfma_f32_16x16x32_bf16 v[0:3], a[76:79], a[12:15], v[0:3]\n"
    "ds_read_b128 a[64:67], v24 offset:0\n"
    "v_mfma_f32_16x16x32_bf16 v[4:7], a[76:79], a[44:47], v[4:7]\n"
    "v_exp_f32 v11, v11\n"
    "v_mfma_f32_16x16x32_bf16 v[0:3], a[80:83], a[16:19], v[0:3]\n"
    "ds_read_b128 a[68:71], v25 offset:0\n"
    "v_cvt_pk_bf16_f32 v211, v10, v11\n"
    "v_mfma_f32_16x16x32_bf16 v[4:7], a[80:83], a[48:51], v[4:7]\n"
    "v_exp_f32 v12, v12\n"
    "v_mfma_f32_16x16x32_bf16 v[0:3], a[84:87], a[20:23], v[0:3]\n"
    "ds_read_b128 a[72:75], v24 offset:1024\n"
    "buffer_load_dwordx4 v35, s[16:19], s40 offen offset:1024 nt lds\n"
    "v_mfma_f32_16x16x32_bf16 v[4:7], a[84:87], a[52:55], v[4:7]\n"
    "v_exp_f32 v13, v13\n"
    "v_mfma_f32_16x16x32_bf16 v[0:3], a[88:91], a[24:27], v[0:3]\n"
    "ds_read_b128 a[76:79], v25 offset:1024\n"
    "v_cvt_pk_bf16_f32 v214, v12, v13\n"
    "v_mfma_f32_16x16x32_bf16 v[4:7], a[88:91], a[56:59], v[4:7]\n"
    "v_exp_f32 v14, v14\n"
    "v_mfma_f32_16x16x32_bf16 v[0:3], a[92:95], a[28:31], v[0:3]\n"
    "ds_read_b128 a[80:83], v24 offset:2048\n"
    "s_waitcnt lgkmcnt(5)\n"
    "v_mfma_f32_16x16x32_bf16 v[4:7], a[92:95], a[60:63], v[4:7]\n"
    "v_exp_f32 v15, v15\n"
    "v_mfma_f32_16x16x32_bf16 v[8:11], a[96:99], a[0:3], v[16:19]\n"
    "ds_read_b128 a[84:87], v25 offset:2048\n"
    "v_cvt_pk_bf16_f32 v215, v14, v15\n"
    "v_mfma_f32_16x16x32_bf16 v[12:15], a[96:99], a[32:35], v[20:23]\n"
    "v_exp_f32 v0, v0\n"
    "v_mfma_f32_16x16x32_bf16 v[8:11], a[100:103], a[4:7], v[8:11]\n"
    "buffer_load_dwordx4 v36, s[16:19], s40 offen offset:2048 nt lds\n"
    "v_mfma_f32_16x16x32_bf16 v[12:15], a[100:103], a[36:39], v[12:15]\n"
    "v_exp_f32 v1, v1\n"
    "v_mfma_f32_16x16x32_bf16 v[8:11], a[104:107], a[8:11], v[8:11]\n"
    "ds_read_b128 a[88:91], v24 offset:3072\n"
    "v_cvt_pk_bf16_f32 v224, v0, v1\n"
    "v_mfma_f32_16x16x32_bf16 v[12:15], a[104:107], a[40:43], v[12:15]\n"
    "v_exp_f32 v2, v2\n"
    "v_mfma_f32_16x16x32_bf16 a[200:203], a[208:211], v[208:211], a[200:203]\n"
    "buffer_load_dwordx4 v37, s[16:19], s40 offen offset:3072 nt lds\n"
    "v_mfma_f32_16x16x32_bf16 v[8:11], a[108:111], a[12:15], v[8:11]\n"
    "ds_read_b128 a[92:95], v25 offset:3072\n"
    "ds_read_b128 a[96:99], v24 offset:8192\n"
    "v_mfma_f32_16x16x32_bf16 v[12:15], a[108:111], a[44:47], v[12:15]\n"
    "v_exp_f32 v3, v3\n"
    "v_mfma_f32_16x16x32_bf16 v[8:11], a[112:115], a[16:19], v[8:11]\n"
    "ds_read_b128 a[100:103], v25 offset:8192\n"
    "v_cvt_pk_bf16_f32 v225, v2, v3\n"
    "v_mfma_f32_16x16x32_bf16 v[12:15], a[112:115], a[48:51], v[12:15]\n"
    "v_exp_f32 v4, v4\n"
    "v_mfma_f32_16x16x32_bf16 v[8:11], a[116:119], a[20:23], v[8:11]\n"
    "ds_read_b128 a[104:107], v24 offset:9216\n"
    "s_add_u32 m0, s28, 0x20000\n"
    "s_add_u32 s40, s30, 0x42000\n"
    "buffer_load_dwordx4 v34, s[16:19], s40 offen offset:0 nt lds\n"
    "v_mfma_f32_16x16x32_bf16 v[12:15], a[116:119], a[52:55], v[12:15]\n"
    "v_exp_f32 v5, v5\n"
    "v_mfma_f32_16x16x32_bf16 v[8:11], a[120:123], a[24:27], v[8:11]\n"
    "ds_read_b128 a[108:111], v25 offset:9216\n"
    "v_cvt_pk_bf16_f32 v228, v4, v5\n"
    "v_mfma_f32_16x16x32_bf16 v[12:15], a[120:123], a[56:59], v[12:15]\n"
    "v_exp_f32 v6, v6\n"
    "v_mfma_f32_16x16x32_bf16 v[8:11], a[124:127], a[28:31], v[8:11]\n"
    "ds_read_b128 a[112:115], v24 offset:10240\n"
    "s_waitcnt vmcnt(25) lgkmcnt(5)\n"
    "v_mfma_f32_16x16x32_bf16 v[12:15], a[124:127], a[60:63], v[12:15]\n"
    "v_exp_f32 v7, v7\n"
    "v_mfma_f32_16x16x32_bf16 v[0:3], a[64:67], a[0:3], v[16:19]\n"
    "ds_read_b128 a[116:119], v25 offset:10240\n"
    "v_cvt_pk_bf16_f32 v229, v6, v7\n"
    "v_mfma_f32_16x16x32_bf16 v[4:7], a[64:67], a[32:35], v[20:23]\n"
    "v_exp_f32 v8, v8\n"
    "v_mfma_f32_16x16x32_bf16 v[0:3], a[68:71], a[4:7], v[0:3]\n"
    "buffer_load_dwordx4 v35, s[16:19], s40 offen offset:1024 nt lds\n"
    "v_mfma_f32_16x16x32_bf16 v[4:7], a[68:71], a[36:39], v[4:7]\n"
    "v_exp_f32 v9, v9\n"
    "v_mfma_f32_16x16x32_bf16 v[0:3], a[72:75], a[8:11], v[0:3]\n"
    "ds_read_b128 a[120:123], v24 offset:11264\n"
    "v_cvt_pk_bf16_f32 v226, v8, v9\n"
    "v_mfma_f32_16x16x32_bf16 v[4:7], a[72:75], a[40:43], v[4:7]\n"
    "v_exp_f32 v10, v10\n"
    "v_mfma_f32_16x16x32_bf16 a[204:207], a[208:211], v[212:215], a[204:207]\n"
    "s_barrier\n"
    "v_mfma_f32_16x16x32_bf16 v[0:3], a[76:79], a[12:15], v[0:3]\n"
    "ds_read_b128 a[124:127], v25 offset:11264\n"
    "ds_read_b128 a[64:67], v24 offset:16384\n"
    "v_mfma_f32_16x16x32_bf16 v[4:7], a[76:79], a[44:47], v[4:7]\n"
    "v_exp_f32 v11, v11\n"
    "v_mfma_f32_16x16x32_bf16 v[0:3], a[80:83], a[16:19], v[0:3]\n"
    "ds_read_b128 a[68:71], v25 offset:16384\n"
    "v_cvt_pk_bf16_f32 v227, v10, v11\n"
    "v_mfma_f32_16x16x32_bf16 v[4:7], a[80:83], a[48:51], v[4:7]\n"
    "v_exp_f32 v12, v12\n"
    "v_mfma_f32_16x16x32_bf16 v[0:3], a[84:87], a[20:23], v[0:3]\n"
    "ds_read_b128 a[72:75], v24 offset:17408\n"
    "buffer_load_dwordx4 v36, s[16:19], s40 offen offset:2048 nt lds\n"
    "v_mfma_f32_16x16x32_bf16 v[4:7], a[84:87], a[52:55], v[4:7]\n"
    "v_exp_f32 v13, v13\n"
    "v_mfma_f32_16x16x32_bf16 v[0:3], a[88:91], a[24:27], v[0:3]\n"
    "ds_read_b128 a[76:79], v25 offset:17408\n"
    "v_cvt_pk_bf16_f32 v230, v12, v13\n"
    "v_mfma_f32_16x16x32_bf16 v[4:7], a[88:91], a[56:59], v[4:7]\n"
    "v_exp_f32 v14, v14\n"
    "v_mfma_f32_16x16x32_bf16 v[0:3], a[92:95], a[28:31], v[0:3]\n"
    "ds_read_b128 a[80:83], v24 offset:18432\n"
    "s_waitcnt lgkmcnt(5)\n"
    "v_mfma_f32_16x16x32_bf16 v[4:7], a[92:95], a[60:63], v[4:7]\n"
    "v_exp_f32 v15, v15\n"
    "v_mfma_f32_16x16x32_bf16 v[8:11], a[96:99], a[0:3], v[16:19]\n"
    "ds_read_b128 a[84:87], v25 offset:18432\n"
    "v_cvt_pk_bf16_f32 v231, v14, v15\n"
    "v_mfma_f32_16x16x32_bf16 v[12:15], a[96:99], a[32:35], v[20:23]\n"
    "v_exp_f32 v0, v0\n"
    "v_mfma_f32_16x16x32_bf16 v[8:11], a[100:103], a[4:7], v[8:11]\n"
    "ds_read_b128 a[88:91], v24 offset:19456\n"
    "buffer_load_dwordx4 v37, s[16:19], s40 offen offset:3072 nt lds\n"
    "v_mfma_f32_16x16x32_bf16 v[12:15], a[100:103], a[36:39], v[12:15]\n"
    "v_exp_f32 v1, v1\n"
    "v_mfma_f32_16x16x32_bf16 v[8:11], a[104:107], a[8:11], v[8:11]\n"
    "ds_read_b128 a[92:95], v25 offset:19456\n"
    "v_cvt_pk_bf16_f32 v0, v0, v1\n"
    "v_accvgpr_write_b32 a176, v0\n"
    "v_mfma_f32_16x16x32_bf16 v[12:15], a[104:107], a[40:43], v[12:15]\n"
    "v_exp_f32 v2, v2\n"
    "v_mfma_f32_16x16x32_bf16 a[200:203], a[208:211], v[224:227], a[200:203]\n"
    "s_add_u32 m0, s28, 0x24000\n"
    "s_add_u32 s40, s30, 0x62000\n"
    "buffer_load_dwordx4 v34, s[16:19], s40 offen offset:0 nt lds\n"
    "v_mfma_f32_16x16x32_bf16 v[8:11], a[108:111], a[12:15], v[8:11]\n"
    "ds_read_b128 a[96:99], v24 offset:24576\n"
    "v_mfma_f32_16x16x32_bf16 v[12:15], a[108:111], a[44:47], v[12:15]\n"
    "v_exp_f32 v3, v3\n"
    "v_mfma_f32_16x16x32_bf16 v[8:11], a[112:115], a[16:19], v[8:11]\n"
    "ds_read_b128 a[100:103], v25 offset:24576\n"
    "v_cvt_pk_bf16_f32 v2, v2, v3\n"
    "v_accvgpr_write_b32 a177, v2\n"
    "v_mfma_f32_16x16x32_bf16 v[12:15], a[112:115], a[48:51], v[12:15]\n"
    "v_exp_f32 v4, v4\n"
    "v_mfma_f32_16x16x32_bf16 v[8:11], a[116:119], a[20:23], v[8:11]\n"
    "ds_read_b128 a[104:107], v24 offset:25600\n"
    "buffer_load_dwordx4 v35, s[16:19], s40 offen offset:1024 nt lds\n"
    "v_mfma_f32_16x16x32_bf16 v[12:15], a[116:119], a[52:55], v[12:15]\n"
    "v_exp_f32 v5, v5\n"
    "v_mfma_f32_16x16x32_bf16 v[8:11], a[120:123], a[24:27], v[8:11]\n"
    "ds_read_b128 a[108:111], v25 offset:25600\n"
    "v_cvt_pk_bf16_f32 v4, v4, v5\n"
    "v_accvgpr_write_b32 a180, v4\n"
    "v_mfma_f32_16x16x32_bf16 v[12:15], a[120:123], a[56:59], v[12:15]\n"
    "v_exp_f32 v6, v6\n"
    "v_mfma_f32_16x16x32_bf16 v[8:11], a[124:127], a[28:31], v[8:11]\n"
    "ds_read_b128 a[112:115], v24 offset:26624\n"
    "s_waitcnt lgkmcnt(5)\n"
    "v_mfma_f32_16x16x32_bf16 v[12:15], a[124:127], a[60:63], v[12:15]\n"
    "v_exp_f32 v7, v7\n"
    "v_mfma_f32_16x16x32_bf16 v[0:3], a[64:67], a[0:3], v[16:19]\n"
    "ds_read_b128 a[116:119], v25 offset:26624\n"
    "v_cvt_pk_bf16_f32 v6, v6, v7\n"
    "v_accvgpr_write_b32 a181, v6\n"
    "v_mfma_f32_16x16x32_bf16 v[4:7], a[64:67], a[32:35], v[20:23]\n"
    "v_exp_f32 v8, v8\n"
    "v_mfma_f32_16x16x32_bf16 v[0:3], a[68:71], a[4:7], v[0:3]\n"
    "ds_read_b128 a[120:123], v24 offset:27648\n"
    "buffer_load_dwordx4 v36, s[16:19], s40 offen offset:2048 nt lds\n"
    "v_mfma_f32_16x16x32_bf16 v[4:7], a[68:71], a[36:39], v[4:7]\n"
    "v_exp_f32 v9, v9\n"
    "v_mfma_f32_16x16x32_bf16 v[0:3], a[72:75], a[8:11], v[0:3]\n"
    "ds_read_b128 a[124:127], v25 offset:27648\n"
    "v_cvt_pk_bf16_f32 v8, v8, v9\n"
    "v_accvgpr_write_b32 a178, v8\n"
    "v_mfma_f32_16x16x32_bf16 v[4:7], a[72:75], a[40:43], v[4:7]\n"
    "v_exp_f32 v10, v10\n"
    "v_mfma_f32_16x16x32_bf16 v[0:3], a[76:79], a[12:15], v[0:3]\n"
    "v_mfma_f32_16x16x32_bf16 v[4:7], a[76:79], a[44:47], v[4:7]\n"
    "v_exp_f32 v11, v11\n"
    "v_mfma_f32_16x16x32_bf16 v[0:3], a[80:83], a[16:19], v[0:3]\n"
    "v_cvt_pk_bf16_f32 v10, v10, v11\n"
    "v_accvgpr_write_b32 a179, v10\n"
    "v_mfma_f32_16x16x32_bf16 v[4:7], a[80:83], a[48:51], v[4:7]\n"
    "v_exp_f32 v12, v12\n"
    "v_mfma_f32_16x16x32_bf16 v[0:3], a[84:87], a[20:23], v[0:3]\n"
    "buffer_load_dwordx4 v37, s[16:19], s40 offen offset:3072 nt lds\n"
    "v_mfma_f32_16x16x32_bf16 v[4:7], a[84:87], a[52:55], v[4:7]\n"
    "v_exp_f32 v13, v13\n"
    "v_mfma_f32_16x16x32_bf16 v[0:3], a[88:91], a[24:27], v[0:3]\n"
    "v_cvt_pk_bf16_f32 v12, v12, v13\n"
    "v_accvgpr_write_b32 a182, v12\n"
    "s_add_u32 m0, s28, 0x0\n"
    "v_mfma_f32_16x16x32_bf16 v[4:7], a[88:91], a[56:59], v[4:7]\n"
    "v_exp_f32 v14, v14\n"
    "v_mfma_f32_16x16x32_bf16 v[0:3], a[92:95], a[28:31], v[0:3]\n"
    "s_waitcnt lgkmcnt(0)\n"
    "s_add_u32 s40, s30, 0x80\n"
    "v_mfma_f32_16x16x32_bf16 v[4:7], a[92:95], a[60:63], v[4:7]\n"
    "v_exp_f32 v15, v15\n"
    "v_mfma_f32_16x16x32_bf16 v[8:11], a[96:99], a[0:3], v[16:19]\n"
    "v_cvt_pk_bf16_f32 v14, v14, v15\n"
    "v_accvgpr_write_b32 a183, v14\n"
    "v_mfma_f32_16x16x32_bf16 v[12:15], a[96:99], a[32:35], v[20:23]\n"
    "v_exp_f32 v0, v0\n"
    "v_mfma_f32_16x16x32_bf16 a[204:207], a[208:211], v[228:231], a[204:207]\n"
    "s_waitcnt vmcnt(28)\n"
    "s_barrier\n"
    "v_mfma_f32_16x16x32_bf16 v[8:11], a[100:103], a[4:7], v[8:11]\n"
    "ds_read_b64_tr_b16 a[64:65], v30 offset:32768\n"
    "v_mfma_f32_16x16x32_bf16 v[12:15], a[100:103], a[36:39], v[12:15]\n"
    "ds_read_b64_tr_b16 a[66:67], v30 offset:40960\n"
    "v_exp_f32 v1, v1\n"
    "v_mfma_f32_16x16x32_bf16 v[8:11], a[104:107], a[8:11], v[8:11]\n"
    "ds_read_b64_tr_b16 a[68:69], v31 offset:32768\n"
    "v_cvt_pk_bf16_f32 v0, v0, v1\n"
    "v_accvgpr_write_b32 a192, v0\n"
    "v_mfma_f32_16x16x32_bf16 v[12:15], a[104:107], a[40:43], v[12:15]\n"
    "ds_read_b64_tr_b16 a[70:71], v31 offset:40960\n"
    "v_exp_f32 v2, v2\n"
    "v_mfma_f32_16x16x32_bf16 a[200:203], a[208:211], a[176:179], a[200:203]\n"
    "buffer_load_dwordx4 v34, s[16:19], s40 offen offset:0 nt lds\n"
    "v_mfma_f32_16x16x32_bf16 v[8:11], a[108:111], a[12:15], v[8:11]\n"
    "ds_read_b64_tr_b16 a[72:73], v32 offset:32768\n"
    "v_mfma_f32_16x16x32_bf16 v[12:15], a[108:111], a[44:47], v[12:15]\n"
    "ds_read_b64_tr_b16 a[74:75], v32 offset:40960\n"
    "v_exp_f32 v3, v3\n"
    "v_mfma_f32_16x16x32_bf16 v[8:11], a[112:115], a[16:19], v[8:11]\n"
    "ds_read_b64_tr_b16 a[76:77], v33 offset:32768\n"
    "v_cvt_pk_bf16_f32 v2, v2, v3\n"
    "v_accvgpr_write_b32 a193, v2\n"
    "v_mfma_f32_16x16x32_bf16 v[12:15], a[112:115], a[48:51], v[12:15]\n"
    "ds_read_b64_tr_b16 a[78:79], v33 offset:40960\n"
    "v_exp_f32 v4, v4\n"
    "v_mfma_f32_16x16x32_bf16 v[8:11], a[116:119], a[20:23], v[8:11]\n"
    "ds_read_b64_tr_b16 a[80:81], v30 offset:33792\n"
    "buffer_load_dwordx4 v35, s[16:19], s40 offen offset:1024 nt lds\n"
    "v_mfma_f32_16x16x32_bf16 v[12:15], a[116:119], a[52:55], v[12:15]\n"
    "ds_read_b64_tr_b16 a[82:83], v30 offset:41984\n"
    "v_exp_f32 v5, v5\n"
    "v_mfma_f32_16x16x32_bf16 v[8:11], a[120:123], a[24:27], v[8:11]\n"
    "ds_read_b64_tr_b16 a[84:85], v31 offset:33792\n"
    "v_cvt_pk_bf16_f32 v4, v4, v5\n"
    "v_accvgpr_write_b32 a196, v4\n"
    "v_mfma_f32_16x16x32_bf16 v[12:15], a[120:123], a[56:59], v[12:15]\n"
    "ds_read_b64_tr_b16 a[86:87], v31 offset:41984\n"
    "v_exp_f32 v6, v6\n"
    "v_mfma_f32_16x16x32_bf16 v[8:11], a[124:127], a[28:31], v[8:11]\n"
    "ds_read_b64_tr_b16 a[88:89], v32 offset:33792\n"
    "v_mfma_f32_16x16x32_bf16 v[12:15], a[124:127], a[60:63], v[12:15]\n"
    "ds_read_b64_tr_b16 a[90:91], v32 offset:41984\n"
    "v_exp_f32 v7, v7\n"
    "s_waitcnt lgkmcnt(10)\n"
    "v_mfma_f32_16x16x32_bf16 a[128:131], a[64:67], v[40:43], 0\n"
    "ds_read_b64_tr_b16 a[92:93], v33 offset:33792\n"
    "v_cvt_pk_bf16_f32 v6, v6, v7\n"
    "v_accvgpr_write_b32 a197, v6\n"
    "v_mfma_f32_16x16x32_bf16 a[132:135], a[64:67], v[44:47], 0\n"
    "ds_read_b64_tr_b16 a[94:95], v33 offset:41984\n"
    "s_waitcnt lgkmcnt(10)\n"
    "v_mfma_f32_16x16x32_bf16 a[136:139], a[68:71], v[40:43], 0\n"
    "ds_read_b64_tr_b16 a[96:97], v30 offset:34816\n"
    "v_exp_f32 v8, v8\n"
    "v_mfma_f32_16x16x32_bf16 a[140:143], a[68:71], v[44:47], 0\n"
    "ds_read_b64_tr_b16 a[98:99], v30 offset:43008\n"
    "v_exp_f32 v9, v9\n"
    "v_mfma_f32_16x16x32_bf16 a[144:147], a[72:75], v[40:43], 0\n"
    "buffer_load_dwordx4 v36, s[16:19], s40 offen offset:2048 nt lds\n"
    "v_mfma_f32_16x16x32_bf16 a[148:151], a[72:75], v[44:47], 0\n"
    "v_exp_f32 v10, v10\n"
    "s_waitcnt lgkmcnt(8)\n"
    "v_mfma_f32_16x16x32_bf16 a[152:155], a[76:79], v[40:43], 0\n"
    "ds_read_b64_tr_b16 a[100:101], v31 offset:34816\n"
    "ds_read_b64_tr_b16 a[102:103], v31 offset:43008\n"
    "v_mfma_f32_16x16x32_bf16 a[156:159], a[76:79], v[44:47], 0\n"
    "ds_read_b64_tr_b16 a[104:105], v32 offset:34816\n"
    "ds_read_b64_tr_b16 a[106:107], v32 offset:43008\n"
    "v_mfma_f32_16x16x32_bf16 a[128:131], a[80:83], v[48:51], a[128:131]\n"
    "ds_read_b64_tr_b16 a[108:109], v33 offset:34816\n"
    "v_exp_f32 v11, v11\n"
    "v_mfma_f32_16x16x32_bf16 a[132:135], a[80:83], v[52:55], a[132:135]\n"
    "ds_read_b64_tr_b16 a[110:111], v33 offset:43008\n"
    "s_waitcnt lgkmcnt(10)\n"
    "v_mfma_f32_16x16x32_bf16 a[136:139], a[84:87], v[48:51], a[136:139]\n"
    "ds_read_b64_tr_b16 a[112:113], v30 offset:35840\n"
    "v_cvt_pk_bf16_f32 v8, v8, v9\n"
    "v_mfma_f32_16x16x32_bf16 a[140:143], a[84:87], v[52:55], a[140:143]\n"
    "ds_read_b64_tr_b16 a[114:115], v30 offset:44032\n"
    "v_accvgpr_write_b32 a194, v8\n"
    "v_mfma_f32_16x16x32_bf16 a[144:147], a[88:91], v[48:51], a[144:147]\n"
    "buffer_load_dwordx4 v37, s[16:19], s40 offen offset:3072 nt lds\n"
    "v_mfma_f32_16x16x32_bf16 a[148:151], a[88:91], v[52:55], a[148:151]\n"
    "s_add_u32 m0, s28, 0x4000\n"
    "s_waitcnt lgkmcnt(8)\n"
    "v_mfma_f32_16x16x32_bf16 a[152:155], a[92:95], v[48:51], a[152:155]\n"
    "ds_read_b64_tr_b16 a[116:117], v31 offset:35840\n"
    "ds_read_b64_tr_b16 a[118:119], v31 offset:44032\n"
    "v_mfma_f32_16x16x32_bf16 a[156:159], a[92:95], v[52:55], a[156:159]\n"
    "ds_read_b64_tr_b16 a[120:121], v32 offset:35840\n"
    "ds_read_b64_tr_b16 a[122:123], v32 offset:44032\n"
    "v_mfma_f32_16x16x32_bf16 a[128:131], a[96:99], v[56:59], a[128:131]\n"
    "ds_read_b64_tr_b16 a[124:125], v33 offset:35840\n"
    "s_waitcnt vmcnt(28) lgkmcnt(9)\n"
    "v_mfma_f32_16x16x32_bf16 a[132:135], a[96:99], v[60:63], a[132:135]\n"
    "ds_read_b64_tr_b16 a[126:127], v33 offset:44032\n"
    "s_barrier\n"
    "v_mfma_f32_16x16x32_bf16 a[136:139], a[100:103], v[56:59], a[136:139]\n"
    "ds_read_b64_tr_b16 a[64:65], v30 offset:49152\n"
    "s_add_u32 s40, s30, 0x20080\n"
    "v_mfma_f32_16x16x32_bf16 a[140:143], a[100:103], v[60:63], a[140:143]\n"
    "ds_read_b64_tr_b16 a[66:67], v30 offset:57344\n"
    "v_cvt_pk_bf16_f32 v10, v10, v11\n"
    "v_mfma_f32_16x16x32_bf16 a[144:147], a[104:107], v[56:59], a[144:147]\n"
    "buffer_load_dwordx4 v34, s[16:19], s40 offen offset:0 nt lds\n"
    "v_mfma_f32_16x16x32_bf16 a[148:151], a[104:107], v[60:63], a[148:151]\n"
    "v_accvgpr_write_b32 a195, v10\n"
    "s_waitcnt lgkmcnt(8)\n"
    "v_mfma_f32_16x16x32_bf16 a[152:155], a[108:111], v[56:59], a[152:155]\n"
    "ds_read_b64_tr_b16 a[68:69], v31 offset:49152\n"
    "ds_read_b64_tr_b16 a[70:71], v31 offset:57344\n"
    "v_mfma_f32_16x16x32_bf16 a[156:159], a[108:111], v[60:63], a[156:159]\n"
    "ds_read_b64_tr_b16 a[72:73], v32 offset:49152\n"
    "ds_read_b64_tr_b16 a[74:75], v32 offset:57344\n"
    "v_mfma_f32_16x16x32_bf16 a[128:131], a[112:115], v[64:67], a[128:131]\n"
    "ds_read_b64_tr_b16 a[76:77], v33 offset:49152\n"
    "v_exp_f32 v12, v12\n"
    "v_mfma_f32_16x16x32_bf16 a[132:135], a[112:115], v[68:71], a[132:135]\n"
    "ds_read_b64_tr_b16 a[78:79], v33 offset:57344\n"
    "s_waitcnt lgkmcnt(10)\n"
    "v_mfma_f32_16x16x32_bf16 a[136:139], a[116:119], v[64:67], a[136:139]\n"
    "ds_read_b64_tr_b16 a[80:81], v30 offset:50176\n"
    "v_exp_f32 v13, v13\n"
    "v_mfma_f32_16x16x32_bf16 a[140:143], a[116:119], v[68:71], a[140:143]\n"
    "ds_read_b64_tr_b16 a[82:83], v30 offset:58368\n"
    "v_exp_f32 v14, v14\n"
    "v_mfma_f32_16x16x32_bf16 a[144:147], a[120:123], v[64:67], a[144:147]\n"
    "buffer_load_dwordx4 v35, s[16:19], s40 offen offset:1024 nt lds\n"
    "v_mfma_f32_16x16x32_bf16 a[148:151], a[120:123], v[68:71], a[148:151]\n"
    "v_exp_f32 v15, v15\n"
    "s_waitcnt lgkmcnt(8)\n"
    "v_mfma_f32_16x16x32_bf16 a[152:155], a[124:127], v[64:67], a[152:155]\n"
    "ds_read_b64_tr_b16 a[84:85], v31 offset:50176\n"
    "ds_read_b64_tr_b16 a[86:87], v31 offset:58368\n"
    "v_mfma_f32_16x16x32_bf16 a[156:159], a[124:127], v[68:71], a[156:159]\n"
    "ds_read_b64_tr_b16 a[88:89], v32 offset:50176\n"
    "ds_read_b64_tr_b16 a[90:91], v32 offset:58368\n"
    "v_mfma_f32_16x16x32_bf16 a[128:131], a[64:67], v[72:75], a[128:131]\n"
    "ds_read_b64_tr_b16 a[92:93], v33 offset:50176\n"
    "v_cvt_pk_bf16_f32 v12, v12, v13\n"
    "v_mfma_f32_16x16x32_bf16 a[132:135], a[64:67], v[76:79], a[132:135]\n"
    "ds_read_b64_tr_b16 a[94:95], v33 offset:58368\n"
    "s_waitcnt lgkmcnt(10)\n"
    "v_mfma_f32_16x16x32_bf16 a[136:139], a[68:71], v[72:75], a[136:139]\n"
    "ds_read_b64_tr_b16 a[96:97], v30 offset:51200\n"
    "v_accvgpr_write_b32 a198, v12\n"
    "v_mfma_f32_16x16x32_bf16 a[140:143], a[68:71], v[76:79], a[140:143]\n"
    "ds_read_b64_tr_b16 a[98:99], v30 offset:59392\n"
    "v_cvt_pk_bf16_f32 v14, v14, v15\n"
    "v_mfma_f32_16x16x32_bf16 a[144:147], a[72:75], v[72:75], a[144:147]\n"
    "buffer_load_dwordx4 v36, s[16:19], s40 offen offset:2048 nt lds\n"
    "v_mfma_f32_16x16x32_bf16 a[148:151], a[72:75], v[76:79], a[148:151]\n"
    "v_accvgpr_write_b32 a199, v14\n"
    "s_waitcnt lgkmcnt(8)\n"
    "STUB5_%=:\n"
    "s_cbranch_execz STUB9_%=\n"
    "v_mfma_f32_16x16x32_bf16 a[152:155], a[76:79], v[72:75], a[152:155]\n"
    "ds_read_b64_tr_b16 a[100:101], v31 offset:51200\n"
    "ds_read_b64_tr_b16 a[102:103], v31 offset:59392\n"
    "v_mfma_f32_16x16x32_bf16 a[156:159], a[76:79], v[76:79], a[156:159]\n"
    "ds_read_b64_tr_b16 a[104:105], v32 offset:51200\n"
    "ds_read_b64_tr_b16 a[106:107], v32 offset:59392\n"
    "v_mfma_f32_16x16x32_bf16 a[128:131], a[80:83], v[80:83], a[128:131]\n"
    "ds_read_b64_tr_b16 a[108:109], v33 offset:51200\n"
    "v_mfma_f32_16x16x32_bf16 a[204:207], a[208:211], a[180:183], a[204:207]\n"
    "v_mfma_f32_16x16x32_bf16 a[132:135], a[80:83], v[84:87], a[132:135]\n"
    "ds_read_b64_tr_b16 a[110:111], v33 offset:59392\n"
    "s_waitcnt lgkmcnt(10)\n"
    "v_mfma_f32_16x16x32_bf16 a[136:139], a[84:87], v[80:83], a[136:139]\n"
    "ds_read_b64_tr_b16 a[112:113], v30 offset:52224\n"
    "v_mfma_f32_16x16x32_bf16 a[200:203], a[208:211], a[192:195], a[200:203]\n"
    "v_mfma_f32_16x16x32_bf16 a[140:143], a[84:87], v[84:87], a[140:143]\n"
    "ds_read_b64_tr_b16 a[114:115], v30 offset:60416\n"
    "v_mfma_f32_16x16x32_bf16 a[204:207], a[208:211], a[196:199], a[204:207]\n"
    "v_mfma_f32_16x16x32_bf16 a[144:147], a[88:91], v[80:83], a[144:147]\n"
    "buffer_load_dwordx4 v37, s[16:19], s40 offen offset:3072 nt lds\n"
    "v_mfma_f32_16x16x32_bf16 a[148:151], a[88:91], v[84:87], a[148:151]\n"
    "v_add_u32 v30, 0x10000, v30\n"
    "s_waitcnt lgkmcnt(8)\n"
    "v_mfma_f32_16x16x32_bf16 a[152:155], a[92:95], v[80:83], a[152:155]\n"
    "ds_read_b64_tr_b16 a[116:117], v31 offset:52224\n"
    "ds_read_b64_tr_b16 a[118:119], v31 offset:60416\n"
    "v_mfma_f32_16x16x32_bf16 a[156:159], a[92:95], v[84:87], a[156:159]\n"
    "ds_read_b64_tr_b16 a[120:121], v32 offset:52224\n"
    "ds_read_b64_tr_b16 a[122:123], v32 offset:60416\n"
    "v_mfma_f32_16x16x32_bf16 a[128:131], a[96:99], v[88:91], a[128:131]\n"
    "ds_read_b64_tr_b16 a[124:125], v33 offset:52224\n"
    "s_waitcnt vmcnt(28) lgkmcnt(9)\n"
    "v_mfma_f32_16x16x32_bf16 a[132:135], a[96:99], v[92:95], a[132:135]\n"
    "ds_read_b64_tr_b16 a[126:127], v33 offset:60416\n"
    "s_barrier\n"
    "v_mfma_f32_16x16x32_bf16 a[136:139], a[100:103], v[88:91], a[136:139]\n"
    "ds_read_b64_tr_b16 a[64:65], v30 offset:0\n"
    "v_add_u32 v31, 0x10000, v31\n"
    "v_mfma_f32_16x16x32_bf16 a[140:143], a[100:103], v[92:95], a[140:143]\n"
    "ds_read_b64_tr_b16 a[66:67], v30 offset:8192\n"
    "v_add_u32 v32, 0x10000, v32\n"
    "v_mfma_f32_16x16x32_bf16 a[144:147], a[104:107], v[88:91], a[144:147]\n"
    "s_add_u32 m0, s28, 0x8000\n"
    "s_add_u32 s40, s30, 0x2080\n"
    "buffer_load_dwordx4 v34, s[16:19], s40 offen offset:0 nt lds\n"
    "v_mfma_f32_16x16x32_bf16 a[148:151], a[104:107], v[92:95], a[148:151]\n"
    "v_add_u32 v33, 0x10000, v33\n"
    "s_waitcnt lgkmcnt(8)\n"
    "v_mfma_f32_16x16x32_bf16 a[152:155], a[108:111], v[88:91], a[152:155]\n"
    "ds_read_b64_tr_b16 a[68:69], v31 offset:0\n"
    "ds_read_b64_tr_b16 a[70:71], v31 offset:8192\n"
    "v_mfma_f32_16x16x32_bf16 a[156:159], a[108:111], v[92:95], a[156:159]\n"
    "ds_read_b64_tr_b16 a[72:73], v32 offset:0\n"
    "ds_read_b64_tr_b16 a[74:75], v32 offset:8192\n"
    "v_mfma_f32_16x16x32_bf16 a[128:131], a[112:115], v[96:99], a[128:131]\n"
    "ds_read_b64_tr_b16 a[76:77], v33 offset:0\n"
    "v_mov_b32 v17, v20\n"
    "v_mfma_f32_16x16x32_bf16 a[132:135], a[112:115], v[100:103], a[132:135]\n"
    "ds_read_b64_tr_b16 a[78:79], v33 offset:8192\n"
    "s_waitcnt lgkmcnt(10)\n"
    "v_mfma_f32_16x16x32_bf16 a[136:139], a[116:119], v[96:99], a[136:139]\n"
    "ds_read_b64_tr_b16 a[80:81], v30 offset:1024\n"
    "v_mov_b32 v1, s34\n"
    "v_mfma_f32_16x16x32_bf16 a[140:143], a[116:119], v[100:103], a[140:143]\n"
    "ds_read_b64_tr_b16 a[82:83], v30 offset:9216\n"
    "v_and_b32 v2, -16, v1\n"
    "v_mfma_f32_16x16x32_bf16 a[144:147], a[120:123], v[96:99], a[144:147]\n"
    "buffer_load_dwordx4 v35, s[16:19], s40 offen offset:1024 nt lds\n"
    "v_mfma_f32_16x16x32_bf16 a[148:151], a[120:123], v[100:103], a[148:151]\n"
    "s_waitcnt lgkmcnt(8)\n"
    "v_mfma_f32_16x16x32_bf16 a[152:155], a[124:127], v[96:99], a[152:155]\n"
    "ds_read_b64_tr_b16 a[84:85], v31 offset:1024\n"
    "ds_read_b64_tr_b16 a[86:87], v31 offset:9216\n"
    "v_mfma_f32_16x16x32_bf16 a[156:159], a[124:127], v[100:103], a[156:159]\n"
    "ds_read_b64_tr_b16 a[88:89], v32 offset:1024\n"
    "ds_read_b64_tr_b16 a[90:91], v32 offset:9216\n"
    "v_mfma_f32_16x16x32_bf16 a[128:131], a[64:67], v[104:107], a[128:131]\n"
    "ds_read_b64_tr_b16 a[92:93], v33 offset:1024\n"
    "v_accvgpr_read_b32 v18, a200\n"
    "v_mfma_f32_16x16x32_bf16 a[132:135], a[64:67], v[108:111], a[132:135]\n"
    "ds_read_b64_tr_b16 a[94:95], v33 offset:9216\n"
    "s_waitcnt lgkmcnt(10)\n"
    "v_mfma_f32_16x16x32_bf16 a[136:139], a[68:71], v[104:107], a[136:139]\n"
    "ds_read_b64_tr_b16 a[96:97], v30 offset:2048\n"
    "v_accvgpr_read_b32 v19, a204\n"
    "v_mfma_f32_16x16x32_bf16 a[140:143], a[68:71], v[108:111], a[140:143]\n"
    "ds_read_b64_tr_b16 a[98:99], v30 offset:10240\n"
    "v_cmp_nge_f32 vcc, s47, v18\n"
    "v_mfma_f32_16x16x32_bf16 a[144:147], a[72:75], v[104:107], a[144:147]\n"
    "buffer_load_dwordx4 v36, s[16:19], s40 offen offset:2048 nt lds\n"
    "v_mfma_f32_16x16x32_bf16 a[148:151], a[72:75], v[108:111], a[148:151]\n"
    "v_cmp_nge_f32 s[48:49], s47, v19\n"
    "s_waitcnt lgkmcnt(8)\n"
    "v_mfma_f32_16x16x32_bf16 a[152:155], a[76:79], v[104:107], a[152:155]\n"
    "ds_read_b64_tr_b16 a[100:101], v31 offset:2048\n"
    "ds_read_b64_tr_b16 a[102:103], v31 offset:10240\n"
    "v_mfma_f32_16x16x32_bf16 a[156:159], a[76:79], v[108:111], a[156:159]\n"
    "ds_read_b64_tr_b16 a[104:105], v32 offset:2048\n"
    "ds_read_b64_tr_b16 a[106:107], v32 offset:10240\n"
    "v_mfma_f32_16x16x32_bf16 a[128:131], a[80:83], v[112:115], a[128:131]\n"
    "ds_read_b64_tr_b16 a[108:109], v33 offset:2048\n"
    "s_or_b64 vcc, vcc, s[48:49]\n"
    "s_cselect_b32 s50, 1, 0\n"
    "v_mfma_f32_16x16x32_bf16 a[132:135], a[80:83], v[116:119], a[132:135]\n"
    "ds_read_b64_tr_b16 a[110:111], v33 offset:10240\n"
    "s_waitcnt lgkmcnt(10)\n"
    "v_mfma_f32_16x16x32_bf16 a[136:139], a[84:87], v[112:115], a[136:139]\n"
    "ds_read_b64_tr_b16 a[112:113], v30 offset:3072\n"
    "s_andn2_b32 s50, s50, s41\n"
    "v_mfma_f32_16x16x32_bf16 a[140:143], a[84:87], v[116:119], a[140:143]\n"
    "ds_read_b64_tr_b16 a[114:115], v30 offset:11264\n"
    "v_mov_b32 v0, s50\n"
    "v_mfma_f32_16x16x32_bf16 a[144:147], a[88:91], v[112:115], a[144:147]\n"
    "buffer_load_dwordx4 v37, s[16:19], s40 offen offset:3072 nt lds\n"
    "v_mfma_f32_16x16x32_bf16 a[148:151], a[88:91], v[116:119], a[148:151]\n"
    "s_add_u32 m0, s28, 0xc000\n"
    "s_waitcnt lgkmcnt(8)\n"
    "v_mfma_f32_16x16x32_bf16 a[152:155], a[92:95], v[112:115], a[152:155]\n"
    "ds_read_b64_tr_b16 a[116:117], v31 offset:3072\n"
    "ds_read_b64_tr_b16 a[118:119], v31 offset:11264\n"
    "v_mfma_f32_16x16x32_bf16 a[156:159], a[92:95], v[116:119], a[156:159]\n"
    "ds_read_b64_tr_b16 a[120:121], v32 offset:3072\n"
    "ds_read_b64_tr_b16 a[122:123], v32 offset:11264\n"
    "v_mfma_f32_16x16x32_bf16 a[128:131], a[96:99], v[120:123], a[128:131]\n"
    "ds_read_b64_tr_b16 a[124:125], v33 offset:3072\n"
    "s_waitcnt vmcnt(28) lgkmcnt(9)\n"
    "v_mfma_f32_16x16x32_bf16 a[132:135], a[96:99], v[124:127], a[132:135]\n"
    "ds_read_b64_tr_b16 a[126:127], v33 offset:11264\n"
    "s_barrier\n"
    "v_mfma_f32_16x16x32_bf16 a[136:139], a[100:103], v[120:123], a[136:139]\n"
    "ds_read_b64_tr_b16 a[64:65], v30 offset:16384\n"
    "s_add_u32 s40, s30, 0x22080\n"
    "v_mfma_f32_16x16x32_bf16 a[140:143], a[100:103], v[124:127], a[140:143]\n"
    "ds_read_b64_tr_b16 a[66:67], v30 offset:24576\n"
    "ds_write_b32 v1, v0\n"
    "v_mfma_f32_16x16x32_bf16 a[144:147], a[104:107], v[120:123], a[144:147]\n"
    "buffer_load_dwordx4 v34, s[16:19], s40 offen offset:0 nt lds\n"
    "v_mfma_f32_16x16x32_bf16 a[148:151], a[104:107], v[124:127], a[148:151]\n"
    "v_lshrrev_b32 v0, 3, v39\n"
    "s_waitcnt lgkmcnt(9)\n"
    "v_mfma_f32_16x16x32_bf16 a[152:155], a[108:111], v[120:123], a[152:155]\n"
    "ds_read_b64_tr_b16 a[68:69], v31 offset:16384\n"
    "ds_read_b64_tr_b16 a[70:71], v31 offset:24576\n"
    "v_mfma_f32_16x16x32_bf16 a[156:159], a[108:111], v[124:127], a[156:159]\n"
    "ds_read_b64_tr_b16 a[72:73], v32 offset:16384\n"
    "ds_read_b64_tr_b16 a[74:75], v32 offset:24576\n"
    "v_mfma_f32_16x16x32_bf16 a[128:131], a[112:115], v[128:131], a[128:131]\n"
    "ds_read_b64_tr_b16 a[76:77], v33 offset:16384\n"
    "v_and_b32 v1, 7, v39\n"
    "v_mfma_f32_16x16x32_bf16 a[132:135], a[112:115], v[132:135], a[132:135]\n"
    "ds_read_b64_tr_b16 a[78:79], v33 offset:24576\n"
    "s_waitcnt lgkmcnt(11)\n"
    "v_mfma_f32_16x16x32_bf16 a[136:139], a[116:119], v[128:131], a[136:139]\n"
    "ds_read_b64_tr_b16 a[80:81], v30 offset:17408\n"
    "v_lshlrev_b32 v1, 4, v1\n"
    "v_mfma_f32_16x16x32_bf16 a[140:143], a[116:119], v[132:135], a[140:143]\n"
    "ds_read_b64_tr_b16 a[82:83], v30 offset:25600\n"
    "v_lshl_add_u32 v24, v0, 11, v1\n"
    "v_mfma_f32_16x16x32_bf16 a[144:147], a[120:123], v[128:131], a[144:147]\n"
    "buffer_load_dwordx4 v35, s[16:19], s40 offen offset:1024 nt lds\n"
    "v_mfma_f32_16x16x32_bf16 a[148:151], a[120:123], v[132:135], a[148:151]\n"
    "v_lshl_add_u32 v25, v0, 11, v1\n"
    "s_waitcnt lgkmcnt(9)\n"
    "v_mfma_f32_16x16x32_bf16 a[152:155], a[124:127], v[128:131], a[152:155]\n"
    "ds_read_b64_tr_b16 a[84:85], v31 offset:17408\n"
    "ds_read_b64_tr_b16 a[86:87], v31 offset:25600\n"
    "v_mfma_f32_16x16x32_bf16 a[156:159], a[124:127], v[132:135], a[156:159]\n"
    "ds_read_b64_tr_b16 a[88:89], v32 offset:17408\n"
    "ds_read_b64_tr_b16 a[90:91], v32 offset:25600\n"
    "v_mfma_f32_16x16x32_bf16 a[128:131], a[64:67], v[136:139], a[128:131]\n"
    "ds_read_b64_tr_b16 a[92:93], v33 offset:17408\n"
    "v_and_b32 v0, 15, v39\n"
    "v_mfma_f32_16x16x32_bf16 a[132:135], a[64:67], v[140:143], a[132:135]\n"
    "ds_read_b64_tr_b16 a[94:95], v33 offset:25600\n"
    "s_waitcnt lgkmcnt(10)\n"
    "v_mfma_f32_16x16x32_bf16 a[136:139], a[68:71], v[136:139], a[136:139]\n"
    "ds_read_b64_tr_b16 a[96:97], v30 offset:18432\n"
    "v_lshlrev_b32 v26, 4, v0\n"
    "v_mfma_f32_16x16x32_bf16 a[140:143], a[68:71], v[140:143], a[140:143]\n"
    "ds_read_b64_tr_b16 a[98:99], v30 offset:26624\n"
    "v_lshlrev_b32 v27, 2, v39\n"
    "v_mfma_f32_16x16x32_bf16 a[144:147], a[72:75], v[136:139], a[144:147]\n"
    "buffer_load_dwordx4 v36, s[16:19], s40 offen offset:2048 nt lds\n"
    "v_mfma_f32_16x16x32_bf16 a[148:151], a[72:75], v[140:143], a[148:151]\n"
    "s_add_u32 m0, s28, 0x10000\n"
    "s_waitcnt lgkmcnt(8)\n"
    "v_mfma_f32_16x16x32_bf16 a[152:155], a[76:79], v[136:139], a[152:155]\n"
    "ds_read_b64_tr_b16 a[100:101], v31 offset:18432\n"
    "ds_read_b64_tr_b16 a[102:103], v31 offset:26624\n"
    "v_mfma_f32_16x16x32_bf16 a[156:159], a[76:79], v[140:143], a[156:159]\n"
    "ds_read_b64_tr_b16 a[104:105], v32 offset:18432\n"
    "ds_read_b64_tr_b16 a[106:107], v32 offset:26624\n"
    "v_mfma_f32_16x16x32_bf16 a[128:131], a[80:83], v[144:147], a[128:131]\n"
    "ds_read_b64_tr_b16 a[108:109], v33 offset:18432\n"
    "s_add_u32 s40, s30, 0x40080\n"
    "v_mfma_f32_16x16x32_bf16 a[132:135], a[80:83], v[148:151], a[132:135]\n"
    "ds_read_b64_tr_b16 a[110:111], v33 offset:26624\n"
    "s_waitcnt lgkmcnt(10)\n"
    "v_mfma_f32_16x16x32_bf16 a[136:139], a[84:87], v[144:147], a[136:139]\n"
    "ds_read_b64_tr_b16 a[112:113], v30 offset:19456\n"
    "v_mfma_f32_16x16x32_bf16 a[140:143], a[84:87], v[148:151], a[140:143]\n"
    "ds_read_b64_tr_b16 a[114:115], v30 offset:27648\n"
    "v_mfma_f32_16x16x32_bf16 a[144:147], a[88:91], v[144:147], a[144:147]\n"
    "ds_read_b64_tr_b16 a[116:117], v31 offset:19456\n"
    "v_mfma_f32_16x16x32_bf16 a[148:151], a[88:91], v[148:151], a[148:151]\n"
    "ds_read_b64_tr_b16 a[118:119], v31 offset:27648\n"
    "s_waitcnt lgkmcnt(10)\n"
    "v_mfma_f32_16x16x32_bf16 a[152:155], a[92:95], v[144:147], a[152:155]\n"
    "ds_read_b64_tr_b16 a[120:121], v32 offset:19456\n"
    "v_mfma_f32_16x16x32_bf16 a[156:159], a[92:95], v[148:151], a[156:159]\n"
    "ds_read_b64_tr_b16 a[122:123], v32 offset:27648\n"
    "v_mfma_f32_16x16x32_bf16 a[128:131], a[96:99], v[152:155], a[128:131]\n"
    "ds_read_b64_tr_b16 a[124:125], v33 offset:19456\n"
    "s_waitcnt vmcnt(27) lgkmcnt(9)\n"
    "v_mfma_f32_16x16x32_bf16 a[132:135], a[96:99], v[156:159], a[132:135]\n"
    "ds_read_b64_tr_b16 a[126:127], v33 offset:27648\n"
    "s_barrier\n"
    "v_mfma_f32_16x16x32_bf16 a[136:139], a[100:103], v[152:155], a[136:139]\n"
    "ds_read_b64_tr_b16 a[64:65], v30 offset:32768\n"
    "ds_read_b128 v[4:7], v2\n"
    "v_mfma_f32_16x16x32_bf16 a[140:143], a[100:103], v[156:159], a[140:143]\n"
    "ds_read_b64_tr_b16 a[66:67], v30 offset:40960\n"
    "v_mfma_f32_16x16x32_bf16 a[144:147], a[104:107], v[152:155], a[144:147]\n"
    "buffer_load_dwordx4 v34, s[16:19], s40 offen offset:0 nt lds\n"
    "v_mfma_f32_16x16x32_bf16 a[148:151], a[104:107], v[156:159], a[148:151]\n"
    "s_waitcnt lgkmcnt(9)\n"
    "v_mfma_f32_16x16x32_bf16 a[152:155], a[108:111], v[152:155], a[152:155]\n"
    "ds_read_b64_tr_b16 a[68:69], v31 offset:32768\n"
    "ds_read_b64_tr_b16 a[70:71], v31 offset:40960\n"
    "v_mfma_f32_16x16x32_bf16 a[156:159], a[108:111], v[156:159], a[156:159]\n"
    "ds_read_b64_tr_b16 a[72:73], v32 offset:32768\n"
    "ds_read_b64_tr_b16 a[74:75], v32 offset:40960\n"
    "v_mfma_f32_16x16x32_bf16 a[128:131], a[112:115], v[160:163], a[128:131]\n"
    "ds_read_b64_tr_b16 a[76:77], v33 offset:32768\n"
    "v_mfma_f32_16x16x32_bf16 a[132:135], a[112:115], v[164:167], a[132:135]\n"
    "ds_read_b64_tr_b16 a[78:79], v33 offset:40960\n"
    "s_waitcnt lgkmcnt(11)\n"
    "v_mfma_f32_16x16x32_bf16 a[136:139], a[116:119], v[160:163], a[136:139]\n"
    "ds_read_b64_tr_b16 a[80:81], v30 offset:33792\n"
    "v_mfma_f32_16x16x32_bf16 a[140:143], a[116:119], v[164:167], a[140:143]\n"
    "ds_read_b64_tr_b16 a[82:83], v30 offset:41984\n"
    "v_mfma_f32_16x16x32_bf16 a[144:147], a[120:123], v[160:163], a[144:147]\n"
    "buffer_load_dwordx4 v35, s[16:19], s40 offen offset:1024 nt lds\n"
    "v_mfma_f32_16x16x32_bf16 a[148:151], a[120:123], v[164:167], a[148:151]\n"
    "s_waitcnt lgkmcnt(8)\n"
    "v_mfma_f32_16x16x32_bf16 a[152:155], a[124:127], v[160:163], a[152:155]\n"
    "ds_read_b64_tr_b16 a[84:85], v31 offset:33792\n"
    "ds_read_b64_tr_b16 a[86:87], v31 offset:41984\n"
    "v_mfma_f32_16x16x32_bf16 a[156:159], a[124:127], v[164:167], a[156:159]\n"
    "ds_read_b64_tr_b16 a[88:89], v32 offset:33792\n"
    "ds_read_b64_tr_b16 a[90:91], v32 offset:41984\n"
    "v_mfma_f32_16x16x32_bf16 a[128:131], a[64:67], v[168:171], a[128:131]\n"
    "ds_read_b64_tr_b16 a[92:93], v33 offset:33792\n"
    "v_or3_b32 v4, v4, v5, v6\n"
    "v_mfma_f32_16x16x32_bf16 a[132:135], a[64:67], v[172:175], a[132:135]\n"
    "ds_read_b64_tr_b16 a[94:95], v33 offset:41984\n"
    "s_waitcnt lgkmcnt(10)\n"
    "v_mfma_f32_16x16x32_bf16 a[136:139], a[68:71], v[168:171], a[136:139]\n"
    "ds_read_b64_tr_b16 a[96:97], v30 offset:34816\n"
    "v_or_b32 v4, v4, v7\n"
    "v_mfma_f32_16x16x32_bf16 a[140:143], a[68:71], v[172:175], a[140:143]\n"
    "ds_read_b64_tr_b16 a[98:99], v30 offset:43008\n"
    "v_readfirstlane_b32 s50, v4\n"
    "v_mfma_f32_16x16x32_bf16 a[144:147], a[72:75], v[168:171], a[144:147]\n"
    "buffer_load_dwordx4 v36, s[16:19], s40 offen offset:2048 nt lds\n"
    "v_mfma_f32_16x16x32_bf16 a[148:151], a[72:75], v[172:175], a[148:151]\n"
    "s_waitcnt lgkmcnt(8)\n"
    "v_mfma_f32_16x16x32_bf16 a[152:155], a[76:79], v[168:171], a[152:155]\n"
    "ds_read_b64_tr_b16 a[100:101], v31 offset:34816\n"
    "ds_read_b64_tr_b16 a[102:103], v31 offset:43008\n"
    "v_mfma_f32_16x16x32_bf16 a[156:159], a[76:79], v[172:175], a[156:159]\n"
    "ds_read_b64_tr_b16 a[104:105], v32 offset:34816\n"
    "ds_read_b64_tr_b16 a[106:107], v32 offset:43008\n"
    "v_mfma_f32_16x16x32_bf16 a[128:131], a[80:83], v[176:179], a[128:131]\n"
    "ds_read_b64_tr_b16 a[108:109], v33 offset:34816\n"
    "s_cmp_lg_u32 s50, 0\n"
    "s_cbranch_scc1 FALLBACK_%=\n"
    "v_mfma_f32_16x16x32_bf16 a[132:135], a[80:83], v[180:183], a[132:135]\n"
    "ds_read_b64_tr_b16 a[110:111], v33 offset:43008\n"
    "s_waitcnt lgkmcnt(10)\n"
    "v_mfma_f32_16x16x32_bf16 a[136:139], a[84:87], v[176:179], a[136:139]\n"
    "ds_read_b64_tr_b16 a[112:113], v30 offset:35840\n"
    "buffer_store_dwordx4 v[16:19], v26, s[24:27], s7 offen sc1\n"
    "v_mfma_f32_16x16x32_bf16 a[140:143], a[84:87], v[180:183], a[140:143]\n"
    "ds_read_b64_tr_b16 a[114:115], v30 offset:44032\n"
    "v_mfma_f32_16x16x32_bf16 a[144:147], a[88:91], v[176:179], a[144:147]\n"
    "buffer_load_dwordx4 v37, s[16:19], s40 offen offset:3072 nt lds\n"
    "v_mfma_f32_16x16x32_bf16 a[148:151], a[88:91], v[180:183], a[148:151]\n"
    "s_add_u32 m0, s28, 0x14000\n"
    "s_waitcnt lgkmcnt(8)\n"
    "v_mfma_f32_16x16x32_bf16 a[152:155], a[92:95], v[176:179], a[152:155]\n"
    "ds_read_b64_tr_b16 a[116:117], v31 offset:35840\n"
    "ds_read_b64_tr_b16 a[118:119], v31 offset:44032\n"
    "v_mfma_f32_16x16x32_bf16 a[156:159], a[92:95], v[180:183], a[156:159]\n"
    "ds_read_b64_tr_b16 a[120:121], v32 offset:35840\n"
    "ds_read_b64_tr_b16 a[122:123], v32 offset:44032\n"
    "v_mfma_f32_16x16x32_bf16 a[128:131], a[96:99], v[184:187], a[128:131]\n"
    "ds_read_b64_tr_b16 a[124:125], v33 offset:35840\n"
    "s_waitcnt vmcnt(28) lgkmcnt(9)\n"
    "v_mfma_f32_16x16x32_bf16 a[132:135], a[96:99], v[188:191], a[132:135]\n"
    "ds_read_b64_tr_b16 a[126:127], v33 offset:44032\n"
    "s_barrier\n"
    "v_mfma_f32_16x16x32_bf16 a[136:139], a[100:103], v[184:187], a[136:139]\n"
    "ds_read_b64_tr_b16 a[64:65], v30 offset:49152\n"
    "s_add_u32 s40, s30, 0x60080\n"
    "v_mfma_f32_16x16x32_bf16 a[140:143], a[100:103], v[188:191], a[140:143]\n"
    "ds_read_b64_tr_b16 a[66:67], v30 offset:57344\n"
    "v_mfma_f32_16x16x32_bf16 a[144:147], a[104:107], v[184:187], a[144:147]\n"
    "buffer_load_dwordx4 v34, s[16:19], s40 offen offset:0 nt lds\n"
    "v_mfma_f32_16x16x32_bf16 a[148:151], a[104:107], v[188:191], a[148:151]\n"
    "s_waitcnt lgkmcnt(8)\n"
    "v_mfma_f32_16x16x32_bf16 a[152:155], a[108:111], v[184:187], a[152:155]\n"
    "ds_read_b64_tr_b16 a[68:69], v31 offset:49152\n"
    "ds_read_b64_tr_b16 a[70:71], v31 offset:57344\n"
    "v_mfma_f32_16x16x32_bf16 a[156:159], a[108:111], v[188:191], a[156:159]\n"
    "ds_read_b64_tr_b16 a[72:73], v32 offset:49152\n"
    "ds_read_b64_tr_b16 a[74:75], v32 offset:57344\n"
    "v_mfma_f32_16x16x32_bf16 a[128:131], a[112:115], v[192:195], a[128:131]\n"
    "ds_read_b64_tr_b16 a[76:77], v33 offset:49152\n"
    "v_mfma_f32_16x16x32_bf16 a[132:135], a[112:115], v[196:199], a[132:135]\n"
    "ds_read_b64_tr_b16 a[78:79], v33 offset:57344\n"
    "s_waitcnt lgkmcnt(10)\n"
    "v_mfma_f32_16x16x32_bf16 a[136:139], a[116:119], v[192:195], a[136:139]\n"
    "ds_read_b64_tr_b16 a[80:81], v30 offset:50176\n"
    "v_mfma_f32_16x16x32_bf16 a[140:143], a[116:119], v[196:199], a[140:143]\n"
    "ds_read_b64_tr_b16 a[82:83], v30 offset:58368\n"
    "v_mfma_f32_16x16x32_bf16 a[144:147], a[120:123], v[192:195], a[144:147]\n"
    "buffer_load_dwordx4 v35, s[16:19], s40 offen offset:1024 nt lds\n"
    "v_mfma_f32_16x16x32_bf16 a[148:151], a[120:123], v[196:199], a[148:151]\n"
    "buffer_load_dword a214, v27, s[24:27], s7 offen offset:1024 sc1\n"
    "s_waitcnt lgkmcnt(8)\n"
    "v_mfma_f32_16x16x32_bf16 a[152:155], a[124:127], v[192:195], a[152:155]\n"
    "ds_read_b64_tr_b16 a[84:85], v31 offset:50176\n"
    "ds_read_b64_tr_b16 a[86:87], v31 offset:58368\n"
    "v_mfma_f32_16x16x32_bf16 a[156:159], a[124:127], v[196:199], a[156:159]\n"
    "ds_read_b64_tr_b16 a[88:89], v32 offset:50176\n"
    "ds_read_b64_tr_b16 a[90:91], v32 offset:58368\n"
    "v_mfma_f32_16x16x32_bf16 a[128:131], a[64:67], v[200:203], a[128:131]\n"
    "ds_read_b64_tr_b16 a[92:93], v33 offset:50176\n"
    "v_mfma_f32_16x16x32_bf16 a[132:135], a[64:67], v[204:207], a[132:135]\n"
    "ds_read_b64_tr_b16 a[94:95], v33 offset:58368\n"
    "s_waitcnt lgkmcnt(10)\n"
    "v_mfma_f32_16x16x32_bf16 a[136:139], a[68:71], v[200:203], a[136:139]\n"
    "ds_read_b64_tr_b16 a[96:97], v30 offset:51200\n"
    "v_mfma_f32_16x16x32_bf16 a[140:143], a[68:71], v[204:207], a[140:143]\n"
    "ds_read_b64_tr_b16 a[98:99], v30 offset:59392\n"
    "v_mfma_f32_16x16x32_bf16 a[144:147], a[72:75], v[200:203], a[144:147]\n"
    "buffer_load_dwordx4 v36, s[16:19], s40 offen offset:2048 nt lds\n"
    "v_mfma_f32_16x16x32_bf16 a[148:151], a[72:75], v[204:207], a[148:151]\n"
    "s_waitcnt lgkmcnt(8)\n"
    "v_mfma_f32_16x16x32_bf16 a[152:155], a[76:79], v[200:203], a[152:155]\n"
    "ds_read_b64_tr_b16 a[100:101], v31 offset:51200\n"
    "ds_read_b64_tr_b16 a[102:103], v31 offset:59392\n"
    "v_mfma_f32_16x16x32_bf16 a[156:159], a[76:79], v[204:207], a[156:159]\n"
    "ds_read_b64_tr_b16 a[104:105], v32 offset:51200\n"
    "ds_read_b64_tr_b16 a[106:107], v32 offset:59392\n"
    "v_mfma_f32_16x16x32_bf16 a[128:131], a[80:83], v[208:211], a[128:131]\n"
    "ds_read_b64_tr_b16 a[108:109], v33 offset:51200\n"
    "v_mfma_f32_16x16x32_bf16 a[132:135], a[80:83], v[212:215], a[132:135]\n"
    "ds_read_b64_tr_b16 a[110:111], v33 offset:59392\n"
    "s_waitcnt lgkmcnt(10)\n"
    "v_mfma_f32_16x16x32_bf16 a[136:139], a[84:87], v[208:211], a[136:139]\n"
    "ds_read_b64_tr_b16 a[112:113], v30 offset:52224\n"
    "v_mfma_f32_16x16x32_bf16 a[140:143], a[84:87], v[212:215], a[140:143]\n"
    "ds_read_b64_tr_b16 a[114:115], v30 offset:60416\n"
    "buffer_load_dword a215, v27, s[24:27], s7 offen offset:1280 sc1\n"
    "v_mfma_f32_16x16x32_bf16 a[144:147], a[88:91], v[208:211], a[144:147]\n"
    "buffer_load_dwordx4 v37, s[16:19], s40 offen offset:3072 nt lds\n"
    "v_mfma_f32_16x16x32_bf16 a[148:151], a[88:91], v[212:215], a[148:151]\n"
    "v_add_u32 v30, 0x10000, v30\n"
    "s_waitcnt lgkmcnt(8)\n"
    "v_mfma_f32_16x16x32_bf16 a[152:155], a[92:95], v[208:211], a[152:155]\n"
    "ds_read_b64_tr_b16 a[116:117], v31 offset:52224\n"
    "ds_read_b64_tr_b16 a[118:119], v31 offset:60416\n"
    "v_mfma_f32_16x16x32_bf16 a[156:159], a[92:95], v[212:215], a[156:159]\n"
    "ds_read_b64_tr_b16 a[120:121], v32 offset:52224\n"
    "ds_read_b64_tr_b16 a[122:123], v32 offset:60416\n"
    "v_mfma_f32_16x16x32_bf16 a[128:131], a[96:99], v[216:219], a[128:131]\n"
    "ds_read_b64_tr_b16 a[124:125], v33 offset:52224\n"
    "s_waitcnt vmcnt(30) lgkmcnt(9)\n"
    "v_mfma_f32_16x16x32_bf16 a[132:135], a[96:99], v[220:223], a[132:135]\n"
    "ds_read_b64_tr_b16 a[126:127], v33 offset:60416\n"
    "s_barrier\n"
    "v_mfma_f32_16x16x32_bf16 a[136:139], a[100:103], v[216:219], a[136:139]\n"
    "ds_read_b64_tr_b16 a[64:65], v30 offset:0\n"
    "v_add_u32 v31, 0x10000, v31\n"
    "v_mfma_f32_16x16x32_bf16 a[140:143], a[100:103], v[220:223], a[140:143]\n"
    "ds_read_b64_tr_b16 a[66:67], v30 offset:8192\n"
    "v_add_u32 v32, 0x10000, v32\n"
    "v_mfma_f32_16x16x32_bf16 a[144:147], a[104:107], v[216:219], a[144:147]\n"
    "s_add_u32 m0, s28, 0xc000\n"
    "s_add_u32 s40, s30, 0x22080\n"
    "buffer_load_dwordx4 v37, s[16:19], s40 offen offset:3072 nt lds\n"
    "v_mfma_f32_16x16x32_bf16 a[148:151], a[104:107], v[220:223], a[148:151]\n"
    "v_add_u32 v33, 0x10000, v33\n"
    "s_waitcnt lgkmcnt(8)\n"
    "v_mfma_f32_16x16x32_bf16 a[152:155], a[108:111], v[216:219], a[152:155]\n"
    "ds_read_b64_tr_b16 a[68:69], v31 offset:0\n"
    "ds_read_b64_tr_b16 a[70:71], v31 offset:8192\n"
    "v_mfma_f32_16x16x32_bf16 a[156:159], a[108:111], v[220:223], a[156:159]\n"
    "ds_read_b64_tr_b16 a[72:73], v32 offset:0\n"
    "ds_read_b64_tr_b16 a[74:75], v32 offset:8192\n"
    "v_mfma_f32_16x16x32_bf16 a[128:131], a[112:115], v[224:227], a[128:131]\n"
    "ds_read_b64_tr_b16 a[76:77], v33 offset:0\n"
    "s_add_u32 m0, s28, 0x18000\n"
    "v_mfma_f32_16x16x32_bf16 a[132:135], a[112:115], v[228:231], a[132:135]\n"
    "ds_read_b64_tr_b16 a[78:79], v33 offset:8192\n"
    "s_waitcnt lgkmcnt(10)\n"
    "v_mfma_f32_16x16x32_bf16 a[136:139], a[116:119], v[224:227], a[136:139]\n"
    "ds_read_b64_tr_b16 a[80:81], v30 offset:1024\n"
    "s_add_u32 s40, s30, 0x42080\n"
    "v_mfma_f32_16x16x32_bf16 a[140:143], a[116:119], v[228:231], a[140:143]\n"
    "ds_read_b64_tr_b16 a[82:83], v30 offset:9216\n"
    "v_mfma_f32_16x16x32_bf16 a[144:147], a[120:123], v[224:227], a[144:147]\n"
    "buffer_load_dwordx4 v34, s[16:19], s40 offen offset:0 nt lds\n"
    "v_mfma_f32_16x16x32_bf16 a[148:151], a[120:123], v[228:231], a[148:151]\n"
    "s_waitcnt lgkmcnt(8)\n"
    "v_mfma_f32_16x16x32_bf16 a[152:155], a[124:127], v[224:227], a[152:155]\n"
    "ds_read_b64_tr_b16 a[84:85], v31 offset:1024\n"
    "ds_read_b64_tr_b16 a[86:87], v31 offset:9216\n"
    "v_mfma_f32_16x16x32_bf16 a[156:159], a[124:127], v[228:231], a[156:159]\n"
    "ds_read_b64_tr_b16 a[88:89], v32 offset:1024\n"
    "ds_read_b64_tr_b16 a[90:91], v32 offset:9216\n"
    "v_mfma_f32_16x16x32_bf16 a[128:131], a[64:67], v[232:235], a[128:131]\n"
    "ds_read_b64_tr_b16 a[92:93], v33 offset:1024\n"
    "v_mfma_f32_16x16x32_bf16 a[132:135], a[64:67], v[236:239], a[132:135]\n"
    "ds_read_b64_tr_b16 a[94:95], v33 offset:9216\n"
    "s_waitcnt lgkmcnt(10)\n"
    "v_mfma_f32_16x16x32_bf16 a[136:139], a[68:71], v[232:235], a[136:139]\n"
    "ds_read_b64_tr_b16 a[96:97], v30 offset:2048\n"
    "v_mfma_f32_16x16x32_bf16 a[140:143], a[68:71], v[236:239], a[140:143]\n"
    "ds_read_b64_tr_b16 a[98:99], v30 offset:10240\n"
    "v_mfma_f32_16x16x32_bf16 a[144:147], a[72:75], v[232:235], a[144:147]\n"
    "buffer_load_dwordx4 v35, s[16:19], s40 offen offset:1024 nt lds\n"
    "v_mfma_f32_16x16x32_bf16 a[148:151], a[72:75], v[236:239], a[148:151]\n"
    "s_waitcnt lgkmcnt(8)\n"
    "v_mfma_f32_16x16x32_bf16 a[152:155], a[76:79], v[232:235], a[152:155]\n"
    "ds_read_b64_tr_b16 a[100:101], v31 offset:2048\n"
    "ds_read_b64_tr_b16 a[102:103], v31 offset:10240\n"
    "v_mfma_f32_16x16x32_bf16 a[156:159], a[76:79], v[236:239], a[156:159]\n"
    "ds_read_b64_tr_b16 a[104:105], v32 offset:2048\n"
    "ds_read_b64_tr_b16 a[106:107], v32 offset:10240\n"
    "v_mfma_f32_16x16x32_bf16 a[128:131], a[80:83], v[240:243], a[128:131]\n"
    "ds_read_b64_tr_b16 a[108:109], v33 offset:2048\n"
    "buffer_load_dword a216, v27, s[24:27], s33 offen offset:1024 sc1\n"
    "v_mfma_f32_16x16x32_bf16 a[132:135], a[80:83], v[244:247], a[132:135]\n"
    "ds_read_b64_tr_b16 a[110:111], v33 offset:10240\n"
    "s_waitcnt lgkmcnt(10)\n"
    "v_mfma_f32_16x16x32_bf16 a[136:139], a[84:87], v[240:243], a[136:139]\n"
    "ds_read_b64_tr_b16 a[112:113], v30 offset:3072\n"
    "v_mfma_f32_16x16x32_bf16 a[140:143], a[84:87], v[244:247], a[140:143]\n"
    "ds_read_b64_tr_b16 a[114:115], v30 offset:11264\n"
    "v_mfma_f32_16x16x32_bf16 a[144:147], a[88:91], v[240:243], a[144:147]\n"
    "buffer_load_dwordx4 v36, s[16:19], s40 offen offset:2048 nt lds\n"
    "v_mfma_f32_16x16x32_bf16 a[148:151], a[88:91], v[244:247], a[148:151]\n"
    "s_waitcnt lgkmcnt(8)\n"
    "v_mfma_f32_16x16x32_bf16 a[152:155], a[92:95], v[240:243], a[152:155]\n"
    "ds_read_b64_tr_b16 a[116:117], v31 offset:3072\n"
    "ds_read_b64_tr_b16 a[118:119], v31 offset:11264\n"
    "v_mfma_f32_16x16x32_bf16 a[156:159], a[92:95], v[244:247], a[156:159]\n"
    "ds_read_b64_tr_b16 a[120:121], v32 offset:3072\n"
    "ds_read_b64_tr_b16 a[122:123], v32 offset:11264\n"
    "v_mfma_f32_16x16x32_bf16 a[128:131], a[96:99], v[248:251], a[128:131]\n"
    "ds_read_b64_tr_b16 a[124:125], v33 offset:3072\n"
    "s_waitcnt vmcnt(31) lgkmcnt(9)\n"
    "v_mfma_f32_16x16x32_bf16 a[132:135], a[96:99], v[252:255], a[132:135]\n"
    "ds_read_b64_tr_b16 a[126:127], v33 offset:11264\n"
    "s_barrier\n"
    "v_mfma_f32_16x16x32_bf16 a[136:139], a[100:103], v[248:251], a[136:139]\n"
    "ds_read_b64_tr_b16 a[64:65], v30 offset:16384\n"
    "v_mfma_f32_16x16x32_bf16 a[140:143], a[100:103], v[252:255], a[140:143]\n"
    "ds_read_b64_tr_b16 a[66:67], v30 offset:24576\n"
    "v_mfma_f32_16x16x32_bf16 a[144:147], a[104:107], v[248:251], a[144:147]\n"
    "buffer_load_dwordx4 v37, s[16:19], s40 offen offset:3072 nt lds\n"
    "v_mfma_f32_16x16x32_bf16 a[148:151], a[104:107], v[252:255], a[148:151]\n"
    "s_add_u32 m0, s28, 0x1c000\n"
    "s_waitcnt lgkmcnt(8)\n"
    "v_mfma_f32_16x16x32_bf16 a[152:155], a[108:111], v[248:251], a[152:155]\n"
    "ds_read_b64_tr_b16 a[68:69], v31 offset:16384\n"
    "ds_read_b64_tr_b16 a[70:71], v31 offset:24576\n"
    "v_mfma_f32_16x16x32_bf16 a[156:159], a[108:111], v[252:255], a[156:159]\n"
    "ds_read_b64_tr_b16 a[72:73], v32 offset:16384\n"
    "ds_read_b64_tr_b16 a[74:75], v32 offset:24576\n"
    "v_mfma_f32_16x16x32_bf16 a[128:131], a[112:115], a[160:163], a[128:131]\n"
    "ds_read_b64_tr_b16 a[76:77], v33 offset:16384\n"
    "s_add_u32 s40, s30, 0x62080\n"
    "v_mfma_f32_16x16x32_bf16 a[132:135], a[112:115], a[164:167], a[132:135]\n"
    "ds_read_b64_tr_b16 a[78:79], v33 offset:24576\n"
    "s_waitcnt lgkmcnt(10)\n"
    "v_mfma_f32_16x16x32_bf16 a[136:139], a[116:119], a[160:163], a[136:139]\n"
    "ds_read_b64_tr_b16 a[80:81], v30 offset:17408\n"
    "v_mfma_f32_16x16x32_bf16 a[140:143], a[116:119], a[164:167], a[140:143]\n"
    "ds_read_b64_tr_b16 a[82:83], v30 offset:25600\n"
    "buffer_load_dword a217, v27, s[24:27], s33 offen offset:1280 sc1\n"
    "v_mfma_f32_16x16x32_bf16 a[144:147], a[120:123], a[160:163], a[144:147]\n"
    "buffer_load_dwordx4 v34, s[16:19], s40 offen offset:0 nt lds\n"
    "v_mfma_f32_16x16x32_bf16 a[148:151], a[120:123], a[164:167], a[148:151]\n"
    "s_waitcnt lgkmcnt(8)\n"
    "v_mfma_f32_16x16x32_bf16 a[152:155], a[124:127], a[160:163], a[152:155]\n"
    "ds_read_b64_tr_b16 a[84:85], v31 offset:17408\n"
    "ds_read_b64_tr_b16 a[86:87], v31 offset:25600\n"
    "v_mfma_f32_16x16x32_bf16 a[156:159], a[124:127], a[164:167], a[156:159]\n"
    "ds_read_b64_tr_b16 a[88:89], v32 offset:17408\n"
    "ds_read_b64_tr_b16 a[90:91], v32 offset:25600\n"
    "v_mfma_f32_16x16x32_bf16 a[128:131], a[64:67], a[168:171], a[128:131]\n"
    "ds_read_b64_tr_b16 a[92:93], v33 offset:17408\n"
    "v_mfma_f32_16x16x32_bf16 a[132:135], a[64:67], a[172:175], a[132:135]\n"
    "ds_read_b64_tr_b16 a[94:95], v33 offset:25600\n"
    "s_waitcnt lgkmcnt(10)\n"
    "v_mfma_f32_16x16x32_bf16 a[136:139], a[68:71], a[168:171], a[136:139]\n"
    "ds_read_b64_tr_b16 a[96:97], v30 offset:18432\n"
    "v_mfma_f32_16x16x32_bf16 a[140:143], a[68:71], a[172:175], a[140:143]\n"
    "ds_read_b64_tr_b16 a[98:99], v30 offset:26624\n"
    "v_mfma_f32_16x16x32_bf16 a[144:147], a[72:75], a[168:171], a[144:147]\n"
    "buffer_load_dwordx4 v35, s[16:19], s40 offen offset:1024 nt lds\n"
    "v_mfma_f32_16x16x32_bf16 a[148:151], a[72:75], a[172:175], a[148:151]\n"
    "s_waitcnt lgkmcnt(8)\n"
    "v_mfma_f32_16x16x32_bf16 a[152:155], a[76:79], a[168:171], a[152:155]\n"
    "ds_read_b64_tr_b16 a[100:101], v31 offset:18432\n"
    "ds_read_b64_tr_b16 a[102:103], v31 offset:26624\n"
    "v_mfma_f32_16x16x32_bf16 a[156:159], a[76:79], a[172:175], a[156:159]\n"
    "ds_read_b64_tr_b16 a[104:105], v32 offset:18432\n"
    "ds_read_b64_tr_b16 a[106:107], v32 offset:26624\n"
    "v_mfma_f32_16x16x32_bf16 a[128:131], a[80:83], a[176:179], a[128:131]\n"
    "ds_read_b64_tr_b16 a[108:109], v33 offset:18432\n"
    "v_mfma_f32_16x16x32_bf16 a[132:135], a[80:83], a[180:183], a[132:135]\n"
    "ds_read_b64_tr_b16 a[110:111], v33 offset:26624\n"
    "s_waitcnt lgkmcnt(10)\n"
    "v_mfma_f32_16x16x32_bf16 a[136:139], a[84:87], a[176:179], a[136:139]\n"
    "ds_read_b64_tr_b16 a[112:113], v30 offset:19456\n"
    "v_mfma_f32_16x16x32_bf16 a[140:143], a[84:87], a[180:183], a[140:143]\n"
    "ds_read_b64_tr_b16 a[114:115], v30 offset:27648\n"
    "v_mfma_f32_16x16x32_bf16 a[144:147], a[88:91], a[176:179], a[144:147]\n"
    "buffer_load_dwordx4 v36, s[16:19], s40 offen offset:2048 nt lds\n"
    "v_mfma_f32_16x16x32_bf16 a[148:151], a[88:91], a[180:183], a[148:151]\n"
    "v_add_u32 v30, 0xfffe0000, v30\n"
    "s_waitcnt lgkmcnt(8)\n"
    "v_mfma_f32_16x16x32_bf16 a[152:155], a[92:95], a[176:179], a[152:155]\n"
    "ds_read_b64_tr_b16 a[116:117], v31 offset:19456\n"
    "ds_read_b64_tr_b16 a[118:119], v31 offset:27648\n"
    "v_mfma_f32_16x16x32_bf16 a[156:159], a[92:95], a[180:183], a[156:159]\n"
    "ds_read_b64_tr_b16 a[120:121], v32 offset:19456\n"
    "ds_read_b64_tr_b16 a[122:123], v32 offset:27648\n"
    "v_mfma_f32_16x16x32_bf16 a[128:131], a[96:99], a[184:187], a[128:131]\n"
    "ds_read_b64_tr_b16 a[124:125], v33 offset:19456\n"
    "s_waitcnt vmcnt(32) lgkmcnt(9)\n"
    "v_mfma_f32_16x16x32_bf16 a[132:135], a[96:99], a[188:191], a[132:135]\n"
    "ds_read_b64_tr_b16 a[126:127], v33 offset:27648\n"
    "s_barrier\n"
    "v_mfma_f32_16x16x32_bf16 a[136:139], a[100:103], a[184:187], a[136:139]\n"
    "ds_read_b64_tr_b16 a[64:65], v30 offset:0\n"
    "v_add_u32 v31, 0xfffe0000, v31\n"
    "STUB6_%=:\n"
    "s_cbranch_execz STUB10_%=\n"
    "v_mfma_f32_16x16x32_bf16 a[140:143], a[100:103], a[188:191], a[140:143]\n"
    "ds_read_b64_tr_b16 a[66:67], v30 offset:8192\n"
    "v_add_u32 v32, 0xfffe0000, v32\n"
    "v_mfma_f32_16x16x32_bf16 a[144:147], a[104:107], a[184:187], a[144:147]\n"
    "buffer_load_dwordx4 v37, s[16:19], s40 offen offset:3072 nt lds\n"
    "v_mfma_f32_16x16x32_bf16 a[148:151], a[104:107], a[188:191], a[148:151]\n"
    "v_add_u32 v33, 0xfffe0000, v33\n"
    "s_waitcnt lgkmcnt(8)\n"
    "v_mfma_f32_16x16x32_bf16 a[152:155], a[108:111], a[184:187], a[152:155]\n"
    "ds_read_b64_tr_b16 a[68:69], v31 offset:0\n"
    "ds_read_b64_tr_b16 a[70:71], v31 offset:8192\n"
    "v_mfma_f32_16x16x32_bf16 a[156:159], a[108:111], a[188:191], a[156:159]\n"
    "ds_read_b64_tr_b16 a[72:73], v32 offset:0\n"
    "ds_read_b64_tr_b16 a[74:75], v32 offset:8192\n"
    "v_mfma_f32_16x16x32_bf16 a[128:131], a[112:115], a[192:195], a[128:131]\n"
    "ds_read_b64_tr_b16 a[76:77], v33 offset:0\n"
    "s_add_u32 m0, s28, 0x20000\n"
    "v_mfma_f32_16x16x32_bf16 a[132:135], a[112:115], a[196:199], a[132:135]\n"
    "ds_read_b64_tr_b16 a[78:79], v33 offset:8192\n"
    "s_waitcnt lgkmcnt(10)\n"
    "v_mfma_f32_16x16x32_bf16 a[136:139], a[116:119], a[192:195], a[136:139]\n"
    "ds_read_b64_tr_b16 a[80:81], v30 offset:1024\n"
    "s_add_u32 s40, s31, 0x0\n"
    "v_mfma_f32_16x16x32_bf16 a[140:143], a[116:119], a[196:199], a[140:143]\n"
    "ds_read_b64_tr_b16 a[82:83], v30 offset:9216\n"
    "v_mfma_f32_16x16x32_bf16 a[144:147], a[120:123], a[192:195], a[144:147]\n"
    "buffer_load_dwordx4 v34, s[16:19], s40 offen offset:0 nt lds\n"
    "v_mfma_f32_16x16x32_bf16 a[148:151], a[120:123], a[196:199], a[148:151]\n"
    "s_waitcnt lgkmcnt(8)\n"
    "v_mfma_f32_16x16x32_bf16 a[152:155], a[124:127], a[192:195], a[152:155]\n"
    "ds_read_b64_tr_b16 a[84:85], v31 offset:1024\n"
    "ds_read_b64_tr_b16 a[86:87], v31 offset:9216\n"
    "v_mfma_f32_16x16x32_bf16 a[156:159], a[124:127], a[196:199], a[156:159]\n"
    "ds_read_b64_tr_b16 a[88:89], v32 offset:1024\n"
    "ds_read_b64_tr_b16 a[90:91], v32 offset:9216\n"
    "v_mfma_f32_16x16x32_bf16 a[0:3], a[64:67], v[40:43], 0\n"
    "ds_read_b64_tr_b16 a[92:93], v33 offset:1024\n"
    "v_mfma_f32_16x16x32_bf16 a[4:7], a[64:67], v[44:47], 0\n"
    "ds_read_b64_tr_b16 a[94:95], v33 offset:9216\n"
    "v_accvgpr_read_b32 v0, a128\n"
    "s_waitcnt lgkmcnt(10)\n"
    "v_mfma_f32_16x16x32_bf16 a[8:11], a[68:71], v[40:43], 0\n"
    "ds_read_b64_tr_b16 a[96:97], v30 offset:2048\n"
    "v_accvgpr_read_b32 v1, a129\n"
    "v_accvgpr_read_b32 v2, a130\n"
    "v_mfma_f32_16x16x32_bf16 a[12:15], a[68:71], v[44:47], 0\n"
    "ds_read_b64_tr_b16 a[98:99], v30 offset:10240\n"
    "v_accvgpr_read_b32 v3, a131\n"
    "v_accvgpr_read_b32 v4, a132\n"
    "v_mfma_f32_16x16x32_bf16 a[16:19], a[72:75], v[40:43], 0\n"
    "buffer_load_dwordx4 v35, s[16:19], s40 offen offset:1024 nt lds\n"
    "v_accvgpr_read_b32 v5, a133\n"
    "v_mfma_f32_16x16x32_bf16 a[20:23], a[72:75], v[44:47], 0\n"
    "v_accvgpr_read_b32 v6, a134\n"
    "v_accvgpr_read_b32 v7, a135\n"
    "s_waitcnt lgkmcnt(8)\n"
    "v_mfma_f32_16x16x32_bf16 a[24:27], a[76:79], v[40:43], 0\n"
    "ds_read_b64_tr_b16 a[100:101], v31 offset:2048\n"
    "ds_read_b64_tr_b16 a[102:103], v31 offset:10240\n"
    "v_cvt_pk_bf16_f32 v8, v0, v1\n"
    "v_mfma_f32_16x16x32_bf16 a[28:31], a[76:79], v[44:47], 0\n"
    "ds_read_b64_tr_b16 a[104:105], v32 offset:2048\n"
    "ds_read_b64_tr_b16 a[106:107], v32 offset:10240\n"
    "v_cvt_pk_bf16_f32 v9, v2, v3\n"
    "v_mfma_f32_16x16x32_bf16 a[0:3], a[80:83], v[48:51], a[0:3]\n"
    "ds_read_b64_tr_b16 a[108:109], v33 offset:2048\n"
    "v_cvt_pk_bf16_f32 v10, v4, v5\n"
    "v_cvt_pk_bf16_f32 v11, v6, v7\n"
    "v_mfma_f32_16x16x32_bf16 a[4:7], a[80:83], v[52:55], a[4:7]\n"
    "ds_read_b64_tr_b16 a[110:111], v33 offset:10240\n"
    "s_waitcnt lgkmcnt(10)\n"
    "v_mfma_f32_16x16x32_bf16 a[8:11], a[84:87], v[48:51], a[8:11]\n"
    "ds_read_b64_tr_b16 a[112:113], v30 offset:3072\n"
    "v_mfma_f32_16x16x32_bf16 a[12:15], a[84:87], v[52:55], a[12:15]\n"
    "ds_read_b64_tr_b16 a[114:115], v30 offset:11264\n"
    "v_mfma_f32_16x16x32_bf16 a[16:19], a[88:91], v[48:51], a[16:19]\n"
    "buffer_store_dwordx4 v[8:11], v24, s[20:23], s30 offen offset:0 sc1\n"
    "v_accvgpr_read_b32 v0, a136\n"
    "v_mfma_f32_16x16x32_bf16 a[20:23], a[88:91], v[52:55], a[20:23]\n"
    "v_accvgpr_read_b32 v1, a137\n"
    "v_accvgpr_read_b32 v2, a138\n"
    "s_waitcnt lgkmcnt(8)\n"
    "v_mfma_f32_16x16x32_bf16 a[24:27], a[92:95], v[48:51], a[24:27]\n"
    "ds_read_b64_tr_b16 a[116:117], v31 offset:3072\n"
    "ds_read_b64_tr_b16 a[118:119], v31 offset:11264\n"
    "v_accvgpr_read_b32 v3, a139\n"
    "v_mfma_f32_16x16x32_bf16 a[28:31], a[92:95], v[52:55], a[28:31]\n"
    "ds_read_b64_tr_b16 a[120:121], v32 offset:3072\n"
    "ds_read_b64_tr_b16 a[122:123], v32 offset:11264\n"
    "v_accvgpr_read_b32 v4, a140\n"
    "v_mfma_f32_16x16x32_bf16 a[0:3], a[96:99], v[56:59], a[0:3]\n"
    "ds_read_b64_tr_b16 a[124:125], v33 offset:3072\n"
    "s_waitcnt vmcnt(32) lgkmcnt(9)\n"
    "v_accvgpr_read_b32 v5, a141\n"
    "v_mfma_f32_16x16x32_bf16 a[4:7], a[96:99], v[60:63], a[4:7]\n"
    "ds_read_b64_tr_b16 a[126:127], v33 offset:11264\n"
    "s_barrier\n"
    "v_mfma_f32_16x16x32_bf16 a[8:11], a[100:103], v[56:59], a[8:11]\n"
    "ds_read_b64_tr_b16 a[64:65], v30 offset:16384\n"
    "v_accvgpr_read_b32 v6, a142\n"
    "v_accvgpr_read_b32 v7, a143\n"
    "v_mfma_f32_16x16x32_bf16 a[12:15], a[100:103], v[60:63], a[12:15]\n"
    "ds_read_b64_tr_b16 a[66:67], v30 offset:24576\n"
    "v_cvt_pk_bf16_f32 v12, v0, v1\n"
    "v_cvt_pk_bf16_f32 v13, v2, v3\n"
    "v_mfma_f32_16x16x32_bf16 a[16:19], a[104:107], v[56:59], a[16:19]\n"
    "buffer_load_dwordx4 v36, s[16:19], s40 offen offset:2048 nt lds\n"
    "v_cvt_pk_bf16_f32 v14, v4, v5\n"
    "v_mfma_f32_16x16x32_bf16 a[20:23], a[104:107], v[60:63], a[20:23]\n"
    "v_cvt_pk_bf16_f32 v15, v6, v7\n"
    "s_waitcnt lgkmcnt(8)\n"
    "v_mfma_f32_16x16x32_bf16 a[24:27], a[108:111], v[56:59], a[24:27]\n"
    "ds_read_b64_tr_b16 a[68:69], v31 offset:16384\n"
    "ds_read_b64_tr_b16 a[70:71], v31 offset:24576\n"
    "v_mfma_f32_16x16x32_bf16 a[28:31], a[108:111], v[60:63], a[28:31]\n"
    "ds_read_b64_tr_b16 a[72:73], v32 offset:16384\n"
    "ds_read_b64_tr_b16 a[74:75], v32 offset:24576\n"
    "v_mfma_f32_16x16x32_bf16 a[0:3], a[112:115], v[64:67], a[0:3]\n"
    "ds_read_b64_tr_b16 a[76:77], v33 offset:16384\n"
    "v_mfma_f32_16x16x32_bf16 a[4:7], a[112:115], v[68:71], a[4:7]\n"
    "ds_read_b64_tr_b16 a[78:79], v33 offset:24576\n"
    "s_waitcnt lgkmcnt(10)\n"
    "v_mfma_f32_16x16x32_bf16 a[8:11], a[116:119], v[64:67], a[8:11]\n"
    "ds_read_b64_tr_b16 a[80:81], v30 offset:17408\n"
    "v_mfma_f32_16x16x32_bf16 a[12:15], a[116:119], v[68:71], a[12:15]\n"
    "ds_read_b64_tr_b16 a[82:83], v30 offset:25600\n"
    "v_mfma_f32_16x16x32_bf16 a[16:19], a[120:123], v[64:67], a[16:19]\n"
    "buffer_store_dwordx4 v[12:15], v24, s[20:23], s30 offen offset:512 sc1\n"
    "v_accvgpr_read_b32 v0, a144\n"
    "v_mfma_f32_16x16x32_bf16 a[20:23], a[120:123], v[68:71], a[20:23]\n"
    "v_accvgpr_read_b32 v1, a145\n"
    "v_accvgpr_read_b32 v2, a146\n"
    "s_waitcnt lgkmcnt(8)\n"
    "v_mfma_f32_16x16x32_bf16 a[24:27], a[124:127], v[64:67], a[24:27]\n"
    "ds_read_b64_tr_b16 a[84:85], v31 offset:17408\n"
    "ds_read_b64_tr_b16 a[86:87], v31 offset:25600\n"
    "v_accvgpr_read_b32 v3, a147\n"
    "v_mfma_f32_16x16x32_bf16 a[28:31], a[124:127], v[68:71], a[28:31]\n"
    "ds_read_b64_tr_b16 a[88:89], v32 offset:17408\n"
    "ds_read_b64_tr_b16 a[90:91], v32 offset:25600\n"
    "v_accvgpr_read_b32 v4, a148\n"
    "v_mfma_f32_16x16x32_bf16 a[0:3], a[64:67], v[72:75], a[0:3]\n"
    "ds_read_b64_tr_b16 a[92:93], v33 offset:17408\n"
    "v_accvgpr_read_b32 v5, a149\n"
    "v_accvgpr_read_b32 v6, a150\n"
    "v_mfma_f32_16x16x32_bf16 a[4:7], a[64:67], v[76:79], a[4:7]\n"
    "ds_read_b64_tr_b16 a[94:95], v33 offset:25600\n"
    "v_accvgpr_read_b32 v7, a151\n"
    "s_waitcnt lgkmcnt(10)\n"
    "v_mfma_f32_16x16x32_bf16 a[8:11], a[68:71], v[72:75], a[8:11]\n"
    "ds_read_b64_tr_b16 a[96:97], v30 offset:18432\n"
    "v_cvt_pk_bf16_f32 v8, v0, v1\n"
    "v_cvt_pk_bf16_f32 v9, v2, v3\n"
    "v_mfma_f32_16x16x32_bf16 a[12:15], a[68:71], v[76:79], a[12:15]\n"
    "ds_read_b64_tr_b16 a[98:99], v30 offset:26624\n"
    "v_cvt_pk_bf16_f32 v10, v4, v5\n"
    "v_cvt_pk_bf16_f32 v11, v6, v7\n"
    "v_mfma_f32_16x16x32_bf16 a[16:19], a[72:75], v[72:75], a[16:19]\n"
    "buffer_load_dwordx4 v37, s[16:19], s40 offen offset:3072 nt lds\n"
    "v_mfma_f32_16x16x32_bf16 a[20:23], a[72:75], v[76:79], a[20:23]\n"
    "s_add_u32 m0, s28, 0x24000\n"
    "s_waitcnt lgkmcnt(8)\n"
    "v_mfma_f32_16x16x32_bf16 a[24:27], a[76:79], v[72:75], a[24:27]\n"
    "ds_read_b64_tr_b16 a[100:101], v31 offset:18432\n"
    "ds_read_b64_tr_b16 a[102:103], v31 offset:26624\n"
    "v_mfma_f32_16x16x32_bf16 a[28:31], a[76:79], v[76:79], a[28:31]\n"
    "ds_read_b64_tr_b16 a[104:105], v32 offset:18432\n"
    "ds_read_b64_tr_b16 a[106:107], v32 offset:26624\n"
    "v_mfma_f32_16x16x32_bf16 a[0:3], a[80:83], v[80:83], a[0:3]\n"
    "ds_read_b64_tr_b16 a[108:109], v33 offset:18432\n"
    "s_add_u32 s40, s31, 0x20000\n"
    "v_mfma_f32_16x16x32_bf16 a[4:7], a[80:83], v[84:87], a[4:7]\n"
    "ds_read_b64_tr_b16 a[110:111], v33 offset:26624\n"
    "s_waitcnt lgkmcnt(10)\n"
    "v_mfma_f32_16x16x32_bf16 a[8:11], a[84:87], v[80:83], a[8:11]\n"
    "ds_read_b64_tr_b16 a[112:113], v30 offset:19456\n"
    "v_mfma_f32_16x16x32_bf16 a[12:15], a[84:87], v[84:87], a[12:15]\n"
    "ds_read_b64_tr_b16 a[114:115], v30 offset:27648\n"
    "v_mfma_f32_16x16x32_bf16 a[16:19], a[88:91], v[80:83], a[16:19]\n"
    "buffer_store_dwordx4 v[8:11], v24, s[20:23], s30 offen offset:1024 sc1\n"
    "v_accvgpr_read_b32 v0, a152\n"
    "v_mfma_f32_16x16x32_bf16 a[20:23], a[88:91], v[84:87], a[20:23]\n"
    "v_accvgpr_read_b32 v1, a153\n"
    "v_accvgpr_read_b32 v2, a154\n"
    "s_waitcnt lgkmcnt(8)\n"
    "v_mfma_f32_16x16x32_bf16 a[24:27], a[92:95], v[80:83], a[24:27]\n"
    "ds_read_b64_tr_b16 a[116:117], v31 offset:19456\n"
    "ds_read_b64_tr_b16 a[118:119], v31 offset:27648\n"
    "v_accvgpr_read_b32 v3, a155\n"
    "v_mfma_f32_16x16x32_bf16 a[28:31], a[92:95], v[84:87], a[28:31]\n"
    "ds_read_b64_tr_b16 a[120:121], v32 offset:19456\n"
    "ds_read_b64_tr_b16 a[122:123], v32 offset:27648\n"
    "v_accvgpr_read_b32 v4, a156\n"
    "v_mfma_f32_16x16x32_bf16 a[0:3], a[96:99], v[88:91], a[0:3]\n"
    "ds_read_b64_tr_b16 a[124:125], v33 offset:19456\n"
    "s_waitcnt vmcnt(32) lgkmcnt(9)\n"
    "v_accvgpr_read_b32 v5, a157\n"
    "v_mfma_f32_16x16x32_bf16 a[4:7], a[96:99], v[92:95], a[4:7]\n"
    "ds_read_b64_tr_b16 a[126:127], v33 offset:27648\n"
    "s_barrier\n"
    "v_mfma_f32_16x16x32_bf16 a[8:11], a[100:103], v[88:91], a[8:11]\n"
    "ds_read_b64_tr_b16 a[64:65], v30 offset:32768\n"
    "v_accvgpr_read_b32 v6, a158\n"
    "v_accvgpr_read_b32 v7, a159\n"
    "v_mfma_f32_16x16x32_bf16 a[12:15], a[100:103], v[92:95], a[12:15]\n"
    "ds_read_b64_tr_b16 a[66:67], v30 offset:40960\n"
    "v_cvt_pk_bf16_f32 v12, v0, v1\n"
    "v_cvt_pk_bf16_f32 v13, v2, v3\n"
    "v_mfma_f32_16x16x32_bf16 a[16:19], a[104:107], v[88:91], a[16:19]\n"
    "buffer_load_dwordx4 v34, s[16:19], s40 offen offset:0 nt lds\n"
    "v_cvt_pk_bf16_f32 v14, v4, v5\n"
    "v_mfma_f32_16x16x32_bf16 a[20:23], a[104:107], v[92:95], a[20:23]\n"
    "v_cvt_pk_bf16_f32 v15, v6, v7\n"
    "s_waitcnt lgkmcnt(8)\n"
    "v_mfma_f32_16x16x32_bf16 a[24:27], a[108:111], v[88:91], a[24:27]\n"
    "ds_read_b64_tr_b16 a[68:69], v31 offset:32768\n"
    "ds_read_b64_tr_b16 a[70:71], v31 offset:40960\n"
    "v_mfma_f32_16x16x32_bf16 a[28:31], a[108:111], v[92:95], a[28:31]\n"
    "ds_read_b64_tr_b16 a[72:73], v32 offset:32768\n"
    "ds_read_b64_tr_b16 a[74:75], v32 offset:40960\n"
    "v_mfma_f32_16x16x32_bf16 a[0:3], a[112:115], v[96:99], a[0:3]\n"
    "ds_read_b64_tr_b16 a[76:77], v33 offset:32768\n"
    "v_mfma_f32_16x16x32_bf16 a[4:7], a[112:115], v[100:103], a[4:7]\n"
    "ds_read_b64_tr_b16 a[78:79], v33 offset:40960\n"
    "s_waitcnt lgkmcnt(10)\n"
    "v_mfma_f32_16x16x32_bf16 a[8:11], a[116:119], v[96:99], a[8:11]\n"
    "ds_read_b64_tr_b16 a[80:81], v30 offset:33792\n"
    "v_mfma_f32_16x16x32_bf16 a[12:15], a[116:119], v[100:103], a[12:15]\n"
    "ds_read_b64_tr_b16 a[82:83], v30 offset:41984\n"
    "v_mfma_f32_16x16x32_bf16 a[16:19], a[120:123], v[96:99], a[16:19]\n"
    "buffer_store_dwordx4 v[12:15], v24, s[20:23], s30 offen offset:1536 sc1\n"
    "v_mfma_f32_16x16x32_bf16 a[20:23], a[120:123], v[100:103], a[20:23]\n"
    "s_waitcnt lgkmcnt(8)\n"
    "v_mfma_f32_16x16x32_bf16 a[24:27], a[124:127], v[96:99], a[24:27]\n"
    "ds_read_b64_tr_b16 a[84:85], v31 offset:33792\n"
    "ds_read_b64_tr_b16 a[86:87], v31 offset:41984\n"
    "v_mfma_f32_16x16x32_bf16 a[28:31], a[124:127], v[100:103], a[28:31]\n"
    "ds_read_b64_tr_b16 a[88:89], v32 offset:33792\n"
    "ds_read_b64_tr_b16 a[90:91], v32 offset:41984\n"
    "v_mfma_f32_16x16x32_bf16 a[0:3], a[64:67], v[104:107], a[0:3]\n"
    "ds_read_b64_tr_b16 a[92:93], v33 offset:33792\n"
    "v_mfma_f32_16x16x32_bf16 a[4:7], a[64:67], v[108:111], a[4:7]\n"
    "ds_read_b64_tr_b16 a[94:95], v33 offset:41984\n"
    "s_waitcnt lgkmcnt(10)\n"
    "v_mfma_f32_16x16x32_bf16 a[8:11], a[68:71], v[104:107], a[8:11]\n"
    "ds_read_b64_tr_b16 a[96:97], v30 offset:34816\n"
    "v_mfma_f32_16x16x32_bf16 a[12:15], a[68:71], v[108:111], a[12:15]\n"
    "ds_read_b64_tr_b16 a[98:99], v30 offset:43008\n"
    "v_mfma_f32_16x16x32_bf16 a[16:19], a[72:75], v[104:107], a[16:19]\n"
    "buffer_load_dwordx4 v35, s[16:19], s40 offen offset:1024 nt lds\n"
    "v_mfma_f32_16x16x32_bf16 a[20:23], a[72:75], v[108:111], a[20:23]\n"
    "s_waitcnt lgkmcnt(8)\n"
    "v_mfma_f32_16x16x32_bf16 a[24:27], a[76:79], v[104:107], a[24:27]\n"
    "ds_read_b64_tr_b16 a[100:101], v31 offset:34816\n"
    "ds_read_b64_tr_b16 a[102:103], v31 offset:43008\n"
    "v_mfma_f32_16x16x32_bf16 a[28:31], a[76:79], v[108:111], a[28:31]\n"
    "ds_read_b64_tr_b16 a[104:105], v32 offset:34816\n"
    "ds_read_b64_tr_b16 a[106:107], v32 offset:43008\n"
    "v_mfma_f32_16x16x32_bf16 a[0:3], a[80:83], v[112:115], a[0:3]\n"
    "ds_read_b64_tr_b16 a[108:109], v33 offset:34816\n"
    "v_mfma_f32_16x16x32_bf16 a[4:7], a[80:83], v[116:119], a[4:7]\n"
    "ds_read_b64_tr_b16 a[110:111], v33 offset:43008\n"
    "s_waitcnt lgkmcnt(10)\n"
    "v_mfma_f32_16x16x32_bf16 a[8:11], a[84:87], v[112:115], a[8:11]\n"
    "ds_read_b64_tr_b16 a[112:113], v30 offset:35840\n"
    "v_mfma_f32_16x16x32_bf16 a[12:15], a[84:87], v[116:119], a[12:15]\n"
    "ds_read_b64_tr_b16 a[114:115], v30 offset:44032\n"
    "v_mfma_f32_16x16x32_bf16 a[16:19], a[88:91], v[112:115], a[16:19]\n"
    "buffer_load_dwordx4 v36, s[16:19], s40 offen offset:2048 nt lds\n"
    "v_mfma_f32_16x16x32_bf16 a[20:23], a[88:91], v[116:119], a[20:23]\n"
    "s_waitcnt lgkmcnt(8)\n"
    "v_mfma_f32_16x16x32_bf16 a[24:27], a[92:95], v[112:115], a[24:27]\n"
    "ds_read_b64_tr_b16 a[116:117], v31 offset:35840\n"
    "ds_read_b64_tr_b16 a[118:119], v31 offset:44032\n"
    "v_mfma_f32_16x16x32_bf16 a[28:31], a[92:95], v[116:119], a[28:31]\n"
    "ds_read_b64_tr_b16 a[120:121], v32 offset:35840\n"
    "ds_read_b64_tr_b16 a[122:123], v32 offset:44032\n"
    "v_mfma_f32_16x16x32_bf16 a[0:3], a[96:99], v[120:123], a[0:3]\n"
    "ds_read_b64_tr_b16 a[124:125], v33 offset:35840\n"
    "s_waitcnt vmcnt(21) lgkmcnt(9)\n"
    "v_mfma_f32_16x16x32_bf16 a[4:7], a[96:99], v[124:127], a[4:7]\n"
    "ds_read_b64_tr_b16 a[126:127], v33 offset:44032\n"
    "s_barrier\n"
    "v_mfma_f32_16x16x32_bf16 a[8:11], a[100:103], v[120:123], a[8:11]\n"
    "ds_read_b64_tr_b16 a[64:65], v30 offset:49152\n"
    "v_mfma_f32_16x16x32_bf16 a[12:15], a[100:103], v[124:127], a[12:15]\n"
    "ds_read_b64_tr_b16 a[66:67], v30 offset:57344\n"
    "v_mfma_f32_16x16x32_bf16 a[16:19], a[104:107], v[120:123], a[16:19]\n"
    "buffer_load_dwordx4 v37, s[16:19], s40 offen offset:3072 nt lds\n"
    "v_mfma_f32_16x16x32_bf16 a[20:23], a[104:107], v[124:127], a[20:23]\n"
    "s_add_u32 m0, s28, 0x0\n"
    "s_waitcnt lgkmcnt(8)\n"
    "v_mfma_f32_16x16x32_bf16 a[24:27], a[108:111], v[120:123], a[24:27]\n"
    "ds_read_b64_tr_b16 a[68:69], v31 offset:49152\n"
    "ds_read_b64_tr_b16 a[70:71], v31 offset:57344\n"
    "v_mfma_f32_16x16x32_bf16 a[28:31], a[108:111], v[124:127], a[28:31]\n"
    "ds_read_b64_tr_b16 a[72:73], v32 offset:49152\n"
    "ds_read_b64_tr_b16 a[74:75], v32 offset:57344\n"
    "v_mfma_f32_16x16x32_bf16 a[0:3], a[112:115], v[128:131], a[0:3]\n"
    "ds_read_b64_tr_b16 a[76:77], v33 offset:49152\n"
    "s_add_u32 s40, s31, 0x2000\n"
    "v_mfma_f32_16x16x32_bf16 a[4:7], a[112:115], v[132:135], a[4:7]\n"
    "ds_read_b64_tr_b16 a[78:79], v33 offset:57344\n"
    "s_waitcnt lgkmcnt(10)\n"
    "v_mfma_f32_16x16x32_bf16 a[8:11], a[116:119], v[128:131], a[8:11]\n"
    "ds_read_b64_tr_b16 a[80:81], v30 offset:50176\n"
    "v_mfma_f32_16x16x32_bf16 a[12:15], a[116:119], v[132:135], a[12:15]\n"
    "ds_read_b64_tr_b16 a[82:83], v30 offset:58368\n"
    "v_mfma_f32_16x16x32_bf16 a[16:19], a[120:123], v[128:131], a[16:19]\n"
    "buffer_load_dwordx4 v34, s[16:19], s40 offen offset:0 nt lds\n"
    "v_mfma_f32_16x16x32_bf16 a[20:23], a[120:123], v[132:135], a[20:23]\n"
    "s_waitcnt lgkmcnt(8)\n"
    "v_mfma_f32_16x16x32_bf16 a[24:27], a[124:127], v[128:131], a[24:27]\n"
    "ds_read_b64_tr_b16 a[84:85], v31 offset:50176\n"
    "ds_read_b64_tr_b16 a[86:87], v31 offset:58368\n"
    "v_mfma_f32_16x16x32_bf16 a[28:31], a[124:127], v[132:135], a[28:31]\n"
    "ds_read_b64_tr_b16 a[88:89], v32 offset:50176\n"
    "ds_read_b64_tr_b16 a[90:91], v32 offset:58368\n"
    "s_memrealtime s[36:37]\n"
    "v_mfma_f32_16x16x32_bf16 a[0:3], a[64:67], v[136:139], a[0:3]\n"
    "ds_read_b64_tr_b16 a[92:93], v33 offset:50176\n"
    "v_mfma_f32_16x16x32_bf16 a[4:7], a[64:67], v[140:143], a[4:7]\n"
    "ds_read_b64_tr_b16 a[94:95], v33 offset:58368\n"
    "s_waitcnt lgkmcnt(10)\n"
    "v_mfma_f32_16x16x32_bf16 a[8:11], a[68:71], v[136:139], a[8:11]\n"
    "ds_read_b64_tr_b16 a[96:97], v30 offset:51200\n"
    "v_mfma_f32_16x16x32_bf16 a[12:15], a[68:71], v[140:143], a[12:15]\n"
    "ds_read_b64_tr_b16 a[98:99], v30 offset:59392\n"
    "v_mfma_f32_16x16x32_bf16 a[16:19], a[72:75], v[136:139], a[16:19]\n"
    "buffer_load_dwordx4 v35, s[16:19], s40 offen offset:1024 nt lds\n"
    "v_mfma_f32_16x16x32_bf16 a[20:23], a[72:75], v[140:143], a[20:23]\n"
    "s_waitcnt lgkmcnt(8)\n"
    "v_mfma_f32_16x16x32_bf16 a[24:27], a[76:79], v[136:139], a[24:27]\n"
    "ds_read_b64_tr_b16 a[100:101], v31 offset:51200\n"
    "ds_read_b64_tr_b16 a[102:103], v31 offset:59392\n"
    "v_mfma_f32_16x16x32_bf16 a[28:31], a[76:79], v[140:143], a[28:31]\n"
    "ds_read_b64_tr_b16 a[104:105], v32 offset:51200\n"
    "ds_read_b64_tr_b16 a[106:107], v32 offset:59392\n"
    "v_mfma_f32_16x16x32_bf16 a[0:3], a[80:83], v[144:147], a[0:3]\n"
    "ds_read_b64_tr_b16 a[108:109], v33 offset:51200\n"
    "v_mfma_f32_16x16x32_bf16 a[4:7], a[80:83], v[148:151], a[4:7]\n"
    "ds_read_b64_tr_b16 a[110:111], v33 offset:59392\n"
    "s_waitcnt lgkmcnt(10)\n"
    "v_mfma_f32_16x16x32_bf16 a[8:11], a[84:87], v[144:147], a[8:11]\n"
    "ds_read_b64_tr_b16 a[112:113], v30 offset:52224\n"
    "v_mfma_f32_16x16x32_bf16 a[12:15], a[84:87], v[148:151], a[12:15]\n"
    "ds_read_b64_tr_b16 a[114:115], v30 offset:60416\n"
    "v_mfma_f32_16x16x32_bf16 a[16:19], a[88:91], v[144:147], a[16:19]\n"
    "buffer_load_dwordx4 v36, s[16:19], s40 offen offset:2048 nt lds\n"
    "v_mfma_f32_16x16x32_bf16 a[20:23], a[88:91], v[148:151], a[20:23]\n"
    "v_add_u32 v30, 0x10000, v30\n"
    "s_waitcnt lgkmcnt(8)\n"
    "v_mfma_f32_16x16x32_bf16 a[24:27], a[92:95], v[144:147], a[24:27]\n"
    "ds_read_b64_tr_b16 a[116:117], v31 offset:52224\n"
    "ds_read_b64_tr_b16 a[118:119], v31 offset:60416\n"
    "v_mfma_f32_16x16x32_bf16 a[28:31], a[92:95], v[148:151], a[28:31]\n"
    "ds_read_b64_tr_b16 a[120:121], v32 offset:52224\n"
    "ds_read_b64_tr_b16 a[122:123], v32 offset:60416\n"
    "v_mfma_f32_16x16x32_bf16 a[0:3], a[96:99], v[152:155], a[0:3]\n"
    "ds_read_b64_tr_b16 a[124:125], v33 offset:52224\n"
    "s_waitcnt lgkmcnt(9)\n"
    "v_mfma_f32_16x16x32_bf16 a[4:7], a[96:99], v[156:159], a[4:7]\n"
    "ds_read_b64_tr_b16 a[126:127], v33 offset:60416\n"
    "s_barrier\n"
    "v_mfma_f32_16x16x32_bf16 a[8:11], a[100:103], v[152:155], a[8:11]\n"
    "ds_read_b64_tr_b16 a[64:65], v30 offset:0\n"
    "v_add_u32 v31, 0x10000, v31\n"
    "v_mfma_f32_16x16x32_bf16 a[12:15], a[100:103], v[156:159], a[12:15]\n"
    "ds_read_b64_tr_b16 a[66:67], v30 offset:8192\n"
    "v_add_u32 v32, 0x10000, v32\n"
    "v_mfma_f32_16x16x32_bf16 a[16:19], a[104:107], v[152:155], a[16:19]\n"
    "buffer_load_dwordx4 v37, s[16:19], s40 offen offset:3072 nt lds\n"
    "v_mfma_f32_16x16x32_bf16 a[20:23], a[104:107], v[156:159], a[20:23]\n"
    "v_add_u32 v33, 0x10000, v33\n"
    "s_waitcnt lgkmcnt(8)\n"
    "v_mfma_f32_16x16x32_bf16 a[24:27], a[108:111], v[152:155], a[24:27]\n"
    "ds_read_b64_tr_b16 a[68:69], v31 offset:0\n"
    "ds_read_b64_tr_b16 a[70:71], v31 offset:8192\n"
    "v_mfma_f32_16x16x32_bf16 a[28:31], a[108:111], v[156:159], a[28:31]\n"
    "ds_read_b64_tr_b16 a[72:73], v32 offset:0\n"
    "ds_read_b64_tr_b16 a[74:75], v32 offset:8192\n"
    "v_mfma_f32_16x16x32_bf16 a[0:3], a[112:115], v[160:163], a[0:3]\n"
    "ds_read_b64_tr_b16 a[76:77], v33 offset:0\n"
    "s_add_u32 m0, s28, 0x4000\n"
    "v_mfma_f32_16x16x32_bf16 a[4:7], a[112:115], v[164:167], a[4:7]\n"
    "ds_read_b64_tr_b16 a[78:79], v33 offset:8192\n"
    "s_waitcnt lgkmcnt(10)\n"
    "v_mfma_f32_16x16x32_bf16 a[8:11], a[116:119], v[160:163], a[8:11]\n"
    "ds_read_b64_tr_b16 a[80:81], v30 offset:1024\n"
    "s_add_u32 s40, s31, 0x22000\n"
    "v_mfma_f32_16x16x32_bf16 a[12:15], a[116:119], v[164:167], a[12:15]\n"
    "ds_read_b64_tr_b16 a[82:83], v30 offset:9216\n"
    "v_mfma_f32_16x16x32_bf16 a[16:19], a[120:123], v[160:163], a[16:19]\n"
    "buffer_load_dwordx4 v34, s[16:19], s40 offen offset:0 nt lds\n"
    "v_mfma_f32_16x16x32_bf16 a[20:23], a[120:123], v[164:167], a[20:23]\n"
    "s_waitcnt lgkmcnt(8)\n"
    "v_mfma_f32_16x16x32_bf16 a[24:27], a[124:127], v[160:163], a[24:27]\n"
    "ds_read_b64_tr_b16 a[84:85], v31 offset:1024\n"
    "ds_read_b64_tr_b16 a[86:87], v31 offset:9216\n"
    "v_mfma_f32_16x16x32_bf16 a[28:31], a[124:127], v[164:167], a[28:31]\n"
    "ds_read_b64_tr_b16 a[88:89], v32 offset:1024\n"
    "ds_read_b64_tr_b16 a[90:91], v32 offset:9216\n"
    "s_lshl_b32 s38, s36, 1\n"
    "s_sub_u32 s38, 0, s38\n"
    "s_bitcmp1_b32 s7, 13\n"
    "s_cselect_b32 s35, 8, 0\n"
    "s_add_u32 s38, s38, s35\n"
    "s_lshl1_add_u32 s39, s36, s38\n"
    "PL19_%=:\n"
    "s_add_u32 s39, s39, 3\n"
    "s_cmpk_lt_u32 s39, 18\n"
    "s_cbranch_scc1 PL19_%=\n"
    "s_memrealtime s[36:37]\n"
    "v_mfma_f32_16x16x32_bf16 a[0:3], a[64:67], v[168:171], a[0:3]\n"
    "ds_read_b64_tr_b16 a[92:93], v33 offset:1024\n"
    "v_mfma_f32_16x16x32_bf16 a[4:7], a[64:67], v[172:175], a[4:7]\n"
    "ds_read_b64_tr_b16 a[94:95], v33 offset:9216\n"
    "s_waitcnt lgkmcnt(10)\n"
    "v_mfma_f32_16x16x32_bf16 a[8:11], a[68:71], v[168:171], a[8:11]\n"
    "ds_read_b64_tr_b16 a[96:97], v30 offset:2048\n"
    "v_mfma_f32_16x16x32_bf16 a[12:15], a[68:71], v[172:175], a[12:15]\n"
    "ds_read_b64_tr_b16 a[98:99], v30 offset:10240\n"
    "v_mfma_f32_16x16x32_bf16 a[16:19], a[72:75], v[168:171], a[16:19]\n"
    "buffer_load_dwordx4 v35, s[16:19], s40 offen offset:1024 nt lds\n"
    "v_mfma_f32_16x16x32_bf16 a[20:23], a[72:75], v[172:175], a[20:23]\n"
    "s_waitcnt lgkmcnt(8)\n"
    "v_mfma_f32_16x16x32_bf16 a[24:27], a[76:79], v[168:171], a[24:27]\n"
    "ds_read_b64_tr_b16 a[100:101], v31 offset:2048\n"
    "ds_read_b64_tr_b16 a[102:103], v31 offset:10240\n"
    "v_mfma_f32_16x16x32_bf16 a[28:31], a[76:79], v[172:175], a[28:31]\n"
    "ds_read_b64_tr_b16 a[104:105], v32 offset:2048\n"
    "ds_read_b64_tr_b16 a[106:107], v32 offset:10240\n"
    "v_mfma_f32_16x16x32_bf16 a[0:3], a[80:83], v[176:179], a[0:3]\n"
    "ds_read_b64_tr_b16 a[108:109], v33 offset:2048\n"
    "v_mfma_f32_16x16x32_bf16 a[4:7], a[80:83], v[180:183], a[4:7]\n"
    "ds_read_b64_tr_b16 a[110:111], v33 offset:10240\n"
    "s_waitcnt lgkmcnt(10)\n"
    "v_mfma_f32_16x16x32_bf16 a[8:11], a[84:87], v[176:179], a[8:11]\n"
    "ds_read_b64_tr_b16 a[112:113], v30 offset:3072\n"
    "v_mfma_f32_16x16x32_bf16 a[12:15], a[84:87], v[180:183], a[12:15]\n"
    "ds_read_b64_tr_b16 a[114:115], v30 offset:11264\n"
    "v_mfma_f32_16x16x32_bf16 a[16:19], a[88:91], v[176:179], a[16:19]\n"
    "buffer_load_dwordx4 v36, s[16:19], s40 offen offset:2048 nt lds\n"
    "v_mfma_f32_16x16x32_bf16 a[20:23], a[88:91], v[180:183], a[20:23]\n"
    "s_waitcnt lgkmcnt(8)\n"
    "v_mfma_f32_16x16x32_bf16 a[24:27], a[92:95], v[176:179], a[24:27]\n"
    "ds_read_b64_tr_b16 a[116:117], v31 offset:3072\n"
    "ds_read_b64_tr_b16 a[118:119], v31 offset:11264\n"
    "v_mfma_f32_16x16x32_bf16 a[28:31], a[92:95], v[180:183], a[28:31]\n"
    "ds_read_b64_tr_b16 a[120:121], v32 offset:3072\n"
    "ds_read_b64_tr_b16 a[122:123], v32 offset:11264\n"
    "v_mfma_f32_16x16x32_bf16 a[0:3], a[96:99], v[184:187], a[0:3]\n"
    "ds_read_b64_tr_b16 a[124:125], v33 offset:3072\n"
    "s_waitcnt lgkmcnt(9)\n"
    "v_mfma_f32_16x16x32_bf16 a[4:7], a[96:99], v[188:191], a[4:7]\n"
    "ds_read_b64_tr_b16 a[126:127], v33 offset:11264\n"
    "s_barrier\n"
    "v_mfma_f32_16x16x32_bf16 a[8:11], a[100:103], v[184:187], a[8:11]\n"
    "ds_read_b64_tr_b16 a[64:65], v30 offset:16384\n"
    "v_mfma_f32_16x16x32_bf16 a[12:15], a[100:103], v[188:191], a[12:15]\n"
    "ds_read_b64_tr_b16 a[66:67], v30 offset:24576\n"
    "v_mfma_f32_16x16x32_bf16 a[16:19], a[104:107], v[184:187], a[16:19]\n"
    "buffer_load_dwordx4 v37, s[16:19], s40 offen offset:3072 nt lds\n"
    "v_mfma_f32_16x16x32_bf16 a[20:23], a[104:107], v[188:191], a[20:23]\n"
    "s_add_u32 m0, s28, 0x8000\n"
    "s_waitcnt lgkmcnt(8)\n"
    "v_mfma_f32_16x16x32_bf16 a[24:27], a[108:111], v[184:187], a[24:27]\n"
    "ds_read_b64_tr_b16 a[68:69], v31 offset:16384\n"
    "ds_read_b64_tr_b16 a[70:71], v31 offset:24576\n"
    "v_mfma_f32_16x16x32_bf16 a[28:31], a[108:111], v[188:191], a[28:31]\n"
    "ds_read_b64_tr_b16 a[72:73], v32 offset:16384\n"
    "ds_read_b64_tr_b16 a[74:75], v32 offset:24576\n"
    "v_mfma_f32_16x16x32_bf16 a[0:3], a[112:115], v[192:195], a[0:3]\n"
    "ds_read_b64_tr_b16 a[76:77], v33 offset:16384\n"
    "s_add_u32 s40, s31, 0x40000\n"
    "v_mfma_f32_16x16x32_bf16 a[4:7], a[112:115], v[196:199], a[4:7]\n"
    "ds_read_b64_tr_b16 a[78:79], v33 offset:24576\n"
    "s_waitcnt lgkmcnt(10)\n"
    "v_mfma_f32_16x16x32_bf16 a[8:11], a[116:119], v[192:195], a[8:11]\n"
    "ds_read_b64_tr_b16 a[80:81], v30 offset:17408\n"
    "v_mfma_f32_16x16x32_bf16 a[12:15], a[116:119], v[196:199], a[12:15]\n"
    "ds_read_b64_tr_b16 a[82:83], v30 offset:25600\n"
    "v_mfma_f32_16x16x32_bf16 a[16:19], a[120:123], v[192:195], a[16:19]\n"
    "buffer_load_dwordx4 v34, s[16:19], s40 offen offset:0 nt lds\n"
    "v_mfma_f32_16x16x32_bf16 a[20:23], a[120:123], v[196:199], a[20:23]\n"
    "s_waitcnt lgkmcnt(8)\n"
    "v_mfma_f32_16x16x32_bf16 a[24:27], a[124:127], v[192:195], a[24:27]\n"
    "ds_read_b64_tr_b16 a[84:85], v31 offset:17408\n"
    "ds_read_b64_tr_b16 a[86:87], v31 offset:25600\n"
    "v_mfma_f32_16x16x32_bf16 a[28:31], a[124:127], v[196:199], a[28:31]\n"
    "ds_read_b64_tr_b16 a[88:89], v32 offset:17408\n"
    "ds_read_b64_tr_b16 a[90:91], v32 offset:25600\n"
    "s_add_u32 s38, s38, s35\n"
    "s_lshl1_add_u32 s39, s36, s38\n"
    "PL20_%=:\n"
    "s_add_u32 s39, s39, 3\n"
    "s_cmpk_lt_u32 s39, 101\n"
    "s_cbranch_scc1 PL20_%=\n"
    "s_memrealtime s[36:37]\n"
    "v_mfma_f32_16x16x32_bf16 a[0:3], a[64:67], v[200:203], a[0:3]\n"
    "ds_read_b64_tr_b16 a[92:93], v33 offset:17408\n"
    "v_mfma_f32_16x16x32_bf16 a[4:7], a[64:67], v[204:207], a[4:7]\n"
    "ds_read_b64_tr_b16 a[94:95], v33 offset:25600\n"
    "s_waitcnt lgkmcnt(10)\n"
    "v_mfma_f32_16x16x32_bf16 a[8:11], a[68:71], v[200:203], a[8:11]\n"
    "ds_read_b64_tr_b16 a[96:97], v30 offset:18432\n"
    "v_mfma_f32_16x16x32_bf16 a[12:15], a[68:71], v[204:207], a[12:15]\n"
    "ds_read_b64_tr_b16 a[98:99], v30 offset:26624\n"
    "v_mfma_f32_16x16x32_bf16 a[16:19], a[72:75], v[200:203], a[16:19]\n"
    "buffer_load_dwordx4 v35, s[16:19], s40 offen offset:1024 nt lds\n"
    "v_mfma_f32_16x16x32_bf16 a[20:23], a[72:75], v[204:207], a[20:23]\n"
    "s_waitcnt lgkmcnt(8)\n"
    "v_mfma_f32_16x16x32_bf16 a[24:27], a[76:79], v[200:203], a[24:27]\n"
    "ds_read_b64_tr_b16 a[100:101], v31 offset:18432\n"
    "ds_read_b64_tr_b16 a[102:103], v31 offset:26624\n"
    "v_mfma_f32_16x16x32_bf16 a[28:31], a[76:79], v[204:207], a[28:31]\n"
    "ds_read_b64_tr_b16 a[104:105], v32 offset:18432\n"
    "ds_read_b64_tr_b16 a[106:107], v32 offset:26624\n"
    "v_mfma_f32_16x16x32_bf16 a[0:3], a[80:83], v[208:211], a[0:3]\n"
    "ds_read_b64_tr_b16 a[108:109], v33 offset:18432\n"
    "v_mfma_f32_16x16x32_bf16 a[4:7], a[80:83], v[212:215], a[4:7]\n"
    "ds_read_b64_tr_b16 a[110:111], v33 offset:26624\n"
    "s_waitcnt lgkmcnt(10)\n"
    "v_mfma_f32_16x16x32_bf16 a[8:11], a[84:87], v[208:211], a[8:11]\n"
    "ds_read_b64_tr_b16 a[112:113], v30 offset:19456\n"
    "v_mfma_f32_16x16x32_bf16 a[12:15], a[84:87], v[212:215], a[12:15]\n"
    "ds_read_b64_tr_b16 a[114:115], v30 offset:27648\n"
    "v_mfma_f32_16x16x32_bf16 a[16:19], a[88:91], v[208:211], a[16:19]\n"
    "buffer_load_dwordx4 v36, s[16:19], s40 offen offset:2048 nt lds\n"
    "v_mfma_f32_16x16x32_bf16 a[20:23], a[88:91], v[212:215], a[20:23]\n"
    "s_waitcnt lgkmcnt(8)\n"
    "v_mfma_f32_16x16x32_bf16 a[24:27], a[92:95], v[208:211], a[24:27]\n"
    "ds_read_b64_tr_b16 a[116:117], v31 offset:19456\n"
    "ds_read_b64_tr_b16 a[118:119], v31 offset:27648\n"
    "v_mfma_f32_16x16x32_bf16 a[28:31], a[92:95], v[212:215], a[28:31]\n"
    "ds_read_b64_tr_b16 a[120:121], v32 offset:19456\n"
    "ds_read_b64_tr_b16 a[122:123], v32 offset:27648\n"
    "v_mfma_f32_16x16x32_bf16 a[0:3], a[96:99], v[216:219], a[0:3]\n"
    "ds_read_b64_tr_b16 a[124:125], v33 offset:19456\n"
    "s_waitcnt vmcnt(28) lgkmcnt(9)\n"
    "v_mfma_f32_16x16x32_bf16 a[4:7], a[96:99], v[220:223], a[4:7]\n"
    "ds_read_b64_tr_b16 a[126:127], v33 offset:27648\n"
    "s_barrier\n"
    "v_mfma_f32_16x16x32_bf16 a[8:11], a[100:103], v[216:219], a[8:11]\n"
    "ds_read_b64_tr_b16 a[64:65], v30 offset:32768\n"
    "STUB7_%=:\n"
    "s_cbranch_execz WRET_%=\n"
    "v_mfma_f32_16x16x32_bf16 a[12:15], a[100:103], v[220:223], a[12:15]\n"
    "ds_read_b64_tr_b16 a[66:67], v30 offset:40960\n"
    "v_mfma_f32_16x16x32_bf16 a[16:19], a[104:107], v[216:219], a[16:19]\n"
    "buffer_load_dwordx4 v37, s[16:19], s40 offen offset:3072 nt lds\n"
    "v_mfma_f32_16x16x32_bf16 a[20:23], a[104:107], v[220:223], a[20:23]\n"
    "s_add_u32 m0, s28, 0xc000\n"
    "s_waitcnt lgkmcnt(8)\n"
    "v_mfma_f32_16x16x32_bf16 a[24:27], a[108:111], v[216:219], a[24:27]\n"
    "ds_read_b64_tr_b16 a[68:69], v31 offset:32768\n"
    "ds_read_b64_tr_b16 a[70:71], v31 offset:40960\n"
    "v_mfma_f32_16x16x32_bf16 a[28:31], a[108:111], v[220:223], a[28:31]\n"
    "ds_read_b64_tr_b16 a[72:73], v32 offset:32768\n"
    "ds_read_b64_tr_b16 a[74:75], v32 offset:40960\n"
    "v_mfma_f32_16x16x32_bf16 a[0:3], a[112:115], v[224:227], a[0:3]\n"
    "ds_read_b64_tr_b16 a[76:77], v33 offset:32768\n"
    "s_add_u32 s40, s31, 0x60000\n"
    "v_mfma_f32_16x16x32_bf16 a[4:7], a[112:115], v[228:231], a[4:7]\n"
    "ds_read_b64_tr_b16 a[78:79], v33 offset:40960\n"
    "s_waitcnt lgkmcnt(10)\n"
    "v_mfma_f32_16x16x32_bf16 a[8:11], a[116:119], v[224:227], a[8:11]\n"
    "ds_read_b64_tr_b16 a[80:81], v30 offset:33792\n"
    "v_mfma_f32_16x16x32_bf16 a[12:15], a[116:119], v[228:231], a[12:15]\n"
    "ds_read_b64_tr_b16 a[82:83], v30 offset:41984\n"
    "v_mfma_f32_16x16x32_bf16 a[16:19], a[120:123], v[224:227], a[16:19]\n"
    "buffer_load_dwordx4 v34, s[16:19], s40 offen offset:0 nt lds\n"
    "v_mfma_f32_16x16x32_bf16 a[20:23], a[120:123], v[228:231], a[20:23]\n"
    "s_waitcnt lgkmcnt(8)\n"
    "v_mfma_f32_16x16x32_bf16 a[24:27], a[124:127], v[224:227], a[24:27]\n"
    "ds_read_b64_tr_b16 a[84:85], v31 offset:33792\n"
    "ds_read_b64_tr_b16 a[86:87], v31 offset:41984\n"
    "v_mfma_f32_16x16x32_bf16 a[28:31], a[124:127], v[228:231], a[28:31]\n"
    "ds_read_b64_tr_b16 a[88:89], v32 offset:33792\n"
    "ds_read_b64_tr_b16 a[90:91], v32 offset:41984\n"
    "s_add_u32 s38, s38, s35\n"
    "s_lshl1_add_u32 s39, s36, s38\n"
    "PL21_%=:\n"
    "s_add_u32 s39, s39, 3\n"
    "s_cmpk_lt_u32 s39, 184\n"
    "s_cbranch_scc1 PL21_%=\n"
    "s_memrealtime s[36:37]\n"
    "v_mfma_f32_16x16x32_bf16 a[0:3], a[64:67], v[232:235], a[0:3]\n"
    "ds_read_b64_tr_b16 a[92:93], v33 offset:33792\n"
    "v_mfma_f32_16x16x32_bf16 a[4:7], a[64:67], v[236:239], a[4:7]\n"
    "ds_read_b64_tr_b16 a[94:95], v33 offset:41984\n"
    "s_waitcnt lgkmcnt(10)\n"
    "v_mfma_f32_16x16x32_bf16 a[8:11], a[68:71], v[232:235], a[8:11]\n"
    "ds_read_b64_tr_b16 a[96:97], v30 offset:34816\n"
    "v_mfma_f32_16x16x32_bf16 a[12:15], a[68:71], v[236:239], a[12:15]\n"
    "ds_read_b64_tr_b16 a[98:99], v30 offset:43008\n"
    "v_mfma_f32_16x16x32_bf16 a[16:19], a[72:75], v[232:235], a[16:19]\n"
    "buffer_load_dwordx4 v35, s[16:19], s40 offen offset:1024 nt lds\n"
    "v_mfma_f32_16x16x32_bf16 a[20:23], a[72:75], v[236:239], a[20:23]\n"
    "s_waitcnt lgkmcnt(8)\n"
    "v_mfma_f32_16x16x32_bf16 a[24:27], a[76:79], v[232:235], a[24:27]\n"
    "ds_read_b64_tr_b16 a[100:101], v31 offset:34816\n"
    "ds_read_b64_tr_b16 a[102:103], v31 offset:43008\n"
    "v_mfma_f32_16x16x32_bf16 a[28:31], a[76:79], v[236:239], a[28:31]\n"
    "ds_read_b64_tr_b16 a[104:105], v32 offset:34816\n"
    "ds_read_b64_tr_b16 a[106:107], v32 offset:43008\n"
    "v_mfma_f32_16x16x32_bf16 a[0:3], a[80:83], v[240:243], a[0:3]\n"
    "ds_read_b64_tr_b16 a[108:109], v33 offset:34816\n"
    "v_mfma_f32_16x16x32_bf16 a[4:7], a[80:83], v[244:247], a[4:7]\n"
    "ds_read_b64_tr_b16 a[110:111], v33 offset:43008\n"
    "s_waitcnt lgkmcnt(10)\n"
    "v_mfma_f32_16x16x32_bf16 a[8:11], a[84:87], v[240:243], a[8:11]\n"
    "ds_read_b64_tr_b16 a[112:113], v30 offset:35840\n"
    "v_mfma_f32_16x16x32_bf16 a[12:15], a[84:87], v[244:247], a[12:15]\n"
    "ds_read_b64_tr_b16 a[114:115], v30 offset:44032\n"
    "v_mfma_f32_16x16x32_bf16 a[16:19], a[88:91], v[240:243], a[16:19]\n"
    "buffer_load_dwordx4 v36, s[16:19], s40 offen offset:2048 nt lds\n"
    "v_mfma_f32_16x16x32_bf16 a[20:23], a[88:91], v[244:247], a[20:23]\n"
    "s_waitcnt lgkmcnt(8)\n"
    "v_mfma_f32_16x16x32_bf16 a[24:27], a[92:95], v[240:243], a[24:27]\n"
    "ds_read_b64_tr_b16 a[116:117], v31 offset:35840\n"
    "ds_read_b64_tr_b16 a[118:119], v31 offset:44032\n"
    "v_mfma_f32_16x16x32_bf16 a[28:31], a[92:95], v[244:247], a[28:31]\n"
    "ds_read_b64_tr_b16 a[120:121], v32 offset:35840\n"
    "ds_read_b64_tr_b16 a[122:123], v32 offset:44032\n"
    "v_mfma_f32_16x16x32_bf16 a[0:3], a[96:99], v[248:251], a[0:3]\n"
    "ds_read_b64_tr_b16 a[124:125], v33 offset:35840\n"
    "s_waitcnt vmcnt(27) lgkmcnt(9)\n"
    "v_accvgpr_read_b32 v29, a214\n"
    "v_mfma_f32_16x16x32_bf16 a[4:7], a[96:99], v[252:255], a[4:7]\n"
    "ds_read_b64_tr_b16 a[126:127], v33 offset:44032\n"
    "s_barrier\n"
    "v_mfma_f32_16x16x32_bf16 a[8:11], a[100:103], v[248:251], a[8:11]\n"
    "ds_read_b64_tr_b16 a[64:65], v30 offset:49152\n"
    "v_add_u32 v29, 1, v29\n"
    "s_waitcnt vmcnt(18)\n"
    "v_mfma_f32_16x16x32_bf16 a[12:15], a[100:103], v[252:255], a[12:15]\n"
    "ds_read_b64_tr_b16 a[66:67], v30 offset:57344\n"
    "buffer_store_dword v29, v27, s[24:27], s7 offen offset:1024 sc1\n"
    "v_mfma_f32_16x16x32_bf16 a[16:19], a[104:107], v[248:251], a[16:19]\n"
    "buffer_load_dwordx4 v37, s[16:19], s40 offen offset:3072 nt lds\n"
    "v_mfma_f32_16x16x32_bf16 a[20:23], a[104:107], v[252:255], a[20:23]\n"
    "s_add_u32 m0, s28, 0x10000\n"
    "s_waitcnt lgkmcnt(8)\n"
    "v_mfma_f32_16x16x32_bf16 a[24:27], a[108:111], v[248:251], a[24:27]\n"
    "ds_read_b64_tr_b16 a[68:69], v31 offset:49152\n"
    "ds_read_b64_tr_b16 a[70:71], v31 offset:57344\n"
    "v_mfma_f32_16x16x32_bf16 a[28:31], a[108:111], v[252:255], a[28:31]\n"
    "ds_read_b64_tr_b16 a[72:73], v32 offset:49152\n"
    "ds_read_b64_tr_b16 a[74:75], v32 offset:57344\n"
    "v_mfma_f32_16x16x32_bf16 a[0:3], a[112:115], a[160:163], a[0:3]\n"
    "ds_read_b64_tr_b16 a[76:77], v33 offset:49152\n"
    "s_add_u32 s40, s31, 0x42000\n"
    "v_mfma_f32_16x16x32_bf16 a[4:7], a[112:115], a[164:167], a[4:7]\n"
    "ds_read_b64_tr_b16 a[78:79], v33 offset:57344\n"
    "s_waitcnt lgkmcnt(10)\n"
    "v_mfma_f32_16x16x32_bf16 a[8:11], a[116:119], a[160:163], a[8:11]\n"
    "ds_read_b64_tr_b16 a[80:81], v30 offset:50176\n"
    "v_mfma_f32_16x16x32_bf16 a[12:15], a[116:119], a[164:167], a[12:15]\n"
    "ds_read_b64_tr_b16 a[82:83], v30 offset:58368\n"
    "v_mfma_f32_16x16x32_bf16 a[16:19], a[120:123], a[160:163], a[16:19]\n"
    "buffer_load_dwordx4 v34, s[16:19], s40 offen offset:0 nt lds\n"
    "v_mfma_f32_16x16x32_bf16 a[20:23], a[120:123], a[164:167], a[20:23]\n"
    "s_waitcnt lgkmcnt(8)\n"
    "v_mfma_f32_16x16x32_bf16 a[24:27], a[124:127], a[160:163], a[24:27]\n"
    "ds_read_b64_tr_b16 a[84:85], v31 offset:50176\n"
    "ds_read_b64_tr_b16 a[86:87], v31 offset:58368\n"
    "v_mfma_f32_16x16x32_bf16 a[28:31], a[124:127], a[164:167], a[28:31]\n"
    "ds_read_b64_tr_b16 a[88:89], v32 offset:50176\n"
    "ds_read_b64_tr_b16 a[90:91], v32 offset:58368\n"
    "s_add_u32 s38, s38, s35\n"
    "s_lshl1_add_u32 s39, s36, s38\n"
    "PL22_%=:\n"
    "s_add_u32 s39, s39, 3\n"
    "s_cmpk_lt_u32 s39, 267\n"
    "s_cbranch_scc1 PL22_%=\n"
    "s_memrealtime s[36:37]\n"
    "v_mfma_f32_16x16x32_bf16 a[0:3], a[64:67], a[168:171], a[0:3]\n"
    "ds_read_b64_tr_b16 a[92:93], v33 offset:50176\n"
    "v_mfma_f32_16x16x32_bf16 a[4:7], a[64:67], a[172:175], a[4:7]\n"
    "ds_read_b64_tr_b16 a[94:95], v33 offset:58368\n"
    "s_waitcnt lgkmcnt(10)\n"
    "v_mfma_f32_16x16x32_bf16 a[8:11], a[68:71], a[168:171], a[8:11]\n"
    "ds_read_b64_tr_b16 a[96:97], v30 offset:51200\n"
    "v_mfma_f32_16x16x32_bf16 a[12:15], a[68:71], a[172:175], a[12:15]\n"
    "ds_read_b64_tr_b16 a[98:99], v30 offset:59392\n"
    "v_mfma_f32_16x16x32_bf16 a[16:19], a[72:75], a[168:171], a[16:19]\n"
    "buffer_load_dwordx4 v35, s[16:19], s40 offen offset:1024 nt lds\n"
    "v_mfma_f32_16x16x32_bf16 a[20:23], a[72:75], a[172:175], a[20:23]\n"
    "s_waitcnt lgkmcnt(8)\n"
    "v_mfma_f32_16x16x32_bf16 a[24:27], a[76:79], a[168:171], a[24:27]\n"
    "ds_read_b64_tr_b16 a[100:101], v31 offset:51200\n"
    "ds_read_b64_tr_b16 a[102:103], v31 offset:59392\n"
    "v_mfma_f32_16x16x32_bf16 a[28:31], a[76:79], a[172:175], a[28:31]\n"
    "ds_read_b64_tr_b16 a[104:105], v32 offset:51200\n"
    "ds_read_b64_tr_b16 a[106:107], v32 offset:59392\n"
    "v_mfma_f32_16x16x32_bf16 a[0:3], a[80:83], a[176:179], a[0:3]\n"
    "ds_read_b64_tr_b16 a[108:109], v33 offset:51200\n"
    "v_mfma_f32_16x16x32_bf16 a[4:7], a[80:83], a[180:183], a[4:7]\n"
    "ds_read_b64_tr_b16 a[110:111], v33 offset:59392\n"
    "s_waitcnt lgkmcnt(10)\n"
    "v_mfma_f32_16x16x32_bf16 a[8:11], a[84:87], a[176:179], a[8:11]\n"
    "ds_read_b64_tr_b16 a[112:113], v30 offset:52224\n"
    "v_mfma_f32_16x16x32_bf16 a[12:15], a[84:87], a[180:183], a[12:15]\n"
    "ds_read_b64_tr_b16 a[114:115], v30 offset:60416\n"
    "v_mfma_f32_16x16x32_bf16 a[16:19], a[88:91], a[176:179], a[16:19]\n"
    "buffer_load_dwordx4 v36, s[16:19], s40 offen offset:2048 nt lds\n"
    "v_mfma_f32_16x16x32_bf16 a[20:23], a[88:91], a[180:183], a[20:23]\n"
    "v_add_u32 v30, 0x10000, v30\n"
    "s_waitcnt lgkmcnt(8)\n"
    "v_mfma_f32_16x16x32_bf16 a[24:27], a[92:95], a[176:179], a[24:27]\n"
    "ds_read_b64_tr_b16 a[116:117], v31 offset:52224\n"
    "ds_read_b64_tr_b16 a[118:119], v31 offset:60416\n"
    "v_mfma_f32_16x16x32_bf16 a[28:31], a[92:95], a[180:183], a[28:31]\n"
    "ds_read_b64_tr_b16 a[120:121], v32 offset:52224\n"
    "ds_read_b64_tr_b16 a[122:123], v32 offset:60416\n"
    "v_mfma_f32_16x16x32_bf16 a[0:3], a[96:99], a[184:187], a[0:3]\n"
    "ds_read_b64_tr_b16 a[124:125], v33 offset:52224\n"
    "s_waitcnt lgkmcnt(9)\n"
    "v_mfma_f32_16x16x32_bf16 a[4:7], a[96:99], a[188:191], a[4:7]\n"
    "ds_read_b64_tr_b16 a[126:127], v33 offset:60416\n"
    "s_barrier\n"
    "v_mfma_f32_16x16x32_bf16 a[8:11], a[100:103], a[184:187], a[8:11]\n"
    "ds_read_b64_tr_b16 a[64:65], v30 offset:0\n"
    "v_add_u32 v31, 0x10000, v31\n"
    "v_mfma_f32_16x16x32_bf16 a[12:15], a[100:103], a[188:191], a[12:15]\n"
    "ds_read_b64_tr_b16 a[66:67], v30 offset:8192\n"
    "v_add_u32 v32, 0x10000, v32\n"
    "v_mfma_f32_16x16x32_bf16 a[16:19], a[104:107], a[184:187], a[16:19]\n"
    "buffer_load_dwordx4 v37, s[16:19], s40 offen offset:3072 nt lds\n"
    "v_mfma_f32_16x16x32_bf16 a[20:23], a[104:107], a[188:191], a[20:23]\n"
    "v_add_u32 v33, 0x10000, v33\n"
    "s_waitcnt lgkmcnt(8)\n"
    "v_mfma_f32_16x16x32_bf16 a[24:27], a[108:111], a[184:187], a[24:27]\n"
    "ds_read_b64_tr_b16 a[68:69], v31 offset:0\n"
    "ds_read_b64_tr_b16 a[70:71], v31 offset:8192\n"
    "v_mfma_f32_16x16x32_bf16 a[28:31], a[108:111], a[188:191], a[28:31]\n"
    "ds_read_b64_tr_b16 a[72:73], v32 offset:0\n"
    "ds_read_b64_tr_b16 a[74:75], v32 offset:8192\n"
    "v_mfma_f32_16x16x32_bf16 a[0:3], a[112:115], a[192:195], a[0:3]\n"
    "ds_read_b64_tr_b16 a[76:77], v33 offset:0\n"
    "s_add_u32 m0, s28, 0x14000\n"
    "v_mfma_f32_16x16x32_bf16 a[4:7], a[112:115], a[196:199], a[4:7]\n"
    "ds_read_b64_tr_b16 a[78:79], v33 offset:8192\n"
    "s_waitcnt lgkmcnt(10)\n"
    "v_mfma_f32_16x16x32_bf16 a[8:11], a[116:119], a[192:195], a[8:11]\n"
    "ds_read_b64_tr_b16 a[80:81], v30 offset:1024\n"
    "s_add_u32 s40, s31, 0x62000\n"
    "v_mfma_f32_16x16x32_bf16 a[12:15], a[116:119], a[196:199], a[12:15]\n"
    "ds_read_b64_tr_b16 a[82:83], v30 offset:9216\n"
    "v_mfma_f32_16x16x32_bf16 a[16:19], a[120:123], a[192:195], a[16:19]\n"
    "buffer_load_dwordx4 v34, s[16:19], s40 offen offset:0 nt lds\n"
    "v_mfma_f32_16x16x32_bf16 a[20:23], a[120:123], a[196:199], a[20:23]\n"
    "s_waitcnt lgkmcnt(8)\n"
    "v_mfma_f32_16x16x32_bf16 a[24:27], a[124:127], a[192:195], a[24:27]\n"
    "ds_read_b64_tr_b16 a[84:85], v31 offset:1024\n"
    "ds_read_b64_tr_b16 a[86:87], v31 offset:9216\n"
    "v_mfma_f32_16x16x32_bf16 a[28:31], a[124:127], a[196:199], a[28:31]\n"
    "ds_read_b64_tr_b16 a[88:89], v32 offset:1024\n"
    "ds_read_b64_tr_b16 a[90:91], v32 offset:9216\n"
    "s_add_u32 s38, s38, s35\n"
    "s_lshl1_add_u32 s39, s36, s38\n"
    "PL23_%=:\n"
    "s_add_u32 s39, s39, 3\n"
    "s_cmpk_lt_u32 s39, 350\n"
    "s_cbranch_scc1 PL23_%=\n"
    "s_memrealtime s[36:37]\n"
    "v_mfma_f32_16x16x32_bf16 a[128:131], a[64:67], v[40:43], 0\n"
    "ds_read_b64_tr_b16 a[92:93], v33 offset:1024\n"
    "v_mfma_f32_16x16x32_bf16 a[132:135], a[64:67], v[44:47], 0\n"
    "ds_read_b64_tr_b16 a[94:95], v33 offset:9216\n"
    "v_accvgpr_read_b32 v0, a0\n"
    "s_waitcnt lgkmcnt(10)\n"
    "v_mfma_f32_16x16x32_bf16 a[136:139], a[68:71], v[40:43], 0\n"
    "ds_read_b64_tr_b16 a[96:97], v30 offset:2048\n"
    "v_accvgpr_read_b32 v1, a1\n"
    "v_accvgpr_read_b32 v2, a2\n"
    "v_mfma_f32_16x16x32_bf16 a[140:143], a[68:71], v[44:47], 0\n"
    "ds_read_b64_tr_b16 a[98:99], v30 offset:10240\n"
    "v_accvgpr_read_b32 v3, a3\n"
    "v_accvgpr_read_b32 v4, a4\n"
    "v_mfma_f32_16x16x32_bf16 a[144:147], a[72:75], v[40:43], 0\n"
    "buffer_load_dwordx4 v35, s[16:19], s40 offen offset:1024 nt lds\n"
    "v_accvgpr_read_b32 v5, a5\n"
    "v_mfma_f32_16x16x32_bf16 a[148:151], a[72:75], v[44:47], 0\n"
    "v_accvgpr_read_b32 v6, a6\n"
    "v_accvgpr_read_b32 v7, a7\n"
    "s_waitcnt lgkmcnt(8)\n"
    "v_mfma_f32_16x16x32_bf16 a[152:155], a[76:79], v[40:43], 0\n"
    "ds_read_b64_tr_b16 a[100:101], v31 offset:2048\n"
    "ds_read_b64_tr_b16 a[102:103], v31 offset:10240\n"
    "v_cvt_pk_bf16_f32 v8, v0, v1\n"
    "v_mfma_f32_16x16x32_bf16 a[156:159], a[76:79], v[44:47], 0\n"
    "ds_read_b64_tr_b16 a[104:105], v32 offset:2048\n"
    "ds_read_b64_tr_b16 a[106:107], v32 offset:10240\n"
    "v_cvt_pk_bf16_f32 v9, v2, v3\n"
    "v_mfma_f32_16x16x32_bf16 a[128:131], a[80:83], v[48:51], a[128:131]\n"
    "ds_read_b64_tr_b16 a[108:109], v33 offset:2048\n"
    "v_cvt_pk_bf16_f32 v10, v4, v5\n"
    "v_cvt_pk_bf16_f32 v11, v6, v7\n"
    "v_mfma_f32_16x16x32_bf16 a[132:135], a[80:83], v[52:55], a[132:135]\n"
    "ds_read_b64_tr_b16 a[110:111], v33 offset:10240\n"
    "s_waitcnt lgkmcnt(10)\n"
    "v_mfma_f32_16x16x32_bf16 a[136:139], a[84:87], v[48:51], a[136:139]\n"
    "ds_read_b64_tr_b16 a[112:113], v30 offset:3072\n"
    "v_mfma_f32_16x16x32_bf16 a[140:143], a[84:87], v[52:55], a[140:143]\n"
    "ds_read_b64_tr_b16 a[114:115], v30 offset:11264\n"
    "v_mfma_f32_16x16x32_bf16 a[144:147], a[88:91], v[48:51], a[144:147]\n"
    "buffer_store_dwordx4 v[8:11], v24, s[20:23], s30 offen offset:128 sc1\n"
    "v_accvgpr_read_b32 v0, a8\n"
    "v_mfma_f32_16x16x32_bf16 a[148:151], a[88:91], v[52:55], a[148:151]\n"
    "v_accvgpr_read_b32 v1, a9\n"
    "v_accvgpr_read_b32 v2, a10\n"
    "s_waitcnt lgkmcnt(8)\n"
    "v_mfma_f32_16x16x32_bf16 a[152:155], a[92:95], v[48:51], a[152:155]\n"
    "ds_read_b64_tr_b16 a[116:117], v31 offset:3072\n"
    "ds_read_b64_tr_b16 a[118:119], v31 offset:11264\n"
    "v_accvgpr_read_b32 v3, a11\n"
    "v_mfma_f32_16x16x32_bf16 a[156:159], a[92:95], v[52:55], a[156:159]\n"
    "ds_read_b64_tr_b16 a[120:121], v32 offset:3072\n"
    "ds_read_b64_tr_b16 a[122:123], v32 offset:11264\n"
    "v_accvgpr_read_b32 v4, a12\n"
    "v_mfma_f32_16x16x32_bf16 a[128:131], a[96:99], v[56:59], a[128:131]\n"
    "ds_read_b64_tr_b16 a[124:125], v33 offset:3072\n"
    "s_waitcnt vmcnt(24) lgkmcnt(9)\n"
    "v_accvgpr_read_b32 v5, a13\n"
    "v_mfma_f32_16x16x32_bf16 a[132:135], a[96:99], v[60:63], a[132:135]\n"
    "ds_read_b64_tr_b16 a[126:127], v33 offset:11264\n"
    "s_barrier\n"
    "v_mfma_f32_16x16x32_bf16 a[136:139], a[100:103], v[56:59], a[136:139]\n"
    "ds_read_b64_tr_b16 a[64:65], v30 offset:16384\n"
    "v_accvgpr_read_b32 v6, a14\n"
    "v_accvgpr_read_b32 v7, a15\n"
    "v_mfma_f32_16x16x32_bf16 a[140:143], a[100:103], v[60:63], a[140:143]\n"
    "ds_read_b64_tr_b16 a[66:67], v30 offset:24576\n"
    "v_cvt_pk_bf16_f32 v12, v0, v1\n"
    "v_cvt_pk_bf16_f32 v13, v2, v3\n"
    "v_mfma_f32_16x16x32_bf16 a[144:147], a[104:107], v[56:59], a[144:147]\n"
    "buffer_load_dwordx4 v36, s[16:19], s40 offen offset:2048 nt lds\n"
    "v_cvt_pk_bf16_f32 v14, v4, v5\n"
    "v_mfma_f32_16x16x32_bf16 a[148:151], a[104:107], v[60:63], a[148:151]\n"
    "v_cvt_pk_bf16_f32 v15, v6, v7\n"
    "s_waitcnt lgkmcnt(8)\n"
    "v_mfma_f32_16x16x32_bf16 a[152:155], a[108:111], v[56:59], a[152:155]\n"
    "ds_read_b64_tr_b16 a[68:69], v31 offset:16384\n"
    "ds_read_b64_tr_b16 a[70:71], v31 offset:24576\n"
    "v_mfma_f32_16x16x32_bf16 a[156:159], a[108:111], v[60:63], a[156:159]\n"
    "ds_read_b64_tr_b16 a[72:73], v32 offset:16384\n"
    "ds_read_b64_tr_b16 a[74:75], v32 offset:24576\n"
    "v_mfma_f32_16x16x32_bf16 a[128:131], a[112:115], v[64:67], a[128:131]\n"
    "ds_read_b64_tr_b16 a[76:77], v33 offset:16384\n"
    "v_mfma_f32_16x16x32_bf16 a[132:135], a[112:115], v[68:71], a[132:135]\n"
    "ds_read_b64_tr_b16 a[78:79], v33 offset:24576\n"
    "s_waitcnt lgkmcnt(10)\n"
    "v_mfma_f32_16x16x32_bf16 a[136:139], a[116:119], v[64:67], a[136:139]\n"
    "ds_read_b64_tr_b16 a[80:81], v30 offset:17408\n"
    "v_mfma_f32_16x16x32_bf16 a[140:143], a[116:119], v[68:71], a[140:143]\n"
    "ds_read_b64_tr_b16 a[82:83], v30 offset:25600\n"
    "v_mfma_f32_16x16x32_bf16 a[144:147], a[120:123], v[64:67], a[144:147]\n"
    "buffer_store_dwordx4 v[12:15], v24, s[20:23], s30 offen offset:640 sc1\n"
    "v_accvgpr_read_b32 v0, a16\n"
    "v_mfma_f32_16x16x32_bf16 a[148:151], a[120:123], v[68:71], a[148:151]\n"
    "v_accvgpr_read_b32 v1, a17\n"
    "v_accvgpr_read_b32 v2, a18\n"
    "s_waitcnt lgkmcnt(8)\n"
    "v_mfma_f32_16x16x32_bf16 a[152:155], a[124:127], v[64:67], a[152:155]\n"
    "ds_read_b64_tr_b16 a[84:85], v31 offset:17408\n"
    "ds_read_b64_tr_b16 a[86:87], v31 offset:25600\n"
    "v_accvgpr_read_b32 v3, a19\n"
    "v_mfma_f32_16x16x32_bf16 a[156:159], a[124:127], v[68:71], a[156:159]\n"
    "ds_read_b64_tr_b16 a[88:89], v32 offset:17408\n"
    "ds_read_b64_tr_b16 a[90:91], v32 offset:25600\n"
    "v_accvgpr_read_b32 v4, a20\n"
    "s_add_u32 s38, s38, s35\n"
    "s_lshl1_add_u32 s39, s36, s38\n"
    "PL24_%=:\n"
    "s_add_u32 s39, s39, 3\n"
    "s_cmpk_lt_u32 s39, 433\n"
    "s_cbranch_scc1 PL24_%=\n"
    "s_memrealtime s[36:37]\n"
    "v_mfma_f32_16x16x32_bf16 a[128:131], a[64:67], v[72:75], a[128:131]\n"
    "ds_read_b64_tr_b16 a[92:93], v33 offset:17408\n"
    "v_accvgpr_read_b32 v5, a21\n"
    "v_accvgpr_read_b32 v6, a22\n"
    "v_mfma_f32_16x16x32_bf16 a[132:135], a[64:67], v[76:79], a[132:135]\n"
    "ds_read_b64_tr_b16 a[94:95], v33 offset:25600\n"
    "v_accvgpr_read_b32 v7, a23\n"
    "s_waitcnt lgkmcnt(10)\n"
    "v_mfma_f32_16x16x32_bf16 a[136:139], a[68:71], v[72:75], a[136:139]\n"
    "ds_read_b64_tr_b16 a[96:97], v30 offset:18432\n"
    "v_cvt_pk_bf16_f32 v8, v0, v1\n"
    "v_cvt_pk_bf16_f32 v9, v2, v3\n"
    "v_mfma_f32_16x16x32_bf16 a[140:143], a[68:71], v[76:79], a[140:143]\n"
    "ds_read_b64_tr_b16 a[98:99], v30 offset:26624\n"
    "v_cvt_pk_bf16_f32 v10, v4, v5\n"
    "v_cvt_pk_bf16_f32 v11, v6, v7\n"
    "v_mfma_f32_16x16x32_bf16 a[144:147], a[72:75], v[72:75], a[144:147]\n"
    "buffer_load_dwordx4 v37, s[16:19], s40 offen offset:3072 nt lds\n"
    "v_mfma_f32_16x16x32_bf16 a[148:151], a[72:75], v[76:79], a[148:151]\n"
    "s_add_u32 m0, s28, 0x18000\n"
    "s_waitcnt lgkmcnt(8)\n"
    "v_mfma_f32_16x16x32_bf16 a[152:155], a[76:79], v[72:75], a[152:155]\n"
    "ds_read_b64_tr_b16 a[100:101], v31 offset:18432\n"
    "ds_read_b64_tr_b16 a[102:103], v31 offset:26624\n"
    "v_mfma_f32_16x16x32_bf16 a[156:159], a[76:79], v[76:79], a[156:159]\n"
    "ds_read_b64_tr_b16 a[104:105], v32 offset:18432\n"
    "ds_read_b64_tr_b16 a[106:107], v32 offset:26624\n"
    "v_mfma_f32_16x16x32_bf16 a[128:131], a[80:83], v[80:83], a[128:131]\n"
    "ds_read_b64_tr_b16 a[108:109], v33 offset:18432\n"
    "s_add_u32 s40, s31, 0x80\n"
    "v_mfma_f32_16x16x32_bf16 a[132:135], a[80:83], v[84:87], a[132:135]\n"
    "ds_read_b64_tr_b16 a[110:111], v33 offset:26624\n"
    "s_waitcnt lgkmcnt(10)\n"
    "v_mfma_f32_16x16x32_bf16 a[136:139], a[84:87], v[80:83], a[136:139]\n"
    "ds_read_b64_tr_b16 a[112:113], v30 offset:19456\n"
    "v_mfma_f32_16x16x32_bf16 a[140:143], a[84:87], v[84:87], a[140:143]\n"
    "ds_read_b64_tr_b16 a[114:115], v30 offset:27648\n"
    "v_mfma_f32_16x16x32_bf16 a[144:147], a[88:91], v[80:83], a[144:147]\n"
    "buffer_store_dwordx4 v[8:11], v24, s[20:23], s30 offen offset:1152 sc1\n"
    "v_accvgpr_read_b32 v0, a24\n"
    "v_mfma_f32_16x16x32_bf16 a[148:151], a[88:91], v[84:87], a[148:151]\n"
    "v_add_u32 v30, 0xfffe0000, v30\n"
    "v_accvgpr_read_b32 v1, a25\n"
    "s_waitcnt lgkmcnt(8)\n"
    "v_mfma_f32_16x16x32_bf16 a[152:155], a[92:95], v[80:83], a[152:155]\n"
    "ds_read_b64_tr_b16 a[116:117], v31 offset:19456\n"
    "ds_read_b64_tr_b16 a[118:119], v31 offset:27648\n"
    "v_accvgpr_read_b32 v2, a26\n"
    "v_mfma_f32_16x16x32_bf16 a[156:159], a[92:95], v[84:87], a[156:159]\n"
    "ds_read_b64_tr_b16 a[120:121], v32 offset:19456\n"
    "ds_read_b64_tr_b16 a[122:123], v32 offset:27648\n"
    "v_accvgpr_read_b32 v3, a27\n"
    "v_mfma_f32_16x16x32_bf16 a[128:131], a[96:99], v[88:91], a[128:131]\n"
    "ds_read_b64_tr_b16 a[124:125], v33 offset:19456\n"
    "s_waitcnt vmcnt(24) lgkmcnt(9)\n"
    "v_accvgpr_read_b32 v4, a28\n"
    "v_mfma_f32_16x16x32_bf16 a[132:135], a[96:99], v[92:95], a[132:135]\n"
    "ds_read_b64_tr_b16 a[126:127], v33 offset:27648\n"
    "s_barrier\n"
    "v_mfma_f32_16x16x32_bf16 a[136:139], a[100:103], v[88:91], a[136:139]\n"
    "ds_read_b64_tr_b16 a[64:65], v30 offset:0\n"
    "v_add_u32 v31, 0xfffe0000, v31\n"
    "v_accvgpr_read_b32 v5, a29\n"
    "v_mfma_f32_16x16x32_bf16 a[140:143], a[100:103], v[92:95], a[140:143]\n"
    "ds_read_b64_tr_b16 a[66:67], v30 offset:8192\n"
    "v_add_u32 v32, 0xfffe0000, v32\n"
    "v_accvgpr_read_b32 v6, a30\n"
    "v_mfma_f32_16x16x32_bf16 a[144:147], a[104:107], v[88:91], a[144:147]\n"
    "buffer_load_dwordx4 v34, s[16:19], s40 offen offset:0 nt lds\n"
    "v_accvgpr_read_b32 v7, a31\n"
    "v_mfma_f32_16x16x32_bf16 a[148:151], a[104:107], v[92:95], a[148:151]\n"
    "v_add_u32 v33, 0xfffe0000, v33\n"
    "v_cvt_pk_bf16_f32 v12, v0, v1\n"
    "s_waitcnt lgkmcnt(8)\n"
    "v_mfma_f32_16x16x32_bf16 a[152:155], a[108:111], v[88:91], a[152:155]\n"
    "ds_read_b64_tr_b16 a[68:69], v31 offset:0\n"
    "ds_read_b64_tr_b16 a[70:71], v31 offset:8192\n"
    "v_cvt_pk_bf16_f32 v13, v2, v3\n"
    "v_mfma_f32_16x16x32_bf16 a[156:159], a[108:111], v[92:95], a[156:159]\n"
    "ds_read_b64_tr_b16 a[72:73], v32 offset:0\n"
    "ds_read_b64_tr_b16 a[74:75], v32 offset:8192\n"
    "v_cvt_pk_bf16_f32 v14, v4, v5\n"
    "v_mfma_f32_16x16x32_bf16 a[128:131], a[112:115], v[96:99], a[128:131]\n"
    "ds_read_b64_tr_b16 a[76:77], v33 offset:0\n"
    "v_cvt_pk_bf16_f32 v15, v6, v7\n"
    "v_mfma_f32_16x16x32_bf16 a[132:135], a[112:115], v[100:103], a[132:135]\n"
    "ds_read_b64_tr_b16 a[78:79], v33 offset:8192\n"
    "s_waitcnt lgkmcnt(10)\n"
    "v_mfma_f32_16x16x32_bf16 a[136:139], a[116:119], v[96:99], a[136:139]\n"
    "ds_read_b64_tr_b16 a[80:81], v30 offset:1024\n"
    "v_mfma_f32_16x16x32_bf16 a[140:143], a[116:119], v[100:103], a[140:143]\n"
    "ds_read_b64_tr_b16 a[82:83], v30 offset:9216\n"
    "v_mfma_f32_16x16x32_bf16 a[144:147], a[120:123], v[96:99], a[144:147]\n"
    "buffer_store_dwordx4 v[12:15], v24, s[20:23], s30 offen offset:1664 sc1\n"
    "v_accvgpr_read_b32 v29, a215\n"
    "v_mfma_f32_16x16x32_bf16 a[148:151], a[120:123], v[100:103], a[148:151]\n"
    "v_add_u32 v29, 1, v29\n"
    "s_waitcnt lgkmcnt(8)\n"
    "v_mfma_f32_16x16x32_bf16 a[152:155], a[124:127], v[96:99], a[152:155]\n"
    "ds_read_b64_tr_b16 a[84:85], v31 offset:1024\n"
    "ds_read_b64_tr_b16 a[86:87], v31 offset:9216\n"
    "v_mfma_f32_16x16x32_bf16 a[156:159], a[124:127], v[100:103], a[156:159]\n"
    "ds_read_b64_tr_b16 a[88:89], v32 offset:1024\n"
    "ds_read_b64_tr_b16 a[90:91], v32 offset:9216\n"
    "s_add_u32 s38, s38, s35\n"
    "s_lshl1_add_u32 s39, s36, s38\n"
    "PL25_%=:\n"
    "s_add_u32 s39, s39, 3\n"
    "s_cmpk_lt_u32 s39, 516\n"
    "s_cbranch_scc1 PL25_%=\n"
    "s_memrealtime s[36:37]\n"
    "v_mfma_f32_16x16x32_bf16 a[128:131], a[64:67], v[104:107], a[128:131]\n"
    "ds_read_b64_tr_b16 a[92:93], v33 offset:1024\n"
    "buffer_load_dword v28, v27, s[24:27], s33 offen offset:1024 sc1\n"
    "v_mfma_f32_16x16x32_bf16 a[132:135], a[64:67], v[108:111], a[132:135]\n"
    "ds_read_b64_tr_b16 a[94:95], v33 offset:9216\n"
    "s_waitcnt lgkmcnt(10)\n"
    "v_mfma_f32_16x16x32_bf16 a[136:139], a[68:71], v[104:107], a[136:139]\n"
    "ds_read_b64_tr_b16 a[96:97], v30 offset:2048\n"
    "v_mfma_f32_16x16x32_bf16 a[140:143], a[68:71], v[108:111], a[140:143]\n"
    "ds_read_b64_tr_b16 a[98:99], v30 offset:10240\n"
    "v_mfma_f32_16x16x32_bf16 a[144:147], a[72:75], v[104:107], a[144:147]\n"
    "buffer_load_dwordx4 v35, s[16:19], s40 offen offset:1024 nt lds\n"
    "v_mfma_f32_16x16x32_bf16 a[148:151], a[72:75], v[108:111], a[148:151]\n"
    "s_waitcnt lgkmcnt(8)\n"
    "v_mfma_f32_16x16x32_bf16 a[152:155], a[76:79], v[104:107], a[152:155]\n"
    "ds_read_b64_tr_b16 a[100:101], v31 offset:2048\n"
    "ds_read_b64_tr_b16 a[102:103], v31 offset:10240\n"
    "v_mfma_f32_16x16x32_bf16 a[156:159], a[76:79], v[108:111], a[156:159]\n"
    "ds_read_b64_tr_b16 a[104:105], v32 offset:2048\n"
    "ds_read_b64_tr_b16 a[106:107], v32 offset:10240\n"
    "v_mfma_f32_16x16x32_bf16 a[128:131], a[80:83], v[112:115], a[128:131]\n"
    "ds_read_b64_tr_b16 a[108:109], v33 offset:2048\n"
    "v_mfma_f32_16x16x32_bf16 a[132:135], a[80:83], v[116:119], a[132:135]\n"
    "ds_read_b64_tr_b16 a[110:111], v33 offset:10240\n"
    "s_waitcnt lgkmcnt(10)\n"
    "v_mfma_f32_16x16x32_bf16 a[136:139], a[84:87], v[112:115], a[136:139]\n"
    "ds_read_b64_tr_b16 a[112:113], v30 offset:3072\n"
    "v_mfma_f32_16x16x32_bf16 a[140:143], a[84:87], v[116:119], a[140:143]\n"
    "ds_read_b64_tr_b16 a[114:115], v30 offset:11264\n"
    "v_mfma_f32_16x16x32_bf16 a[144:147], a[88:91], v[112:115], a[144:147]\n"
    "buffer_load_dwordx4 v36, s[16:19], s40 offen offset:2048 nt lds\n"
    "v_mfma_f32_16x16x32_bf16 a[148:151], a[88:91], v[116:119], a[148:151]\n"
    "s_waitcnt lgkmcnt(8)\n"
    "v_mfma_f32_16x16x32_bf16 a[152:155], a[92:95], v[112:115], a[152:155]\n"
    "ds_read_b64_tr_b16 a[116:117], v31 offset:3072\n"
    "ds_read_b64_tr_b16 a[118:119], v31 offset:11264\n"
    "v_mfma_f32_16x16x32_bf16 a[156:159], a[92:95], v[116:119], a[156:159]\n"
    "ds_read_b64_tr_b16 a[120:121], v32 offset:3072\n"
    "ds_read_b64_tr_b16 a[122:123], v32 offset:11264\n"
    "v_mfma_f32_16x16x32_bf16 a[128:131], a[96:99], v[120:123], a[128:131]\n"
    "ds_read_b64_tr_b16 a[124:125], v33 offset:3072\n"
    "s_waitcnt vmcnt(25) lgkmcnt(9)\n"
    "v_mfma_f32_16x16x32_bf16 a[132:135], a[96:99], v[124:127], a[132:135]\n"
    "ds_read_b64_tr_b16 a[126:127], v33 offset:11264\n"
    "s_barrier\n"
    "v_mfma_f32_16x16x32_bf16 a[136:139], a[100:103], v[120:123], a[136:139]\n"
    "ds_read_b64_tr_b16 a[64:65], v30 offset:16384\n"
    "v_mfma_f32_16x16x32_bf16 a[140:143], a[100:103], v[124:127], a[140:143]\n"
    "ds_read_b64_tr_b16 a[66:67], v30 offset:24576\n"
    "v_mfma_f32_16x16x32_bf16 a[144:147], a[104:107], v[120:123], a[144:147]\n"
    "buffer_load_dwordx4 v37, s[16:19], s40 offen offset:3072 nt lds\n"
    "v_mfma_f32_16x16x32_bf16 a[148:151], a[104:107], v[124:127], a[148:151]\n"
    "s_add_u32 m0, s28, 0x1c000\n"
    "s_waitcnt lgkmcnt(8)\n"
    "v_mfma_f32_16x16x32_bf16 a[152:155], a[108:111], v[120:123], a[152:155]\n"
    "ds_read_b64_tr_b16 a[68:69], v31 offset:16384\n"
    "ds_read_b64_tr_b16 a[70:71], v31 offset:24576\n"
    "v_mfma_f32_16x16x32_bf16 a[156:159], a[108:111], v[124:127], a[156:159]\n"
    "ds_read_b64_tr_b16 a[72:73], v32 offset:16384\n"
    "ds_read_b64_tr_b16 a[74:75], v32 offset:24576\n"
    "v_mfma_f32_16x16x32_bf16 a[128:131], a[112:115], v[128:131], a[128:131]\n"
    "ds_read_b64_tr_b16 a[76:77], v33 offset:16384\n"
    "s_add_u32 s40, s31, 0x20080\n"
    "v_mfma_f32_16x16x32_bf16 a[132:135], a[112:115], v[132:135], a[132:135]\n"
    "ds_read_b64_tr_b16 a[78:79], v33 offset:24576\n"
    "s_waitcnt lgkmcnt(10)\n"
    "v_mfma_f32_16x16x32_bf16 a[136:139], a[116:119], v[128:131], a[136:139]\n"
    "ds_read_b64_tr_b16 a[80:81], v30 offset:17408\n"
    "v_mfma_f32_16x16x32_bf16 a[140:143], a[116:119], v[132:135], a[140:143]\n"
    "ds_read_b64_tr_b16 a[82:83], v30 offset:25600\n"
    "v_mfma_f32_16x16x32_bf16 a[144:147], a[120:123], v[128:131], a[144:147]\n"
    "buffer_load_dwordx4 v34, s[16:19], s40 offen offset:0 nt lds\n"
    "v_mfma_f32_16x16x32_bf16 a[148:151], a[120:123], v[132:135], a[148:151]\n"
    "s_waitcnt lgkmcnt(8)\n"
    "v_mfma_f32_16x16x32_bf16 a[152:155], a[124:127], v[128:131], a[152:155]\n"
    "ds_read_b64_tr_b16 a[84:85], v31 offset:17408\n"
    "ds_read_b64_tr_b16 a[86:87], v31 offset:25600\n"
    "v_mfma_f32_16x16x32_bf16 a[156:159], a[124:127], v[132:135], a[156:159]\n"
    "ds_read_b64_tr_b16 a[88:89], v32 offset:17408\n"
    "ds_read_b64_tr_b16 a[90:91], v32 offset:25600\n"
    "s_add_u32 s38, s38, s35\n"
    "s_lshl1_add_u32 s39, s36, s38\n"
    "PL26_%=:\n"
    "s_add_u32 s39, s39, 3\n"
    "s_cmpk_lt_u32 s39, 599\n"
    "s_cbranch_scc1 PL26_%=\n"
    "s_memrealtime s[36:37]\n"
    "v_mfma_f32_16x16x32_bf16 a[128:131], a[64:67], v[136:139], a[128:131]\n"
    "ds_read_b64_tr_b16 a[92:93], v33 offset:17408\n"
    "v_mfma_f32_16x16x32_bf16 a[132:135], a[64:67], v[140:143], a[132:135]\n"
    "ds_read_b64_tr_b16 a[94:95], v33 offset:25600\n"
    "s_waitcnt lgkmcnt(10)\n"
    "v_mfma_f32_16x16x32_bf16 a[136:139], a[68:71], v[136:139], a[136:139]\n"
    "ds_read_b64_tr_b16 a[96:97], v30 offset:18432\n"
    "v_mfma_f32_16x16x32_bf16 a[140:143], a[68:71], v[140:143], a[140:143]\n"
    "ds_read_b64_tr_b16 a[98:99], v30 offset:26624\n"
    "v_mfma_f32_16x16x32_bf16 a[144:147], a[72:75], v[136:139], a[144:147]\n"
    "buffer_load_dwordx4 v35, s[16:19], s40 offen offset:1024 nt lds\n"
    "v_mfma_f32_16x16x32_bf16 a[148:151], a[72:75], v[140:143], a[148:151]\n"
    "s_waitcnt lgkmcnt(8)\n"
    "v_mfma_f32_16x16x32_bf16 a[152:155], a[76:79], v[136:139], a[152:155]\n"
    "ds_read_b64_tr_b16 a[100:101], v31 offset:18432\n"
    "ds_read_b64_tr_b16 a[102:103], v31 offset:26624\n"
    "v_mfma_f32_16x16x32_bf16 a[156:159], a[76:79], v[140:143], a[156:159]\n"
    "ds_read_b64_tr_b16 a[104:105], v32 offset:18432\n"
    "ds_read_b64_tr_b16 a[106:107], v32 offset:26624\n"
    "v_mfma_f32_16x16x32_bf16 a[128:131], a[80:83], v[144:147], a[128:131]\n"
    "ds_read_b64_tr_b16 a[108:109], v33 offset:18432\n"
    "v_mfma_f32_16x16x32_bf16 a[132:135], a[80:83], v[148:151], a[132:135]\n"
    "ds_read_b64_tr_b16 a[110:111], v33 offset:26624\n"
    "s_waitcnt lgkmcnt(10)\n"
    "v_mfma_f32_16x16x32_bf16 a[136:139], a[84:87], v[144:147], a[136:139]\n"
    "ds_read_b64_tr_b16 a[112:113], v30 offset:19456\n"
    "v_mfma_f32_16x16x32_bf16 a[140:143], a[84:87], v[148:151], a[140:143]\n"
    "ds_read_b64_tr_b16 a[114:115], v30 offset:27648\n"
    "STUB8_%=:\n"
    "s_cbranch_execz WRET_%=\n"
    "v_mfma_f32_16x16x32_bf16 a[144:147], a[88:91], v[144:147], a[144:147]\n"
    "buffer_load_dwordx4 v36, s[16:19], s40 offen offset:2048 nt lds\n"
    "v_mfma_f32_16x16x32_bf16 a[148:151], a[88:91], v[148:151], a[148:151]\n"
    "s_waitcnt lgkmcnt(8)\n"
    "v_mfma_f32_16x16x32_bf16 a[152:155], a[92:95], v[144:147], a[152:155]\n"
    "ds_read_b64_tr_b16 a[116:117], v31 offset:19456\n"
    "ds_read_b64_tr_b16 a[118:119], v31 offset:27648\n"
    "v_mfma_f32_16x16x32_bf16 a[156:159], a[92:95], v[148:151], a[156:159]\n"
    "ds_read_b64_tr_b16 a[120:121], v32 offset:19456\n"
    "ds_read_b64_tr_b16 a[122:123], v32 offset:27648\n"
    "v_mfma_f32_16x16x32_bf16 a[128:131], a[96:99], v[152:155], a[128:131]\n"
    "ds_read_b64_tr_b16 a[124:125], v33 offset:19456\n"
    "s_waitcnt vmcnt(25) lgkmcnt(9)\n"
    "v_mfma_f32_16x16x32_bf16 a[132:135], a[96:99], v[156:159], a[132:135]\n"
    "ds_read_b64_tr_b16 a[126:127], v33 offset:27648\n"
    "s_barrier\n"
    "v_mfma_f32_16x16x32_bf16 a[136:139], a[100:103], v[152:155], a[136:139]\n"
    "ds_read_b64_tr_b16 a[64:65], v30 offset:32768\n"
    "v_mfma_f32_16x16x32_bf16 a[140:143], a[100:103], v[156:159], a[140:143]\n"
    "ds_read_b64_tr_b16 a[66:67], v30 offset:40960\n"
    "v_mfma_f32_16x16x32_bf16 a[144:147], a[104:107], v[152:155], a[144:147]\n"
    "buffer_load_dwordx4 v37, s[16:19], s40 offen offset:3072 nt lds\n"
    "v_mfma_f32_16x16x32_bf16 a[148:151], a[104:107], v[156:159], a[148:151]\n"
    "s_add_u32 m0, s28, 0x20000\n"
    "s_waitcnt lgkmcnt(8)\n"
    "v_mfma_f32_16x16x32_bf16 a[152:155], a[108:111], v[152:155], a[152:155]\n"
    "ds_read_b64_tr_b16 a[68:69], v31 offset:32768\n"
    "ds_read_b64_tr_b16 a[70:71], v31 offset:40960\n"
    "v_mfma_f32_16x16x32_bf16 a[156:159], a[108:111], v[156:159], a[156:159]\n"
    "ds_read_b64_tr_b16 a[72:73], v32 offset:32768\n"
    "ds_read_b64_tr_b16 a[74:75], v32 offset:40960\n"
    "v_mfma_f32_16x16x32_bf16 a[128:131], a[112:115], v[160:163], a[128:131]\n"
    "ds_read_b64_tr_b16 a[76:77], v33 offset:32768\n"
    "s_add_u32 s40, s31, 0x2080\n"
    "v_mfma_f32_16x16x32_bf16 a[132:135], a[112:115], v[164:167], a[132:135]\n"
    "ds_read_b64_tr_b16 a[78:79], v33 offset:40960\n"
    "s_waitcnt lgkmcnt(10)\n"
    "v_mfma_f32_16x16x32_bf16 a[136:139], a[116:119], v[160:163], a[136:139]\n"
    "ds_read_b64_tr_b16 a[80:81], v30 offset:33792\n"
    "v_mfma_f32_16x16x32_bf16 a[140:143], a[116:119], v[164:167], a[140:143]\n"
    "ds_read_b64_tr_b16 a[82:83], v30 offset:41984\n"
    "v_mfma_f32_16x16x32_bf16 a[144:147], a[120:123], v[160:163], a[144:147]\n"
    "buffer_load_dwordx4 v34, s[16:19], s40 offen offset:0 nt lds\n"
    "v_mfma_f32_16x16x32_bf16 a[148:151], a[120:123], v[164:167], a[148:151]\n"
    "s_waitcnt lgkmcnt(8)\n"
    "v_mfma_f32_16x16x32_bf16 a[152:155], a[124:127], v[160:163], a[152:155]\n"
    "ds_read_b64_tr_b16 a[84:85], v31 offset:33792\n"
    "ds_read_b64_tr_b16 a[86:87], v31 offset:41984\n"
    "v_mfma_f32_16x16x32_bf16 a[156:159], a[124:127], v[164:167], a[156:159]\n"
    "ds_read_b64_tr_b16 a[88:89], v32 offset:33792\n"
    "ds_read_b64_tr_b16 a[90:91], v32 offset:41984\n"
    "s_add_u32 s38, s38, s35\n"
    "s_lshl1_add_u32 s39, s36, s38\n"
    "PL27_%=:\n"
    "s_add_u32 s39, s39, 3\n"
    "s_cmpk_lt_u32 s39, 682\n"
    "s_cbranch_scc1 PL27_%=\n"
    "s_memrealtime s[36:37]\n"
    "v_mfma_f32_16x16x32_bf16 a[128:131], a[64:67], v[168:171], a[128:131]\n"
    "ds_read_b64_tr_b16 a[92:93], v33 offset:33792\n"
    "v_mfma_f32_16x16x32_bf16 a[132:135], a[64:67], v[172:175], a[132:135]\n"
    "ds_read_b64_tr_b16 a[94:95], v33 offset:41984\n"
    "s_waitcnt lgkmcnt(10)\n"
    "v_mfma_f32_16x16x32_bf16 a[136:139], a[68:71], v[168:171], a[136:139]\n"
    "ds_read_b64_tr_b16 a[96:97], v30 offset:34816\n"
    "v_mfma_f32_16x16x32_bf16 a[140:143], a[68:71], v[172:175], a[140:143]\n"
    "ds_read_b64_tr_b16 a[98:99], v30 offset:43008\n"
    "v_mfma_f32_16x16x32_bf16 a[144:147], a[72:75], v[168:171], a[144:147]\n"
    "buffer_load_dwordx4 v35, s[16:19], s40 offen offset:1024 nt lds\n"
    "v_mfma_f32_16x16x32_bf16 a[148:151], a[72:75], v[172:175], a[148:151]\n"
    "s_waitcnt lgkmcnt(8)\n"
    "v_mfma_f32_16x16x32_bf16 a[152:155], a[76:79], v[168:171], a[152:155]\n"
    "ds_read_b64_tr_b16 a[100:101], v31 offset:34816\n"
    "ds_read_b64_tr_b16 a[102:103], v31 offset:43008\n"
    "v_mfma_f32_16x16x32_bf16 a[156:159], a[76:79], v[172:175], a[156:159]\n"
    "ds_read_b64_tr_b16 a[104:105], v32 offset:34816\n"
    "ds_read_b64_tr_b16 a[106:107], v32 offset:43008\n"
    "v_mfma_f32_16x16x32_bf16 a[128:131], a[80:83], v[176:179], a[128:131]\n"
    "ds_read_b64_tr_b16 a[108:109], v33 offset:34816\n"
    "v_mfma_f32_16x16x32_bf16 a[132:135], a[80:83], v[180:183], a[132:135]\n"
    "ds_read_b64_tr_b16 a[110:111], v33 offset:43008\n"
    "s_waitcnt lgkmcnt(10)\n"
    "v_mfma_f32_16x16x32_bf16 a[136:139], a[84:87], v[176:179], a[136:139]\n"
    "ds_read_b64_tr_b16 a[112:113], v30 offset:35840\n"
    "v_mfma_f32_16x16x32_bf16 a[140:143], a[84:87], v[180:183], a[140:143]\n"
    "ds_read_b64_tr_b16 a[114:115], v30 offset:44032\n"
    "v_mfma_f32_16x16x32_bf16 a[144:147], a[88:91], v[176:179], a[144:147]\n"
    "buffer_load_dwordx4 v36, s[16:19], s40 offen offset:2048 nt lds\n"
    "v_mfma_f32_16x16x32_bf16 a[148:151], a[88:91], v[180:183], a[148:151]\n"
    "s_waitcnt lgkmcnt(8)\n"
    "v_mfma_f32_16x16x32_bf16 a[152:155], a[92:95], v[176:179], a[152:155]\n"
    "ds_read_b64_tr_b16 a[116:117], v31 offset:35840\n"
    "ds_read_b64_tr_b16 a[118:119], v31 offset:44032\n"
    "v_mfma_f32_16x16x32_bf16 a[156:159], a[92:95], v[180:183], a[156:159]\n"
    "ds_read_b64_tr_b16 a[120:121], v32 offset:35840\n"
    "ds_read_b64_tr_b16 a[122:123], v32 offset:44032\n"
    "v_mfma_f32_16x16x32_bf16 a[128:131], a[96:99], v[184:187], a[128:131]\n"
    "ds_read_b64_tr_b16 a[124:125], v33 offset:35840\n"
    "s_waitcnt vmcnt(24) lgkmcnt(9)\n"
    "s_waitcnt vmcnt(10)\n"
    "v_mfma_f32_16x16x32_bf16 a[132:135], a[96:99], v[188:191], a[132:135]\n"
    "ds_read_b64_tr_b16 a[126:127], v33 offset:44032\n"
    "s_barrier\n"
    "v_mfma_f32_16x16x32_bf16 a[136:139], a[100:103], v[184:187], a[136:139]\n"
    "ds_read_b64_tr_b16 a[64:65], v30 offset:49152\n"
    "buffer_store_dword v29, v27, s[24:27], s7 offen offset:1280 sc1\n"
    "v_mfma_f32_16x16x32_bf16 a[140:143], a[100:103], v[188:191], a[140:143]\n"
    "ds_read_b64_tr_b16 a[66:67], v30 offset:57344\n"
    "v_accvgpr_read_b32 v38, a216\n"
    "v_cmp_eq_u32 vcc, v28, v38\n"
    "v_mfma_f32_16x16x32_bf16 a[144:147], a[104:107], v[184:187], a[144:147]\n"
    "buffer_load_dwordx4 v37, s[16:19], s40 offen offset:3072 nt lds\n"
    "s_cbranch_vccnz SPINA_%=\n"
    "BACKA_%=:\n"
    "v_mfma_f32_16x16x32_bf16 a[148:151], a[104:107], v[188:191], a[148:151]\n"
    "s_add_u32 m0, s28, 0x24000\n"
    "buffer_load_dwordx4 a[48:51], v26, s[24:27], s33 offen sc1\n"
    "s_waitcnt lgkmcnt(8)\n"
    "v_mfma_f32_16x16x32_bf16 a[152:155], a[108:111], v[184:187], a[152:155]\n"
    "ds_read_b64_tr_b16 a[68:69], v31 offset:49152\n"
    "ds_read_b64_tr_b16 a[70:71], v31 offset:57344\n"
    "v_mfma_f32_16x16x32_bf16 a[156:159], a[108:111], v[188:191], a[156:159]\n"
    "ds_read_b64_tr_b16 a[72:73], v32 offset:49152\n"
    "ds_read_b64_tr_b16 a[74:75], v32 offset:57344\n"
    "v_mfma_f32_16x16x32_bf16 a[128:131], a[112:115], v[192:195], a[128:131]\n"
    "ds_read_b64_tr_b16 a[76:77], v33 offset:49152\n"
    "s_add_u32 s40, s31, 0x22080\n"
    "v_mfma_f32_16x16x32_bf16 a[132:135], a[112:115], v[196:199], a[132:135]\n"
    "ds_read_b64_tr_b16 a[78:79], v33 offset:57344\n"
    "s_waitcnt lgkmcnt(10)\n"
    "v_mfma_f32_16x16x32_bf16 a[136:139], a[116:119], v[192:195], a[136:139]\n"
    "ds_read_b64_tr_b16 a[80:81], v30 offset:50176\n"
    "v_mfma_f32_16x16x32_bf16 a[140:143], a[116:119], v[196:199], a[140:143]\n"
    "ds_read_b64_tr_b16 a[82:83], v30 offset:58368\n"
    "v_mfma_f32_16x16x32_bf16 a[144:147], a[120:123], v[192:195], a[144:147]\n"
    "buffer_load_dwordx4 a[32:35], v24, s[20:23], s31 offen offset:0 sc1\n"
    "v_mfma_f32_16x16x32_bf16 a[148:151], a[120:123], v[196:199], a[148:151]\n"
    "s_waitcnt lgkmcnt(8)\n"
    "v_mfma_f32_16x16x32_bf16 a[152:155], a[124:127], v[192:195], a[152:155]\n"
    "ds_read_b64_tr_b16 a[84:85], v31 offset:50176\n"
    "ds_read_b64_tr_b16 a[86:87], v31 offset:58368\n"
    "v_mfma_f32_16x16x32_bf16 a[156:159], a[124:127], v[196:199], a[156:159]\n"
    "ds_read_b64_tr_b16 a[88:89], v32 offset:50176\n"
    "ds_read_b64_tr_b16 a[90:91], v32 offset:58368\n"
    "s_add_u32 s38, s38, s35\n"
    "s_lshl1_add_u32 s39, s36, s38\n"
    "PL28_%=:\n"
    "s_add_u32 s39, s39, 3\n"
    "s_cmpk_lt_u32 s39, 765\n"
    "s_cbranch_scc1 PL28_%=\n"
    "s_memrealtime s[36:37]\n"
    "v_mfma_f32_16x16x32_bf16 a[128:131], a[64:67], v[200:203], a[128:131]\n"
    "ds_read_b64_tr_b16 a[92:93], v33 offset:50176\n"
    "s_lshl1_add_u32 s50, s28, 0x10000\n"
    "v_mul_u32_u24 v26, 17, v26\n"
    "v_mfma_f32_16x16x32_bf16 a[132:135], a[64:67], v[204:207], a[132:135]\n"
    "ds_read_b64_tr_b16 a[94:95], v33 offset:58368\n"
    "s_sub_u32 s50, s50, %3\n"
    "s_waitcnt lgkmcnt(10)\n"
    "v_mfma_f32_16x16x32_bf16 a[136:139], a[68:71], v[200:203], a[136:139]\n"
    "ds_read_b64_tr_b16 a[96:97], v30 offset:51200\n"
    "v_lshrrev_b32 v29, 4, v39\n"
    "v_lshl_add_u32 v26, v29, 3, v26\n"
    "v_mfma_f32_16x16x32_bf16 a[140:143], a[68:71], v[204:207], a[140:143]\n"
    "ds_read_b64_tr_b16 a[98:99], v30 offset:59392\n"
    "v_lshrrev_b32 v29, 3, v39\n"
    "v_add_u32 v26, s50, v26\n"
    "v_mfma_f32_16x16x32_bf16 a[144:147], a[72:75], v[200:203], a[144:147]\n"
    "buffer_load_dwordx4 a[36:39], v24, s[20:23], s31 offen offset:512 sc1\n"
    "v_mul_u32_u24 v29, 0x6f0, v29\n"
    "v_mfma_f32_16x16x32_bf16 a[148:151], a[72:75], v[204:207], a[148:151]\n"
    "v_sub_u32 v29, v24, v29\n"
    "v_add_u32 v29, s50, v29\n"
    "s_waitcnt lgkmcnt(8)\n"
    "v_mfma_f32_16x16x32_bf16 a[152:155], a[76:79], v[200:203], a[152:155]\n"
    "ds_read_b64_tr_b16 a[100:101], v31 offset:51200\n"
    "ds_read_b64_tr_b16 a[102:103], v31 offset:59392\n"
    "v_mfma_f32_16x16x32_bf16 a[156:159], a[76:79], v[204:207], a[156:159]\n"
    "ds_read_b64_tr_b16 a[104:105], v32 offset:51200\n"
    "ds_read_b64_tr_b16 a[106:107], v32 offset:59392\n"
    "v_mfma_f32_16x16x32_bf16 a[128:131], a[80:83], v[208:211], a[128:131]\n"
    "ds_read_b64_tr_b16 a[108:109], v33 offset:51200\n"
    "v_mfma_f32_16x16x32_bf16 a[132:135], a[80:83], v[212:215], a[132:135]\n"
    "ds_read_b64_tr_b16 a[110:111], v33 offset:59392\n"
    "s_waitcnt lgkmcnt(10)\n"
    "v_mfma_f32_16x16x32_bf16 a[136:139], a[84:87], v[208:211], a[136:139]\n"
    "ds_read_b64_tr_b16 a[112:113], v30 offset:52224\n"
    "v_mfma_f32_16x16x32_bf16 a[140:143], a[84:87], v[212:215], a[140:143]\n"
    "ds_read_b64_tr_b16 a[114:115], v30 offset:60416\n"
    "v_mfma_f32_16x16x32_bf16 a[144:147], a[88:91], v[208:211], a[144:147]\n"
    "buffer_load_dwordx4 a[40:43], v24, s[20:23], s31 offen offset:1024 sc1\n"
    "v_mfma_f32_16x16x32_bf16 a[148:151], a[88:91], v[212:215], a[148:151]\n"
    "v_add_u32 v30, 0x10000, v30\n"
    "s_waitcnt lgkmcnt(8)\n"
    "v_mfma_f32_16x16x32_bf16 a[152:155], a[92:95], v[208:211], a[152:155]\n"
    "ds_read_b64_tr_b16 a[116:117], v31 offset:52224\n"
    "ds_read_b64_tr_b16 a[118:119], v31 offset:60416\n"
    "v_mfma_f32_16x16x32_bf16 a[156:159], a[92:95], v[212:215], a[156:159]\n"
    "ds_read_b64_tr_b16 a[120:121], v32 offset:52224\n"
    "ds_read_b64_tr_b16 a[122:123], v32 offset:60416\n"
    "v_mfma_f32_16x16x32_bf16 a[128:131], a[96:99], v[216:219], a[128:131]\n"
    "ds_read_b64_tr_b16 a[124:125], v33 offset:52224\n"
    "s_waitcnt lgkmcnt(9)\n"
    "v_mfma_f32_16x16x32_bf16 a[132:135], a[96:99], v[220:223], a[132:135]\n"
    "ds_read_b64_tr_b16 a[126:127], v33 offset:60416\n"
    "s_barrier\n"
    "v_mfma_f32_16x16x32_bf16 a[136:139], a[100:103], v[216:219], a[136:139]\n"
    "ds_read_b64_tr_b16 a[64:65], v30 offset:0\n"
    "v_add_u32 v31, 0x10000, v31\n"
    "v_mfma_f32_16x16x32_bf16 a[140:143], a[100:103], v[220:223], a[140:143]\n"
    "ds_read_b64_tr_b16 a[66:67], v30 offset:8192\n"
    "v_add_u32 v32, 0x10000, v32\n"
    "v_mfma_f32_16x16x32_bf16 a[144:147], a[104:107], v[216:219], a[144:147]\n"
    "buffer_load_dwordx4 a[44:47], v24, s[20:23], s31 offen offset:1536 sc1\n"
    "v_accvgpr_read_b32 v38, a214\n"
    "v_mfma_f32_16x16x32_bf16 a[148:151], a[104:107], v[220:223], a[148:151]\n"
    "v_add_u32 v33, 0x10000, v33\n"
    "v_add_u32 v38, 2, v38\n"
    "s_waitcnt lgkmcnt(8)\n"
    "v_mfma_f32_16x16x32_bf16 a[152:155], a[108:111], v[216:219], a[152:155]\n"
    "ds_read_b64_tr_b16 a[68:69], v31 offset:0\n"
    "ds_read_b64_tr_b16 a[70:71], v31 offset:8192\n"
    "buffer_store_dword v38, v27, s[24:27], s7 offen offset:1024 sc1\n"
    "v_mfma_f32_16x16x32_bf16 a[156:159], a[108:111], v[220:223], a[156:159]\n"
    "ds_read_b64_tr_b16 a[72:73], v32 offset:0\n"
    "ds_read_b64_tr_b16 a[74:75], v32 offset:8192\n"
    "v_mfma_f32_16x16x32_bf16 a[128:131], a[112:115], v[224:227], a[128:131]\n"
    "ds_read_b64_tr_b16 a[76:77], v33 offset:0\n"
    "v_mfma_f32_16x16x32_bf16 a[132:135], a[112:115], v[228:231], a[132:135]\n"
    "ds_read_b64_tr_b16 a[78:79], v33 offset:8192\n"
    "s_waitcnt lgkmcnt(10)\n"
    "v_mfma_f32_16x16x32_bf16 a[136:139], a[116:119], v[224:227], a[136:139]\n"
    "ds_read_b64_tr_b16 a[80:81], v30 offset:1024\n"
    "v_mfma_f32_16x16x32_bf16 a[140:143], a[116:119], v[228:231], a[140:143]\n"
    "ds_read_b64_tr_b16 a[82:83], v30 offset:9216\n"
    "v_mfma_f32_16x16x32_bf16 a[144:147], a[120:123], v[224:227], a[144:147]\n"
    "buffer_load_dwordx4 v34, s[16:19], s40 offen offset:0 nt lds\n"
    "v_mfma_f32_16x16x32_bf16 a[148:151], a[120:123], v[228:231], a[148:151]\n"
    "s_waitcnt lgkmcnt(8)\n"
    "v_mfma_f32_16x16x32_bf16 a[152:155], a[124:127], v[224:227], a[152:155]\n"
    "ds_read_b64_tr_b16 a[84:85], v31 offset:1024\n"
    "ds_read_b64_tr_b16 a[86:87], v31 offset:9216\n"
    "v_mfma_f32_16x16x32_bf16 a[156:159], a[124:127], v[228:231], a[156:159]\n"
    "ds_read_b64_tr_b16 a[88:89], v32 offset:1024\n"
    "ds_read_b64_tr_b16 a[90:91], v32 offset:9216\n"
    "s_add_u32 s38, s38, s35\n"
    "s_lshl1_add_u32 s39, s36, s38\n"
    "PL29_%=:\n"
    "s_add_u32 s39, s39, 3\n"
    "s_cmpk_lt_u32 s39, 848\n"
    "s_cbranch_scc1 PL29_%=\n"
    "s_memrealtime s[36:37]\n"
    "v_mfma_f32_16x16x32_bf16 a[128:131], a[64:67], v[232:235], a[128:131]\n"
    "ds_read_b64_tr_b16 a[92:93], v33 offset:1024\n"
    "v_mfma_f32_16x16x32_bf16 a[132:135], a[64:67], v[236:239], a[132:135]\n"
    "ds_read_b64_tr_b16 a[94:95], v33 offset:9216\n"
    "s_waitcnt lgkmcnt(10)\n"
    "v_mfma_f32_16x16x32_bf16 a[136:139], a[68:71], v[232:235], a[136:139]\n"
    "ds_read_b64_tr_b16 a[96:97], v30 offset:2048\n"
    "v_mfma_f32_16x16x32_bf16 a[140:143], a[68:71], v[236:239], a[140:143]\n"
    "ds_read_b64_tr_b16 a[98:99], v30 offset:10240\n"
    "v_mfma_f32_16x16x32_bf16 a[144:147], a[72:75], v[232:235], a[144:147]\n"
    "buffer_load_dwordx4 v35, s[16:19], s40 offen offset:1024 nt lds\n"
    "v_mfma_f32_16x16x32_bf16 a[148:151], a[72:75], v[236:239], a[148:151]\n"
    "s_waitcnt lgkmcnt(8)\n"
    "v_mfma_f32_16x16x32_bf16 a[152:155], a[76:79], v[232:235], a[152:155]\n"
    "ds_read_b64_tr_b16 a[100:101], v31 offset:2048\n"
    "ds_read_b64_tr_b16 a[102:103], v31 offset:10240\n"
    "v_mfma_f32_16x16x32_bf16 a[156:159], a[76:79], v[236:239], a[156:159]\n"
    "ds_read_b64_tr_b16 a[104:105], v32 offset:2048\n"
    "ds_read_b64_tr_b16 a[106:107], v32 offset:10240\n"
    "v_mfma_f32_16x16x32_bf16 a[128:131], a[80:83], v[240:243], a[128:131]\n"
    "ds_read_b64_tr_b16 a[108:109], v33 offset:2048\n"
    "v_mfma_f32_16x16x32_bf16 a[132:135], a[80:83], v[244:247], a[132:135]\n"
    "ds_read_b64_tr_b16 a[110:111], v33 offset:10240\n"
    "s_waitcnt lgkmcnt(10)\n"
    "v_mfma_f32_16x16x32_bf16 a[136:139], a[84:87], v[240:243], a[136:139]\n"
    "ds_read_b64_tr_b16 a[112:113], v30 offset:3072\n"
    "v_mfma_f32_16x16x32_bf16 a[140:143], a[84:87], v[244:247], a[140:143]\n"
    "ds_read_b64_tr_b16 a[114:115], v30 offset:11264\n"
    "v_mfma_f32_16x16x32_bf16 a[144:147], a[88:91], v[240:243], a[144:147]\n"
    "buffer_load_dwordx4 v36, s[16:19], s40 offen offset:2048 nt lds\n"
    "v_mfma_f32_16x16x32_bf16 a[148:151], a[88:91], v[244:247], a[148:151]\n"
    "s_waitcnt lgkmcnt(8)\n"
    "v_mfma_f32_16x16x32_bf16 a[152:155], a[92:95], v[240:243], a[152:155]\n"
    "ds_read_b64_tr_b16 a[116:117], v31 offset:3072\n"
    "ds_read_b64_tr_b16 a[118:119], v31 offset:11264\n"
    "v_mfma_f32_16x16x32_bf16 a[156:159], a[92:95], v[244:247], a[156:159]\n"
    "ds_read_b64_tr_b16 a[120:121], v32 offset:3072\n"
    "ds_read_b64_tr_b16 a[122:123], v32 offset:11264\n"
    "v_mfma_f32_16x16x32_bf16 a[128:131], a[96:99], v[248:251], a[128:131]\n"
    "ds_read_b64_tr_b16 a[124:125], v33 offset:3072\n"
    "s_waitcnt lgkmcnt(9)\n"
    "v_mfma_f32_16x16x32_bf16 a[132:135], a[96:99], v[252:255], a[132:135]\n"
    "ds_read_b64_tr_b16 a[126:127], v33 offset:11264\n"
    "s_barrier\n"
    "v_mfma_f32_16x16x32_bf16 a[136:139], a[100:103], v[248:251], a[136:139]\n"
    "ds_read_b64_tr_b16 a[64:65], v30 offset:16384\n"
    "v_mfma_f32_16x16x32_bf16 a[140:143], a[100:103], v[252:255], a[140:143]\n"
    "ds_read_b64_tr_b16 a[66:67], v30 offset:24576\n"
    "v_mfma_f32_16x16x32_bf16 a[144:147], a[104:107], v[248:251], a[144:147]\n"
    "buffer_load_dwordx4 v37, s[16:19], s40 offen offset:3072 nt lds\n"
    "v_mfma_f32_16x16x32_bf16 a[148:151], a[104:107], v[252:255], a[148:151]\n"
    "s_add_u32 m0, s28, 0x0\n"
    "s_waitcnt lgkmcnt(8)\n"
    "v_mfma_f32_16x16x32_bf16 a[152:155], a[108:111], v[248:251], a[152:155]\n"
    "ds_read_b64_tr_b16 a[68:69], v31 offset:16384\n"
    "ds_read_b64_tr_b16 a[70:71], v31 offset:24576\n"
    "v_mfma_f32_16x16x32_bf16 a[156:159], a[108:111], v[252:255], a[156:159]\n"
    "ds_read_b64_tr_b16 a[72:73], v32 offset:16384\n"
    "ds_read_b64_tr_b16 a[74:75], v32 offset:24576\n"
    "v_mfma_f32_16x16x32_bf16 a[128:131], a[112:115], a[160:163], a[128:131]\n"
    "ds_read_b64_tr_b16 a[76:77], v33 offset:16384\n"
    "s_add_u32 s40, s31, 0x40080\n"
    "v_mfma_f32_16x16x32_bf16 a[132:135], a[112:115], a[164:167], a[132:135]\n"
    "ds_read_b64_tr_b16 a[78:79], v33 offset:24576\n"
    "s_waitcnt lgkmcnt(10)\n"
    "v_mfma_f32_16x16x32_bf16 a[136:139], a[116:119], a[160:163], a[136:139]\n"
    "ds_read_b64_tr_b16 a[80:81], v30 offset:17408\n"
    "v_mfma_f32_16x16x32_bf16 a[140:143], a[116:119], a[164:167], a[140:143]\n"
    "ds_read_b64_tr_b16 a[82:83], v30 offset:25600\n"
    "v_mfma_f32_16x16x32_bf16 a[144:147], a[120:123], a[160:163], a[144:147]\n"
    "buffer_load_dwordx4 v34, s[16:19], s40 offen offset:0 nt lds\n"
    "v_mfma_f32_16x16x32_bf16 a[148:151], a[120:123], a[164:167], a[148:151]\n"
    "s_waitcnt lgkmcnt(8)\n"
    "v_mfma_f32_16x16x32_bf16 a[152:155], a[124:127], a[160:163], a[152:155]\n"
    "ds_read_b64_tr_b16 a[84:85], v31 offset:17408\n"
    "ds_read_b64_tr_b16 a[86:87], v31 offset:25600\n"
    "v_mfma_f32_16x16x32_bf16 a[156:159], a[124:127], a[164:167], a[156:159]\n"
    "ds_read_b64_tr_b16 a[88:89], v32 offset:17408\n"
    "ds_read_b64_tr_b16 a[90:91], v32 offset:25600\n"
    "s_add_u32 s38, s38, s35\n"
    "s_lshl1_add_u32 s39, s36, s38\n"
    "PL30_%=:\n"
    "s_add_u32 s39, s39, 3\n"
    "s_cmpk_lt_u32 s39, 931\n"
    "s_cbranch_scc1 PL30_%=\n"
    "s_memrealtime s[36:37]\n"
    "v_mfma_f32_16x16x32_bf16 a[128:131], a[64:67], a[168:171], a[128:131]\n"
    "ds_read_b64_tr_b16 a[92:93], v33 offset:17408\n"
    "buffer_load_dword v28, v27, s[24:27], s33 offen offset:1280 sc1\n"
    "v_mfma_f32_16x16x32_bf16 a[132:135], a[64:67], a[172:175], a[132:135]\n"
    "ds_read_b64_tr_b16 a[94:95], v33 offset:25600\n"
    "s_waitcnt lgkmcnt(10)\n"
    "v_mfma_f32_16x16x32_bf16 a[136:139], a[68:71], a[168:171], a[136:139]\n"
    "ds_read_b64_tr_b16 a[96:97], v30 offset:18432\n"
    "v_mfma_f32_16x16x32_bf16 a[140:143], a[68:71], a[172:175], a[140:143]\n"
    "ds_read_b64_tr_b16 a[98:99], v30 offset:26624\n"
    "v_mfma_f32_16x16x32_bf16 a[144:147], a[72:75], a[168:171], a[144:147]\n"
    "buffer_load_dwordx4 v35, s[16:19], s40 offen offset:1024 nt lds\n"
    "v_mfma_f32_16x16x32_bf16 a[148:151], a[72:75], a[172:175], a[148:151]\n"
    "s_waitcnt lgkmcnt(8)\n"
    "v_mfma_f32_16x16x32_bf16 a[152:155], a[76:79], a[168:171], a[152:155]\n"
    "ds_read_b64_tr_b16 a[100:101], v31 offset:18432\n"
    "ds_read_b64_tr_b16 a[102:103], v31 offset:26624\n"
    "v_mfma_f32_16x16x32_bf16 a[156:159], a[76:79], a[172:175], a[156:159]\n"
    "ds_read_b64_tr_b16 a[104:105], v32 offset:18432\n"
    "ds_read_b64_tr_b16 a[106:107], v32 offset:26624\n"
    "v_mfma_f32_16x16x32_bf16 a[128:131], a[80:83], a[176:179], a[128:131]\n"
    "ds_read_b64_tr_b16 a[108:109], v33 offset:18432\n"
    "s_waitcnt vmcnt(12)\n"
    "v_mfma_f32_16x16x32_bf16 a[132:135], a[80:83], a[180:183], a[132:135]\n"
    "ds_read_b64_tr_b16 a[110:111], v33 offset:26624\n"
    "s_waitcnt lgkmcnt(10)\n"
    "v_mfma_f32_16x16x32_bf16 a[136:139], a[84:87], a[176:179], a[136:139]\n"
    "ds_read_b64_tr_b16 a[112:113], v30 offset:19456\n"
    "v_accvgpr_read_b32 v0, a48\n"
    "v_mfma_f32_16x16x32_bf16 a[140:143], a[84:87], a[180:183], a[140:143]\n"
    "ds_read_b64_tr_b16 a[114:115], v30 offset:27648\n"
    "v_accvgpr_read_b32 v1, a49\n"
    "v_mfma_f32_16x16x32_bf16 a[144:147], a[88:91], a[176:179], a[144:147]\n"
    "buffer_load_dwordx4 v36, s[16:19], s40 offen offset:2048 nt lds\n"
    "v_mfma_f32_16x16x32_bf16 a[148:151], a[88:91], a[180:183], a[148:151]\n"
    "v_accvgpr_read_b32 v2, a50\n"
    "s_waitcnt lgkmcnt(8)\n"
    "v_mfma_f32_16x16x32_bf16 a[152:155], a[92:95], a[176:179], a[152:155]\n"
    "ds_read_b64_tr_b16 a[116:117], v31 offset:19456\n"
    "ds_read_b64_tr_b16 a[118:119], v31 offset:27648\n"
    "v_mfma_f32_16x16x32_bf16 a[156:159], a[92:95], a[180:183], a[156:159]\n"
    "ds_read_b64_tr_b16 a[120:121], v32 offset:19456\n"
    "ds_read_b64_tr_b16 a[122:123], v32 offset:27648\n"
    "v_mfma_f32_16x16x32_bf16 a[128:131], a[96:99], a[184:187], a[128:131]\n"
    "ds_read_b64_tr_b16 a[124:125], v33 offset:19456\n"
    "s_waitcnt lgkmcnt(9)\n"
    "v_mfma_f32_16x16x32_bf16 a[132:135], a[96:99], a[188:191], a[132:135]\n"
    "ds_read_b64_tr_b16 a[126:127], v33 offset:27648\n"
    "s_barrier\n"
    "v_mfma_f32_16x16x32_bf16 a[136:139], a[100:103], a[184:187], a[136:139]\n"
    "ds_read_b64_tr_b16 a[64:65], v30 offset:32768\n"
    "v_accvgpr_read_b32 v3, a51\n"
    "v_mfma_f32_16x16x32_bf16 a[140:143], a[100:103], a[188:191], a[140:143]\n"
    "ds_read_b64_tr_b16 a[66:67], v30 offset:40960\n"
    "v_min_f32 v4, v16, v0\n"
    "v_mfma_f32_16x16x32_bf16 a[144:147], a[104:107], a[184:187], a[144:147]\n"
    "buffer_load_dwordx4 v37, s[16:19], s40 offen offset:3072 nt lds\n"
    "v_mfma_f32_16x16x32_bf16 a[148:151], a[104:107], a[188:191], a[148:151]\n"
    "s_add_u32 m0, s28, 0x4000\n"
    "s_waitcnt lgkmcnt(8)\n"
    "v_mfma_f32_16x16x32_bf16 a[152:155], a[108:111], a[184:187], a[152:155]\n"
    "ds_read_b64_tr_b16 a[68:69], v31 offset:32768\n"
    "ds_read_b64_tr_b16 a[70:71], v31 offset:40960\n"
    "v_mfma_f32_16x16x32_bf16 a[156:159], a[108:111], a[188:191], a[156:159]\n"
    "ds_read_b64_tr_b16 a[72:73], v32 offset:32768\n"
    "ds_read_b64_tr_b16 a[74:75], v32 offset:40960\n"
    "v_mfma_f32_16x16x32_bf16 a[128:131], a[112:115], a[192:195], a[128:131]\n"
    "ds_read_b64_tr_b16 a[76:77], v33 offset:32768\n"
    "s_add_u32 s40, s31, 0x60080\n"
    "v_mfma_f32_16x16x32_bf16 a[132:135], a[112:115], a[196:199], a[132:135]\n"
    "ds_read_b64_tr_b16 a[78:79], v33 offset:40960\n"
    "s_waitcnt lgkmcnt(10)\n"
    "v_mfma_f32_16x16x32_bf16 a[136:139], a[116:119], a[192:195], a[136:139]\n"
    "ds_read_b64_tr_b16 a[80:81], v30 offset:33792\n"
    "v_min_f32 v5, v17, v1\n"
    "v_mfma_f32_16x16x32_bf16 a[140:143], a[116:119], a[196:199], a[140:143]\n"
    "ds_read_b64_tr_b16 a[82:83], v30 offset:41984\n"
    "v_sub_f32 v20, v4, v16\n"
    "v_mfma_f32_16x16x32_bf16 a[144:147], a[120:123], a[192:195], a[144:147]\n"
    "buffer_load_dwordx4 v34, s[16:19], s40 offen offset:0 nt lds\n"
    "v_mfma_f32_16x16x32_bf16 a[148:151], a[120:123], a[196:199], a[148:151]\n"
    "v_sub_f32 v22, v4, v0\n"
    "s_waitcnt lgkmcnt(8)\n"
    "v_mfma_f32_16x16x32_bf16 a[152:155], a[124:127], a[192:195], a[152:155]\n"
    "ds_read_b64_tr_b16 a[84:85], v31 offset:33792\n"
    "ds_read_b64_tr_b16 a[86:87], v31 offset:41984\n"
    "v_mfma_f32_16x16x32_bf16 a[156:159], a[124:127], a[196:199], a[156:159]\n"
    "ds_read_b64_tr_b16 a[88:89], v32 offset:33792\n"
    "ds_read_b64_tr_b16 a[90:91], v32 offset:41984\n"
    "s_add_u32 s38, s38, s35\n"
    "s_lshl1_add_u32 s39, s36, s38\n"
    "PL31_%=:\n"
    "s_add_u32 s39, s39, 3\n"
    "s_cmpk_lt_u32 s39, 1014\n"
    "s_cbranch_scc1 PL31_%=\n"
    "s_memrealtime s[36:37]\n"
    "v_mfma_f32_16x16x32_bf16 a[0:3], a[64:67], v[40:43], 0\n"
    "ds_read_b64_tr_b16 a[92:93], v33 offset:33792\n"
    "v_sub_f32 v21, v5, v17\n"
    "v_sub_f32 v23, v5, v1\n"
    "v_mfma_f32_16x16x32_bf16 a[4:7], a[64:67], v[44:47], 0\n"
    "ds_read_b64_tr_b16 a[94:95], v33 offset:41984\n"
    "v_exp_f32 v20, v20\n"
    "s_waitcnt lgkmcnt(10)\n"
    "v_mfma_f32_16x16x32_bf16 a[8:11], a[68:71], v[40:43], 0\n"
    "ds_read_b64_tr_b16 a[96:97], v30 offset:34816\n"
    "v_exp_f32 v21, v21\n"
    "v_mfma_f32_16x16x32_bf16 a[12:15], a[68:71], v[44:47], 0\n"
    "ds_read_b64_tr_b16 a[98:99], v30 offset:43008\n"
    "v_exp_f32 v22, v22\n"
    "v_mfma_f32_16x16x32_bf16 a[16:19], a[72:75], v[40:43], 0\n"
    "buffer_load_dwordx4 v35, s[16:19], s40 offen offset:1024 nt lds\n"
    "v_exp_f32 v23, v23\n"
    "v_mfma_f32_16x16x32_bf16 a[20:23], a[72:75], v[44:47], 0\n"
    "v_mul_f32 v4, v20, v18\n"
    "v_mul_f32 v5, v21, v19\n"
    "s_waitcnt lgkmcnt(8)\n"
    "v_mfma_f32_16x16x32_bf16 a[24:27], a[76:79], v[40:43], 0\n"
    "ds_read_b64_tr_b16 a[100:101], v31 offset:34816\n"
    "ds_read_b64_tr_b16 a[102:103], v31 offset:43008\n"
    "v_fmac_f32 v4, v22, v2\n"
    "v_mfma_f32_16x16x32_bf16 a[28:31], a[76:79], v[44:47], 0\n"
    "ds_read_b64_tr_b16 a[104:105], v32 offset:34816\n"
    "ds_read_b64_tr_b16 a[106:107], v32 offset:43008\n"
    "v_fmac_f32 v5, v23, v3\n"
    "v_mfma_f32_16x16x32_bf16 a[0:3], a[80:83], v[48:51], a[0:3]\n"
    "ds_read_b64_tr_b16 a[108:109], v33 offset:34816\n"
    "v_rcp_f32 v4, v4\n"
    "v_mfma_f32_16x16x32_bf16 a[4:7], a[80:83], v[52:55], a[4:7]\n"
    "ds_read_b64_tr_b16 a[110:111], v33 offset:43008\n"
    "v_rcp_f32 v5, v5\n"
    "s_waitcnt lgkmcnt(10)\n"
    "v_mfma_f32_16x16x32_bf16 a[8:11], a[84:87], v[48:51], a[8:11]\n"
    "ds_read_b64_tr_b16 a[112:113], v30 offset:35840\n"
    "v_mul_f32 v20, v20, v4\n"
    "v_mul_f32 v22, v22, v4\n"
    "v_mfma_f32_16x16x32_bf16 a[12:15], a[84:87], v[52:55], a[12:15]\n"
    "ds_read_b64_tr_b16 a[114:115], v30 offset:44032\n"
    "v_mul_f32 v21, v21, v5\n"
    "v_mul_f32 v23, v23, v5\n"
    "v_mfma_f32_16x16x32_bf16 a[16:19], a[88:91], v[48:51], a[16:19]\n"
    "buffer_load_dwordx4 v36, s[16:19], s40 offen offset:2048 nt lds\n"
    "s_waitcnt vmcnt(16)\n"
    "v_mfma_f32_16x16x32_bf16 a[20:23], a[88:91], v[52:55], a[20:23]\n"
    "v_accvgpr_read_b32 v5, a32\n"
    "v_accvgpr_read_b32 v7, a33\n"
    "s_waitcnt lgkmcnt(8)\n"
    "v_mfma_f32_16x16x32_bf16 a[24:27], a[92:95], v[48:51], a[24:27]\n"
    "ds_read_b64_tr_b16 a[116:117], v31 offset:35840\n"
    "ds_read_b64_tr_b16 a[118:119], v31 offset:44032\n"
    "v_accvgpr_read_b32 v0, a128\n"
    "v_mfma_f32_16x16x32_bf16 a[28:31], a[92:95], v[52:55], a[28:31]\n"
    "ds_read_b64_tr_b16 a[120:121], v32 offset:35840\n"
    "ds_read_b64_tr_b16 a[122:123], v32 offset:44032\n"
    "v_accvgpr_read_b32 v1, a129\n"
    "v_mfma_f32_16x16x32_bf16 a[0:3], a[96:99], v[56:59], a[0:3]\n"
    "ds_read_b64_tr_b16 a[124:125], v33 offset:35840\n"
    "s_waitcnt lgkmcnt(9)\n"
    "v_accvgpr_read_b32 v2, a130\n"
    "v_mfma_f32_16x16x32_bf16 a[4:7], a[96:99], v[60:63], a[4:7]\n"
    "ds_read_b64_tr_b16 a[126:127], v33 offset:44032\n"
    "s_barrier\n"
    "v_mfma_f32_16x16x32_bf16 a[8:11], a[100:103], v[56:59], a[8:11]\n"
    "ds_read_b64_tr_b16 a[64:65], v30 offset:49152\n"
    "v_accvgpr_read_b32 v3, a131\n"
    "v_lshlrev_b32 v4, 16, v5\n"
    "v_mfma_f32_16x16x32_bf16 a[12:15], a[100:103], v[60:63], a[12:15]\n"
    "ds_read_b64_tr_b16 a[66:67], v30 offset:57344\n"
    "v_and_b32 v5, s46, v5\n"
    "v_lshlrev_b32 v6, 16, v7\n"
    "v_mfma_f32_16x16x32_bf16 a[16:19], a[104:107], v[56:59], a[16:19]\n"
    "buffer_load_dwordx4 v37, s[16:19], s40 offen offset:3072 nt lds\n"
    "v_and_b32 v7, s46, v7\n"
    "v_mfma_f32_16x16x32_bf16 a[20:23], a[104:107], v[60:63], a[20:23]\n"
    "s_add_u32 m0, s28, 0x8000\n"
    "v_mul_f32 v0, v20, v0\n"
    "s_waitcnt lgkmcnt(8)\n"
    "v_mfma_f32_16x16x32_bf16 a[24:27], a[108:111], v[56:59], a[24:27]\n"
    "ds_read_b64_tr_b16 a[68:69], v31 offset:49152\n"
    "ds_read_b64_tr_b16 a[70:71], v31 offset:57344\n"
    "v_mul_f32 v1, v20, v1\n"
    "v_mfma_f32_16x16x32_bf16 a[28:31], a[108:111], v[60:63], a[28:31]\n"
    "ds_read_b64_tr_b16 a[72:73], v32 offset:49152\n"
    "ds_read_b64_tr_b16 a[74:75], v32 offset:57344\n"
    "v_mul_f32 v2, v20, v2\n"
    "v_mfma_f32_16x16x32_bf16 a[0:3], a[112:115], v[64:67], a[0:3]\n"
    "ds_read_b64_tr_b16 a[76:77], v33 offset:49152\n"
    "s_add_u32 s40, s31, 0x42080\n"
    "v_mul_f32 v3, v20, v3\n"
    "v_mfma_f32_16x16x32_bf16 a[4:7], a[112:115], v[68:71], a[4:7]\n"
    "ds_read_b64_tr_b16 a[78:79], v33 offset:57344\n"
    "v_fmac_f32 v0, v22, v4\n"
    "s_waitcnt lgkmcnt(10)\n"
    "v_mfma_f32_16x16x32_bf16 a[8:11], a[116:119], v[64:67], a[8:11]\n"
    "ds_read_b64_tr_b16 a[80:81], v30 offset:50176\n"
    "v_fmac_f32 v1, v22, v5\n"
    "v_fmac_f32 v2, v22, v6\n"
    "v_mfma_f32_16x16x32_bf16 a[12:15], a[116:119], v[68:71], a[12:15]\n"
    "ds_read_b64_tr_b16 a[82:83], v30 offset:58368\n"
    "v_fmac_f32 v3, v22, v7\n"
    "v_cvt_pk_bf16_f32 v8, v0, v1\n"
    "v_mfma_f32_16x16x32_bf16 a[16:19], a[120:123], v[64:67], a[16:19]\n"
    "buffer_load_dwordx4 v34, s[16:19], s40 offen offset:0 nt lds\n"
    "v_cvt_pk_bf16_f32 v9, v2, v3\n"
    "v_mfma_f32_16x16x32_bf16 a[20:23], a[120:123], v[68:71], a[20:23]\n"
    "s_waitcnt vmcnt(17)\n"
    "v_accvgpr_read_b32 v5, a36\n"
    "s_waitcnt lgkmcnt(8)\n"
    "v_mfma_f32_16x16x32_bf16 a[24:27], a[124:127], v[64:67], a[24:27]\n"
    "ds_read_b64_tr_b16 a[84:85], v31 offset:50176\n"
    "ds_read_b64_tr_b16 a[86:87], v31 offset:58368\n"
    "v_accvgpr_read_b32 v7, a37\n"
    "v_mfma_f32_16x16x32_bf16 a[28:31], a[124:127], v[68:71], a[28:31]\n"
    "ds_read_b64_tr_b16 a[88:89], v32 offset:50176\n"
    "ds_read_b64_tr_b16 a[90:91], v32 offset:58368\n"
    "v_accvgpr_read_b32 v0, a136\n"
    "s_add_u32 s38, s38, s35\n"
    "s_lshl1_add_u32 s39, s36, s38\n"
    "PL32_%=:\n"
    "s_add_u32 s39, s39, 3\n"
    "s_cmpk_lt_u32 s39, 1097\n"
    "s_cbranch_scc1 PL32_%=\n"
    "s_memrealtime s[36:37]\n"
    "v_mfma_f32_16x16x32_bf16 a[0:3], a[64:67], v[72:75], a[0:3]\n"
    "ds_read_b64_tr_b16 a[92:93], v33 offset:50176\n"
    "v_accvgpr_read_b32 v1, a137\n"
    "v_accvgpr_read_b32 v2, a138\n"
    "v_mfma_f32_16x16x32_bf16 a[4:7], a[64:67], v[76:79], a[4:7]\n"
    "ds_read_b64_tr_b16 a[94:95], v33 offset:58368\n"
    "v_accvgpr_read_b32 v3, a139\n"
    "s_waitcnt lgkmcnt(10)\n"
    "v_mfma_f32_16x16x32_bf16 a[8:11], a[68:71], v[72:75], a[8:11]\n"
    "ds_read_b64_tr_b16 a[96:97], v30 offset:51200\n"
    "v_lshlrev_b32 v4, 16, v5\n"
    "v_and_b32 v5, s46, v5\n"
    "v_mfma_f32_16x16x32_bf16 a[12:15], a[68:71], v[76:79], a[12:15]\n"
    "ds_read_b64_tr_b16 a[98:99], v30 offset:59392\n"
    "v_lshlrev_b32 v6, 16, v7\n"
    "v_and_b32 v7, s46, v7\n"
    "v_mfma_f32_16x16x32_bf16 a[16:19], a[72:75], v[72:75], a[16:19]\n"
    "buffer_load_dwordx4 v35, s[16:19], s40 offen offset:1024 nt lds\n"
    "v_mul_f32 v0, v20, v0\n"
    "v_mfma_f32_16x16x32_bf16 a[20:23], a[72:75], v[76:79], a[20:23]\n"
    "v_mul_f32 v1, v20, v1\n"
    "v_mul_f32 v2, v20, v2\n"
    "s_waitcnt lgkmcnt(8)\n"
    "v_mfma_f32_16x16x32_bf16 a[24:27], a[76:79], v[72:75], a[24:27]\n"
    "ds_read_b64_tr_b16 a[100:101], v31 offset:51200\n"
    "ds_read_b64_tr_b16 a[102:103], v31 offset:59392\n"
    "v_mul_f32 v3, v20, v3\n"
    "STUB9_%=:\n"
    "s_cbranch_execz WRET_%=\n"
    "v_mfma_f32_16x16x32_bf16 a[28:31], a[76:79], v[76:79], a[28:31]\n"
    "ds_read_b64_tr_b16 a[104:105], v32 offset:51200\n"
    "ds_read_b64_tr_b16 a[106:107], v32 offset:59392\n"
    "v_fmac_f32 v0, v22, v4\n"
    "v_mfma_f32_16x16x32_bf16 a[0:3], a[80:83], v[80:83], a[0:3]\n"
    "ds_read_b64_tr_b16 a[108:109], v33 offset:51200\n"
    "v_fmac_f32 v1, v22, v5\n"
    "v_fmac_f32 v2, v22, v6\n"
    "v_mfma_f32_16x16x32_bf16 a[4:7], a[80:83], v[84:87], a[4:7]\n"
    "ds_read_b64_tr_b16 a[110:111], v33 offset:59392\n"
    "v_fmac_f32 v3, v22, v7\n"
    "s_waitcnt lgkmcnt(10)\n"
    "v_mfma_f32_16x16x32_bf16 a[8:11], a[84:87], v[80:83], a[8:11]\n"
    "ds_read_b64_tr_b16 a[112:113], v30 offset:52224\n"
    "v_cvt_pk_bf16_f32 v10, v0, v1\n"
    "v_cvt_pk_bf16_f32 v11, v2, v3\n"
    "v_mfma_f32_16x16x32_bf16 a[12:15], a[84:87], v[84:87], a[12:15]\n"
    "ds_read_b64_tr_b16 a[114:115], v30 offset:60416\n"
    "ds_write2_b64 v26, v[8:9], v[10:11] offset0:0 offset1:4\n"
    "v_accvgpr_read_b32 v5, a34\n"
    "v_mfma_f32_16x16x32_bf16 a[16:19], a[88:91], v[80:83], a[16:19]\n"
    "buffer_load_dwordx4 v36, s[16:19], s40 offen offset:2048 nt lds\n"
    "v_accvgpr_read_b32 v7, a35\n"
    "v_mfma_f32_16x16x32_bf16 a[20:23], a[88:91], v[84:87], a[20:23]\n"
    "v_add_u32 v30, 0x10000, v30\n"
    "v_accvgpr_read_b32 v0, a132\n"
    "s_waitcnt lgkmcnt(8)\n"
    "v_mfma_f32_16x16x32_bf16 a[24:27], a[92:95], v[80:83], a[24:27]\n"
    "ds_read_b64_tr_b16 a[116:117], v31 offset:52224\n"
    "ds_read_b64_tr_b16 a[118:119], v31 offset:60416\n"
    "v_accvgpr_read_b32 v1, a133\n"
    "v_mfma_f32_16x16x32_bf16 a[28:31], a[92:95], v[84:87], a[28:31]\n"
    "ds_read_b64_tr_b16 a[120:121], v32 offset:52224\n"
    "ds_read_b64_tr_b16 a[122:123], v32 offset:60416\n"
    "v_accvgpr_read_b32 v2, a134\n"
    "v_mfma_f32_16x16x32_bf16 a[0:3], a[96:99], v[88:91], a[0:3]\n"
    "ds_read_b64_tr_b16 a[124:125], v33 offset:52224\n"
    "s_waitcnt lgkmcnt(10)\n"
    "v_accvgpr_read_b32 v3, a135\n"
    "v_mfma_f32_16x16x32_bf16 a[4:7], a[96:99], v[92:95], a[4:7]\n"
    "ds_read_b64_tr_b16 a[126:127], v33 offset:60416\n"
    "s_waitcnt lgkmcnt(10)\n"
    "s_barrier\n"
    "v_mfma_f32_16x16x32_bf16 a[8:11], a[100:103], v[88:91], a[8:11]\n"
    "ds_read_b64_tr_b16 a[64:65], v30 offset:0\n"
    "v_add_u32 v31, 0x10000, v31\n"
    "s_waitcnt vmcnt(10)\n"
    "v_mfma_f32_16x16x32_bf16 a[12:15], a[100:103], v[92:95], a[12:15]\n"
    "ds_read_b64_tr_b16 a[66:67], v30 offset:8192\n"
    "v_add_u32 v32, 0x10000, v32\n"
    "v_accvgpr_read_b32 v38, a217\n"
    "v_mfma_f32_16x16x32_bf16 a[16:19], a[104:107], v[88:91], a[16:19]\n"
    "buffer_load_dwordx4 v37, s[16:19], s40 offen offset:3072 nt lds\n"
    "v_cmp_eq_u32 vcc, v28, v38\n"
    "v_mfma_f32_16x16x32_bf16 a[20:23], a[104:107], v[92:95], a[20:23]\n"
    "v_add_u32 v33, 0x10000, v33\n"
    "s_cbranch_vccnz SPINB_%=\n"
    "BACKB_%=:\n"
    "s_waitcnt lgkmcnt(8)\n"
    "v_mfma_f32_16x16x32_bf16 a[24:27], a[108:111], v[88:91], a[24:27]\n"
    "ds_read_b64_tr_b16 a[68:69], v31 offset:0\n"
    "ds_read_b64_tr_b16 a[70:71], v31 offset:8192\n"
    "v_lshlrev_b32 v4, 16, v5\n"
    "v_mfma_f32_16x16x32_bf16 a[28:31], a[108:111], v[92:95], a[28:31]\n"
    "ds_read_b64_tr_b16 a[72:73], v32 offset:0\n"
    "ds_read_b64_tr_b16 a[74:75], v32 offset:8192\n"
    "v_and_b32 v5, s46, v5\n"
    "v_mfma_f32_16x16x32_bf16 a[0:3], a[112:115], v[96:99], a[0:3]\n"
    "ds_read_b64_tr_b16 a[76:77], v33 offset:0\n"
    "s_add_u32 m0, s28, 0xc000\n"
    "v_lshlrev_b32 v6, 16, v7\n"
    "v_mfma_f32_16x16x32_bf16 a[4:7], a[112:115], v[100:103], a[4:7]\n"
    "ds_read_b64_tr_b16 a[78:79], v33 offset:8192\n"
    "v_and_b32 v7, s46, v7\n"
    "s_waitcnt lgkmcnt(10)\n"
    "v_mfma_f32_16x16x32_bf16 a[8:11], a[116:119], v[96:99], a[8:11]\n"
    "ds_read_b64_tr_b16 a[80:81], v30 offset:1024\n"
    "s_add_u32 s40, s31, 0x62080\n"
    "v_mul_f32 v0, v21, v0\n"
    "v_mfma_f32_16x16x32_bf16 a[12:15], a[116:119], v[100:103], a[12:15]\n"
    "ds_read_b64_tr_b16 a[82:83], v30 offset:9216\n"
    "v_mul_f32 v1, v21, v1\n"
    "v_mul_f32 v2, v21, v2\n"
    "v_mfma_f32_16x16x32_bf16 a[16:19], a[120:123], v[96:99], a[16:19]\n"
    "buffer_load_dwordx4 v[40:43], v24, s[20:23], s31 offen offset:128 sc1\n"
    "v_mul_f32 v3, v21, v3\n"
    "v_mfma_f32_16x16x32_bf16 a[20:23], a[120:123], v[100:103], a[20:23]\n"
    "v_fmac_f32 v0, v23, v4\n"
    "v_fmac_f32 v1, v23, v5\n"
    "s_waitcnt lgkmcnt(8)\n"
    "v_mfma_f32_16x16x32_bf16 a[24:27], a[124:127], v[96:99], a[24:27]\n"
    "ds_read_b64_tr_b16 a[84:85], v31 offset:1024\n"
    "ds_read_b64_tr_b16 a[86:87], v31 offset:9216\n"
    "v_fmac_f32 v2, v23, v6\n"
    "v_mfma_f32_16x16x32_bf16 a[28:31], a[124:127], v[100:103], a[28:31]\n"
    "ds_read_b64_tr_b16 a[88:89], v32 offset:1024\n"
    "ds_read_b64_tr_b16 a[90:91], v32 offset:9216\n"
    "v_fmac_f32 v3, v23, v7\n"
    "s_add_u32 s38, s38, s35\n"
    "s_lshl1_add_u32 s39, s36, s38\n"
    "PL33_%=:\n"
    "s_add_u32 s39, s39, 3\n"
    "s_cmpk_lt_u32 s39, 1180\n"
    "s_cbranch_scc1 PL33_%=\n"
    "v_mfma_f32_16x16x32_bf16 a[0:3], a[64:67], v[104:107], a[0:3]\n"
    "ds_read_b64_tr_b16 a[92:93], v33 offset:1024\n"
    "v_cvt_pk_bf16_f32 v12, v0, v1\n"
    "v_cvt_pk_bf16_f32 v13, v2, v3\n"
    "v_mfma_f32_16x16x32_bf16 a[4:7], a[64:67], v[108:111], a[4:7]\n"
    "ds_read_b64_tr_b16 a[94:95], v33 offset:9216\n"
    "v_accvgpr_read_b32 v5, a38\n"
    "s_waitcnt lgkmcnt(10)\n"
    "v_mfma_f32_16x16x32_bf16 a[8:11], a[68:71], v[104:107], a[8:11]\n"
    "ds_read_b64_tr_b16 a[96:97], v30 offset:2048\n"
    "v_accvgpr_read_b32 v7, a39\n"
    "v_accvgpr_read_b32 v0, a140\n"
    "v_mfma_f32_16x16x32_bf16 a[12:15], a[68:71], v[108:111], a[12:15]\n"
    "ds_read_b64_tr_b16 a[98:99], v30 offset:10240\n"
    "v_accvgpr_read_b32 v1, a141\n"
    "v_accvgpr_read_b32 v2, a142\n"
    "v_mfma_f32_16x16x32_bf16 a[16:19], a[72:75], v[104:107], a[16:19]\n"
    "buffer_load_dwordx4 v[44:47], v24, s[20:23], s31 offen offset:640 sc1\n"
    "v_accvgpr_read_b32 v3, a143\n"
    "v_mfma_f32_16x16x32_bf16 a[20:23], a[72:75], v[108:111], a[20:23]\n"
    "v_lshlrev_b32 v4, 16, v5\n"
    "v_and_b32 v5, s46, v5\n"
    "s_waitcnt lgkmcnt(8)\n"
    "v_mfma_f32_16x16x32_bf16 a[24:27], a[76:79], v[104:107], a[24:27]\n"
    "ds_read_b64_tr_b16 a[100:101], v31 offset:2048\n"
    "ds_read_b64_tr_b16 a[102:103], v31 offset:10240\n"
    "v_lshlrev_b32 v6, 16, v7\n"
    "v_mfma_f32_16x16x32_bf16 a[28:31], a[76:79], v[108:111], a[28:31]\n"
    "ds_read_b64_tr_b16 a[104:105], v32 offset:2048\n"
    "ds_read_b64_tr_b16 a[106:107], v32 offset:10240\n"
    "v_and_b32 v7, s46, v7\n"
    "v_mfma_f32_16x16x32_bf16 a[0:3], a[80:83], v[112:115], a[0:3]\n"
    "ds_read_b64_tr_b16 a[108:109], v33 offset:2048\n"
    "v_mul_f32 v0, v21, v0\n"
    "v_mul_f32 v1, v21, v1\n"
    "v_mfma_f32_16x16x32_bf16 a[4:7], a[80:83], v[116:119], a[4:7]\n"
    "ds_read_b64_tr_b16 a[110:111], v33 offset:10240\n"
    "v_mul_f32 v2, v21, v2\n"
    "s_waitcnt lgkmcnt(10)\n"
    "v_mfma_f32_16x16x32_bf16 a[8:11], a[84:87], v[112:115], a[8:11]\n"
    "ds_read_b64_tr_b16 a[112:113], v30 offset:3072\n"
    "v_mul_f32 v3, v21, v3\n"
    "v_fmac_f32 v0, v23, v4\n"
    "v_mfma_f32_16x16x32_bf16 a[12:15], a[84:87], v[116:119], a[12:15]\n"
    "ds_read_b64_tr_b16 a[114:115], v30 offset:11264\n"
    "v_fmac_f32 v1, v23, v5\n"
    "v_fmac_f32 v2, v23, v6\n"
    "v_mfma_f32_16x16x32_bf16 a[16:19], a[88:91], v[112:115], a[16:19]\n"
    "buffer_load_dwordx4 v[48:51], v24, s[20:23], s31 offen offset:1152 sc1\n"
    "v_fmac_f32 v3, v23, v7\n"
    "v_mfma_f32_16x16x32_bf16 a[20:23], a[88:91], v[116:119], a[20:23]\n"
    "v_cvt_pk_bf16_f32 v14, v0, v1\n"
    "v_cvt_pk_bf16_f32 v15, v2, v3\n"
    "s_waitcnt lgkmcnt(8)\n"
    "v_mfma_f32_16x16x32_bf16 a[24:27], a[92:95], v[112:115], a[24:27]\n"
    "ds_read_b64_tr_b16 a[116:117], v31 offset:3072\n"
    "ds_read_b64_tr_b16 a[118:119], v31 offset:11264\n"
    "ds_write2_b64 v26, v[12:13], v[14:15] offset0:16 offset1:20\n"
    "v_mfma_f32_16x16x32_bf16 a[28:31], a[92:95], v[116:119], a[28:31]\n"
    "ds_read_b64_tr_b16 a[120:121], v32 offset:3072\n"
    "ds_read_b64_tr_b16 a[122:123], v32 offset:11264\n"
    "v_accvgpr_read_b32 v5, a40\n"
    "v_mfma_f32_16x16x32_bf16 a[0:3], a[96:99], v[120:123], a[0:3]\n"
    "ds_read_b64_tr_b16 a[124:125], v33 offset:3072\n"
    "s_waitcnt lgkmcnt(10)\n"
    "v_accvgpr_read_b32 v7, a41\n"
    "v_mfma_f32_16x16x32_bf16 a[4:7], a[96:99], v[124:127], a[4:7]\n"
    "ds_read_b64_tr_b16 a[126:127], v33 offset:11264\n"
    "s_waitcnt lgkmcnt(10)\n"
    "s_barrier\n"
    "v_mfma_f32_16x16x32_bf16 a[8:11], a[100:103], v[120:123], a[8:11]\n"
    "ds_read_b64_tr_b16 a[64:65], v30 offset:16384\n"
    "v_accvgpr_read_b32 v0, a144\n"
    "v_accvgpr_read_b32 v1, a145\n"
    "v_mfma_f32_16x16x32_bf16 a[12:15], a[100:103], v[124:127], a[12:15]\n"
    "ds_read_b64_tr_b16 a[66:67], v30 offset:24576\n"
    "v_accvgpr_read_b32 v2, a146\n"
    "v_accvgpr_read_b32 v3, a147\n"
    "v_mfma_f32_16x16x32_bf16 a[16:19], a[104:107], v[120:123], a[16:19]\n"
    "buffer_load_dwordx4 v[52:55], v24, s[20:23], s31 offen offset:1664 sc1\n"
    "v_accvgpr_read_b32 v38, a215\n"
    "v_mfma_f32_16x16x32_bf16 a[20:23], a[104:107], v[124:127], a[20:23]\n"
    "v_add_u32 v38, 2, v38\n"
    "buffer_store_dword v38, v27, s[24:27], s7 offen offset:1280 sc1\n"
    "s_waitcnt lgkmcnt(8)\n"
    "v_mfma_f32_16x16x32_bf16 a[24:27], a[108:111], v[120:123], a[24:27]\n"
    "ds_read_b64_tr_b16 a[68:69], v31 offset:16384\n"
    "ds_read_b64_tr_b16 a[70:71], v31 offset:24576\n"
    "v_lshlrev_b32 v4, 16, v5\n"
    "v_mfma_f32_16x16x32_bf16 a[28:31], a[108:111], v[124:127], a[28:31]\n"
    "ds_read_b64_tr_b16 a[72:73], v32 offset:16384\n"
    "ds_read_b64_tr_b16 a[74:75], v32 offset:24576\n"
    "v_and_b32 v5, s46, v5\n"
    "v_mfma_f32_16x16x32_bf16 a[0:3], a[112:115], v[128:131], a[0:3]\n"
    "ds_read_b64_tr_b16 a[76:77], v33 offset:16384\n"
    "v_lshlrev_b32 v6, 16, v7\n"
    "v_and_b32 v7, s46, v7\n"
    "v_mfma_f32_16x16x32_bf16 a[4:7], a[112:115], v[132:135], a[4:7]\n"
    "ds_read_b64_tr_b16 a[78:79], v33 offset:24576\n"
    "v_mul_f32 v0, v20, v0\n"
    "s_waitcnt lgkmcnt(10)\n"
    "v_mfma_f32_16x16x32_bf16 a[8:11], a[116:119], v[128:131], a[8:11]\n"
    "ds_read_b64_tr_b16 a[80:81], v30 offset:17408\n"
    "v_mul_f32 v1, v20, v1\n"
    "v_mul_f32 v2, v20, v2\n"
    "v_mfma_f32_16x16x32_bf16 a[12:15], a[116:119], v[132:135], a[12:15]\n"
    "ds_read_b64_tr_b16 a[82:83], v30 offset:25600\n"
    "v_mul_f32 v3, v20, v3\n"
    "v_fmac_f32 v0, v22, v4\n"
    "v_mfma_f32_16x16x32_bf16 a[16:19], a[120:123], v[128:131], a[16:19]\n"
    "buffer_load_dwordx4 v34, s[16:19], s40 offen offset:0 nt lds\n"
    "v_fmac_f32 v1, v22, v5\n"
    "v_mfma_f32_16x16x32_bf16 a[20:23], a[120:123], v[132:135], a[20:23]\n"
    "v_fmac_f32 v2, v22, v6\n"
    "v_fmac_f32 v3, v22, v7\n"
    "s_waitcnt lgkmcnt(8)\n"
    "v_mfma_f32_16x16x32_bf16 a[24:27], a[124:127], v[128:131], a[24:27]\n"
    "ds_read_b64_tr_b16 a[84:85], v31 offset:17408\n"
    "ds_read_b64_tr_b16 a[86:87], v31 offset:25600\n"
    "v_cvt_pk_bf16_f32 v8, v0, v1\n"
    "v_mfma_f32_16x16x32_bf16 a[28:31], a[124:127], v[132:135], a[28:31]\n"
    "ds_read_b64_tr_b16 a[88:89], v32 offset:17408\n"
    "ds_read_b64_tr_b16 a[90:91], v32 offset:25600\n"
    "v_cvt_pk_bf16_f32 v9, v2, v3\n"
    "v_mfma_f32_16x16x32_bf16 a[0:3], a[64:67], v[136:139], a[0:3]\n"
    "ds_read_b64_tr_b16 a[92:93], v33 offset:17408\n"
    "v_accvgpr_read_b32 v5, a44\n"
    "v_accvgpr_read_b32 v7, a45\n"
    "v_mfma_f32_16x16x32_bf16 a[4:7], a[64:67], v[140:143], a[4:7]\n"
    "ds_read_b64_tr_b16 a[94:95], v33 offset:25600\n"
    "v_accvgpr_read_b32 v0, a152\n"
    "s_waitcnt lgkmcnt(10)\n"
    "v_mfma_f32_16x16x32_bf16 a[8:11], a[68:71], v[136:139], a[8:11]\n"
    "ds_read_b64_tr_b16 a[96:97], v30 offset:18432\n"
    "v_accvgpr_read_b32 v1, a153\n"
    "v_accvgpr_read_b32 v2, a154\n"
    "v_mfma_f32_16x16x32_bf16 a[12:15], a[68:71], v[140:143], a[12:15]\n"
    "ds_read_b64_tr_b16 a[98:99], v30 offset:26624\n"
    "v_accvgpr_read_b32 v3, a155\n"
    "v_lshlrev_b32 v4, 16, v5\n"
    "v_mfma_f32_16x16x32_bf16 a[16:19], a[72:75], v[136:139], a[16:19]\n"
    "buffer_load_dwordx4 v35, s[16:19], s40 offen offset:1024 nt lds\n"
    "v_and_b32 v5, s46, v5\n"
    "v_mfma_f32_16x16x32_bf16 a[20:23], a[72:75], v[140:143], a[20:23]\n"
    "v_lshlrev_b32 v6, 16, v7\n"
    "v_and_b32 v7, s46, v7\n"
    "s_waitcnt lgkmcnt(8)\n"
    "v_mfma_f32_16x16x32_bf16 a[24:27], a[76:79], v[136:139], a[24:27]\n"
    "ds_read_b64_tr_b16 a[100:101], v31 offset:18432\n"
    "ds_read_b64_tr_b16 a[102:103], v31 offset:26624\n"
    "v_mul_f32 v0, v20, v0\n"
    "v_mfma_f32_16x16x32_bf16 a[28:31], a[76:79], v[140:143], a[28:31]\n"
    "ds_read_b64_tr_b16 a[104:105], v32 offset:18432\n"
    "ds_read_b64_tr_b16 a[106:107], v32 offset:26624\n"
    "v_mul_f32 v1, v20, v1\n"
    "v_mfma_f32_16x16x32_bf16 a[0:3], a[80:83], v[144:147], a[0:3]\n"
    "ds_read_b64_tr_b16 a[108:109], v33 offset:18432\n"
    "v_mul_f32 v2, v20, v2\n"
    "v_mul_f32 v3, v20, v3\n"
    "v_mfma_f32_16x16x32_bf16 a[4:7], a[80:83], v[148:151], a[4:7]\n"
    "ds_read_b64_tr_b16 a[110:111], v33 offset:26624\n"
    "v_fmac_f32 v0, v22, v4\n"
    "s_waitcnt lgkmcnt(10)\n"
    "v_mfma_f32_16x16x32_bf16 a[8:11], a[84:87], v[144:147], a[8:11]\n"
    "ds_read_b64_tr_b16 a[112:113], v30 offset:19456\n"
    "v_fmac_f32 v1, v22, v5\n"
    "v_fmac_f32 v2, v22, v6\n"
    "v_mfma_f32_16x16x32_bf16 a[12:15], a[84:87], v[148:151], a[12:15]\n"
    "ds_read_b64_tr_b16 a[114:115], v30 offset:27648\n"
    "v_fmac_f32 v3, v22, v7\n"
    "v_cvt_pk_bf16_f32 v10, v0, v1\n"
    "v_mfma_f32_16x16x32_bf16 a[16:19], a[88:91], v[144:147], a[16:19]\n"
    "buffer_load_dwordx4 v36, s[16:19], s40 offen offset:2048 nt lds\n"
    "v_cvt_pk_bf16_f32 v11, v2, v3\n"
    "v_mfma_f32_16x16x32_bf16 a[20:23], a[88:91], v[148:151], a[20:23]\n"
    "v_add_u32 v30, 0xfffe0000, v30\n"
    "ds_write2_b64 v26, v[8:9], v[10:11] offset0:8 offset1:12\n"
    "s_waitcnt lgkmcnt(8)\n"
    "v_mfma_f32_16x16x32_bf16 a[24:27], a[92:95], v[144:147], a[24:27]\n"
    "ds_read_b64_tr_b16 a[116:117], v31 offset:19456\n"
    "ds_read_b64_tr_b16 a[118:119], v31 offset:27648\n"
    "ds_read_b128 v[88:91], v29 offset:0\n"
    "v_mfma_f32_16x16x32_bf16 a[28:31], a[92:95], v[148:151], a[28:31]\n"
    "ds_read_b64_tr_b16 a[120:121], v32 offset:19456\n"
    "ds_read_b64_tr_b16 a[122:123], v32 offset:27648\n"
    "v_mfma_f32_16x16x32_bf16 a[0:3], a[96:99], v[152:155], a[0:3]\n"
    "ds_read_b64_tr_b16 a[124:125], v33 offset:19456\n"
    "s_waitcnt vmcnt(16) lgkmcnt(11)\n"
    "v_mfma_f32_16x16x32_bf16 a[4:7], a[96:99], v[156:159], a[4:7]\n"
    "ds_read_b64_tr_b16 a[126:127], v33 offset:27648\n"
    "s_waitcnt lgkmcnt(10)\n"
    "s_barrier\n"
    "v_mfma_f32_16x16x32_bf16 a[8:11], a[100:103], v[152:155], a[8:11]\n"
    "ds_read_b64_tr_b16 a[64:65], v30 offset:0\n"
    "v_add_u32 v31, 0xfffe0000, v31\n"
    "ds_read_b128 v[92:95], v29 offset:2176\n"
    "v_mfma_f32_16x16x32_bf16 a[12:15], a[100:103], v[156:159], a[12:15]\n"
    "ds_read_b64_tr_b16 a[66:67], v30 offset:8192\n"
    "v_add_u32 v32, 0xfffe0000, v32\n"
    "v_accvgpr_read_b32 v5, a42\n"
    "v_mfma_f32_16x16x32_bf16 a[16:19], a[104:107], v[152:155], a[16:19]\n"
    "buffer_load_dwordx4 v37, s[16:19], s40 offen offset:3072 nt lds\n"
    "v_accvgpr_read_b32 v7, a43\n"
    "v_mfma_f32_16x16x32_bf16 a[20:23], a[104:107], v[156:159], a[20:23]\n"
    "v_add_u32 v33, 0xfffe0000, v33\n"
    "v_accvgpr_read_b32 v0, a148\n"
    "s_waitcnt lgkmcnt(8)\n"
    "v_mfma_f32_16x16x32_bf16 a[24:27], a[108:111], v[152:155], a[24:27]\n"
    "ds_read_b64_tr_b16 a[68:69], v31 offset:0\n"
    "ds_read_b64_tr_b16 a[70:71], v31 offset:8192\n"
    "v_accvgpr_read_b32 v1, a149\n"
    "v_mfma_f32_16x16x32_bf16 a[28:31], a[108:111], v[156:159], a[28:31]\n"
    "ds_read_b64_tr_b16 a[72:73], v32 offset:0\n"
    "ds_read_b64_tr_b16 a[74:75], v32 offset:8192\n"
    "v_accvgpr_read_b32 v2, a150\n"
    "v_mfma_f32_16x16x32_bf16 a[0:3], a[112:115], v[160:163], a[0:3]\n"
    "ds_read_b64_tr_b16 a[76:77], v33 offset:0\n"
    "v_accvgpr_read_b32 v3, a151\n"
    "v_lshlrev_b32 v4, 16, v5\n"
    "v_mfma_f32_16x16x32_bf16 a[4:7], a[112:115], v[164:167], a[4:7]\n"
    "ds_read_b64_tr_b16 a[78:79], v33 offset:8192\n"
    "v_and_b32 v5, s46, v5\n"
    "s_waitcnt lgkmcnt(10)\n"
    "v_mfma_f32_16x16x32_bf16 a[8:11], a[116:119], v[160:163], a[8:11]\n"
    "ds_read_b64_tr_b16 a[80:81], v30 offset:1024\n"
    "v_lshlrev_b32 v6, 16, v7\n"
    "v_and_b32 v7, s46, v7\n"
    "v_mfma_f32_16x16x32_bf16 a[12:15], a[116:119], v[164:167], a[12:15]\n"
    "ds_read_b64_tr_b16 a[82:83], v30 offset:9216\n"
    "v_mul_f32 v0, v21, v0\n"
    "v_mul_f32 v1, v21, v1\n"
    "v_mfma_f32_16x16x32_bf16 a[16:19], a[120:123], v[160:163], a[16:19]\n"
    "buffer_store_dwordx4 v[88:91], v25, s[20:23], s31 offen offset:0 sc1\n"
    "v_mul_f32 v2, v21, v2\n"
    "v_mfma_f32_16x16x32_bf16 a[20:23], a[120:123], v[164:167], a[20:23]\n"
    "v_mul_f32 v3, v21, v3\n"
    "v_fmac_f32 v0, v23, v4\n"
    "s_waitcnt lgkmcnt(8)\n"
    "v_mfma_f32_16x16x32_bf16 a[24:27], a[124:127], v[160:163], a[24:27]\n"
    "ds_read_b64_tr_b16 a[84:85], v31 offset:1024\n"
    "ds_read_b64_tr_b16 a[86:87], v31 offset:9216\n"
    "v_fmac_f32 v1, v23, v5\n"
    "v_mfma_f32_16x16x32_bf16 a[28:31], a[124:127], v[164:167], a[28:31]\n"
    "ds_read_b64_tr_b16 a[88:89], v32 offset:1024\n"
    "ds_read_b64_tr_b16 a[90:91], v32 offset:9216\n"
    "v_fmac_f32 v2, v23, v6\n"
    "v_mfma_f32_16x16x32_bf16 a[0:3], a[64:67], v[168:171], a[0:3]\n"
    "ds_read_b64_tr_b16 a[92:93], v33 offset:1024\n"
    "v_fmac_f32 v3, v23, v7\n"
    "v_cvt_pk_bf16_f32 v12, v0, v1\n"
    "v_mfma_f32_16x16x32_bf16 a[4:7], a[64:67], v[172:175], a[4:7]\n"
    "ds_read_b64_tr_b16 a[94:95], v33 offset:9216\n"
    "v_cvt_pk_bf16_f32 v13, v2, v3\n"
    "s_waitcnt lgkmcnt(10)\n"
    "v_mfma_f32_16x16x32_bf16 a[8:11], a[68:71], v[168:171], a[8:11]\n"
    "ds_read_b64_tr_b16 a[96:97], v30 offset:2048\n"
    "v_accvgpr_read_b32 v5, a46\n"
    "v_accvgpr_read_b32 v7, a47\n"
    "v_mfma_f32_16x16x32_bf16 a[12:15], a[68:71], v[172:175], a[12:15]\n"
    "ds_read_b64_tr_b16 a[98:99], v30 offset:10240\n"
    "v_accvgpr_read_b32 v0, a156\n"
    "v_accvgpr_read_b32 v1, a157\n"
    "v_mfma_f32_16x16x32_bf16 a[16:19], a[72:75], v[168:171], a[16:19]\n"
    "buffer_store_dwordx4 v[92:95], v25, s[20:23], s31 offen offset:1024 sc1\n"
    "v_accvgpr_read_b32 v2, a158\n"
    "v_mfma_f32_16x16x32_bf16 a[20:23], a[72:75], v[172:175], a[20:23]\n"
    "v_accvgpr_read_b32 v3, a159\n"
    "v_lshlrev_b32 v4, 16, v5\n"
    "s_waitcnt lgkmcnt(8)\n"
    "v_mfma_f32_16x16x32_bf16 a[24:27], a[76:79], v[168:171], a[24:27]\n"
    "ds_read_b64_tr_b16 a[100:101], v31 offset:2048\n"
    "ds_read_b64_tr_b16 a[102:103], v31 offset:10240\n"
    "v_and_b32 v5, s46, v5\n"
    "v_mfma_f32_16x16x32_bf16 a[28:31], a[76:79], v[172:175], a[28:31]\n"
    "ds_read_b64_tr_b16 a[104:105], v32 offset:2048\n"
    "ds_read_b64_tr_b16 a[106:107], v32 offset:10240\n"
    "v_lshlrev_b32 v6, 16, v7\n"
    "v_mfma_f32_16x16x32_bf16 a[0:3], a[80:83], v[176:179], a[0:3]\n"
    "ds_read_b64_tr_b16 a[108:109], v33 offset:2048\n"
    "v_and_b32 v7, s46, v7\n"
    "v_mul_f32 v0, v21, v0\n"
    "v_mfma_f32_16x16x32_bf16 a[4:7], a[80:83], v[180:183], a[4:7]\n"
    "ds_read_b64_tr_b16 a[110:111], v33 offset:10240\n"
    "v_mul_f32 v1, v21, v1\n"
    "s_waitcnt lgkmcnt(10)\n"
    "v_mfma_f32_16x16x32_bf16 a[8:11], a[84:87], v[176:179], a[8:11]\n"
    "ds_read_b64_tr_b16 a[112:113], v30 offset:3072\n"
    "v_mul_f32 v2, v21, v2\n"
    "v_mul_f32 v3, v21, v3\n"
    "v_mfma_f32_16x16x32_bf16 a[12:15], a[84:87], v[180:183], a[12:15]\n"
    "ds_read_b64_tr_b16 a[114:115], v30 offset:11264\n"
    "v_fmac_f32 v0, v23, v4\n"
    "v_fmac_f32 v1, v23, v5\n"
    "v_mfma_f32_16x16x32_bf16 a[16:19], a[88:91], v[176:179], a[16:19]\n"
    "ds_read_b64_tr_b16 a[116:117], v31 offset:3072\n"
    "v_fmac_f32 v2, v23, v6\n"
    "v_fmac_f32 v3, v23, v7\n"
    "v_mfma_f32_16x16x32_bf16 a[20:23], a[88:91], v[180:183], a[20:23]\n"
    "ds_read_b64_tr_b16 a[118:119], v31 offset:11264\n"
    "v_cvt_pk_bf16_f32 v14, v0, v1\n"
    "s_waitcnt lgkmcnt(10)\n"
    "v_mfma_f32_16x16x32_bf16 a[24:27], a[92:95], v[176:179], a[24:27]\n"
    "ds_read_b64_tr_b16 a[120:121], v32 offset:3072\n"
    "v_cvt_pk_bf16_f32 v15, v2, v3\n"
    "ds_write2_b64 v26, v[12:13], v[14:15] offset0:24 offset1:28\n"
    "v_mfma_f32_16x16x32_bf16 a[28:31], a[92:95], v[180:183], a[28:31]\n"
    "ds_read_b64_tr_b16 a[122:123], v32 offset:11264\n"
    "v_mfma_f32_16x16x32_bf16 a[0:3], a[96:99], v[184:187], a[0:3]\n"
    "ds_read_b64_tr_b16 a[124:125], v33 offset:3072\n"
    "s_waitcnt vmcnt(15) lgkmcnt(10)\n"
    "v_mfma_f32_16x16x32_bf16 a[4:7], a[96:99], v[188:191], a[4:7]\n"
    "ds_read_b64_tr_b16 a[126:127], v33 offset:11264\n"
    "s_waitcnt lgkmcnt(10)\n"
    "s_barrier\n"
    "v_mfma_f32_16x16x32_bf16 a[8:11], a[100:103], v[184:187], a[8:11]\n"
    "ds_read_b64_tr_b16 a[64:65], v30 offset:16384\n"
    "ds_read_b128 v[96:99], v29 offset:128\n"
    "v_mfma_f32_16x16x32_bf16 a[12:15], a[100:103], v[188:191], a[12:15]\n"
    "ds_read_b64_tr_b16 a[66:67], v30 offset:24576\n"
    "v_mfma_f32_16x16x32_bf16 a[16:19], a[104:107], v[184:187], a[16:19]\n"
    "ds_read_b64_tr_b16 a[68:69], v31 offset:16384\n"
    "v_mfma_f32_16x16x32_bf16 a[20:23], a[104:107], v[188:191], a[20:23]\n"
    "ds_read_b64_tr_b16 a[70:71], v31 offset:24576\n"
    "s_waitcnt lgkmcnt(10)\n"
    "v_mfma_f32_16x16x32_bf16 a[24:27], a[108:111], v[184:187], a[24:27]\n"
    "ds_read_b64_tr_b16 a[72:73], v32 offset:16384\n"
    "ds_read_b128 v[100:103], v29 offset:2304\n"
    "v_mfma_f32_16x16x32_bf16 a[28:31], a[108:111], v[188:191], a[28:31]\n"
    "ds_read_b64_tr_b16 a[74:75], v32 offset:24576\n"
    "v_mfma_f32_16x16x32_bf16 a[0:3], a[112:115], v[192:195], a[0:3]\n"
    "ds_read_b64_tr_b16 a[76:77], v33 offset:16384\n"
    "v_mfma_f32_16x16x32_bf16 a[4:7], a[112:115], v[196:199], a[4:7]\n"
    "ds_read_b64_tr_b16 a[78:79], v33 offset:24576\n"
    "s_waitcnt lgkmcnt(10)\n"
    "v_mfma_f32_16x16x32_bf16 a[8:11], a[116:119], v[192:195], a[8:11]\n"
    "ds_read_b64_tr_b16 a[80:81], v30 offset:17408\n"
    "v_mfma_f32_16x16x32_bf16 a[12:15], a[116:119], v[196:199], a[12:15]\n"
    "ds_read_b64_tr_b16 a[82:83], v30 offset:25600\n"
    "v_mfma_f32_16x16x32_bf16 a[16:19], a[120:123], v[192:195], a[16:19]\n"
    "ds_read_b64_tr_b16 a[84:85], v31 offset:17408\n"
    "v_mfma_f32_16x16x32_bf16 a[20:23], a[120:123], v[196:199], a[20:23]\n"
    "ds_read_b64_tr_b16 a[86:87], v31 offset:25600\n"
    "s_waitcnt lgkmcnt(10)\n"
    "v_mfma_f32_16x16x32_bf16 a[24:27], a[124:127], v[192:195], a[24:27]\n"
    "ds_read_b64_tr_b16 a[88:89], v32 offset:17408\n"
    "v_mfma_f32_16x16x32_bf16 a[28:31], a[124:127], v[196:199], a[28:31]\n"
    "ds_read_b64_tr_b16 a[90:91], v32 offset:25600\n"
    "v_mfma_f32_16x16x32_bf16 a[0:3], a[64:67], v[200:203], a[0:3]\n"
    "ds_read_b64_tr_b16 a[92:93], v33 offset:17408\n"
    "s_waitcnt vmcnt(7)\n"
    "v_mfma_f32_16x16x32_bf16 a[4:7], a[64:67], v[204:207], a[4:7]\n"
    "ds_read_b64_tr_b16 a[94:95], v33 offset:25600\n"
    "s_waitcnt lgkmcnt(10)\n"
    "v_mfma_f32_16x16x32_bf16 a[8:11], a[68:71], v[200:203], a[8:11]\n"
    "ds_read_b64_tr_b16 a[96:97], v30 offset:18432\n"
    "v_lshlrev_b32 v56, 16, v40\n"
    "v_mfma_f32_16x16x32_bf16 a[12:15], a[68:71], v[204:207], a[12:15]\n"
    "ds_read_b64_tr_b16 a[98:99], v30 offset:26624\n"
    "v_and_b32 v57, s46, v40\n"
    "v_mfma_f32_16x16x32_bf16 a[16:19], a[72:75], v[200:203], a[16:19]\n"
    "buffer_store_dwordx4 v[96:99], v25, s[20:23], s31 offen offset:512 sc1\n"
    "v_mfma_f32_16x16x32_bf16 a[20:23], a[72:75], v[204:207], a[20:23]\n"
    "v_lshlrev_b32 v58, 16, v41\n"
    "s_waitcnt lgkmcnt(8)\n"
    "v_mfma_f32_16x16x32_bf16 a[24:27], a[76:79], v[200:203], a[24:27]\n"
    "ds_read_b64_tr_b16 a[100:101], v31 offset:18432\n"
    "ds_read_b64_tr_b16 a[102:103], v31 offset:26624\n"
    "v_mfma_f32_16x16x32_bf16 a[28:31], a[76:79], v[204:207], a[28:31]\n"
    "ds_read_b64_tr_b16 a[104:105], v32 offset:18432\n"
    "ds_read_b64_tr_b16 a[106:107], v32 offset:26624\n"
    "v_mfma_f32_16x16x32_bf16 a[0:3], a[80:83], v[208:211], a[0:3]\n"
    "ds_read_b64_tr_b16 a[108:109], v33 offset:18432\n"
    "v_and_b32 v59, s46, v41\n"
    "v_mfma_f32_16x16x32_bf16 a[4:7], a[80:83], v[212:215], a[4:7]\n"
    "ds_read_b64_tr_b16 a[110:111], v33 offset:26624\n"
    "s_waitcnt lgkmcnt(10)\n"
    "v_mfma_f32_16x16x32_bf16 a[8:11], a[84:87], v[208:211], a[8:11]\n"
    "ds_read_b64_tr_b16 a[112:113], v30 offset:19456\n"
    "v_mul_f32 v56, v22, v56\n"
    "v_mfma_f32_16x16x32_bf16 a[12:15], a[84:87], v[212:215], a[12:15]\n"
    "ds_read_b64_tr_b16 a[114:115], v30 offset:27648\n"
    "v_mul_f32 v57, v22, v57\n"
    "v_mfma_f32_16x16x32_bf16 a[16:19], a[88:91], v[208:211], a[16:19]\n"
    "buffer_store_dwordx4 v[100:103], v25, s[20:23], s31 offen offset:1536 sc1\n"
    "v_mfma_f32_16x16x32_bf16 a[20:23], a[88:91], v[212:215], a[20:23]\n"
    "v_mul_f32 v58, v22, v58\n"
    "s_waitcnt lgkmcnt(8)\n"
    "v_mfma_f32_16x16x32_bf16 a[24:27], a[92:95], v[208:211], a[24:27]\n"
    "ds_read_b64_tr_b16 a[116:117], v31 offset:19456\n"
    "ds_read_b64_tr_b16 a[118:119], v31 offset:27648\n"
    "v_mfma_f32_16x16x32_bf16 a[28:31], a[92:95], v[212:215], a[28:31]\n"
    "ds_read_b64_tr_b16 a[120:121], v32 offset:19456\n"
    "ds_read_b64_tr_b16 a[122:123], v32 offset:27648\n"
    "v_mfma_f32_16x16x32_bf16 a[0:3], a[96:99], v[216:219], a[0:3]\n"
    "ds_read_b64_tr_b16 a[124:125], v33 offset:19456\n"
    "s_waitcnt lgkmcnt(9)\n"
    "v_mfma_f32_16x16x32_bf16 a[4:7], a[96:99], v[220:223], a[4:7]\n"
    "ds_read_b64_tr_b16 a[126:127], v33 offset:27648\n"
    "s_barrier\n"
    "v_mfma_f32_16x16x32_bf16 a[8:11], a[100:103], v[216:219], a[8:11]\n"
    "ds_read_b64_tr_b16 a[64:65], v30 offset:32768\n"
    "v_mul_f32 v59, v22, v59\n"
    "v_mfma_f32_16x16x32_bf16 a[12:15], a[100:103], v[220:223], a[12:15]\n"
    "ds_read_b64_tr_b16 a[66:67], v30 offset:40960\n"
    "v_lshlrev_b32 v60, 16, v42\n"
    "v_mfma_f32_16x16x32_bf16 a[16:19], a[104:107], v[216:219], a[16:19]\n"
    "ds_read_b64_tr_b16 a[68:69], v31 offset:32768\n"
    "v_and_b32 v61, s46, v42\n"
    "v_mfma_f32_16x16x32_bf16 a[20:23], a[104:107], v[220:223], a[20:23]\n"
    "ds_read_b64_tr_b16 a[70:71], v31 offset:40960\n"
    "s_waitcnt lgkmcnt(10)\n"
    "v_mfma_f32_16x16x32_bf16 a[24:27], a[108:111], v[216:219], a[24:27]\n"
    "ds_read_b64_tr_b16 a[72:73], v32 offset:32768\n"
    "v_lshlrev_b32 v62, 16, v43\n"
    "v_mfma_f32_16x16x32_bf16 a[28:31], a[108:111], v[220:223], a[28:31]\n"
    "ds_read_b64_tr_b16 a[74:75], v32 offset:40960\n"
    "v_and_b32 v63, s46, v43\n"
    "v_mfma_f32_16x16x32_bf16 a[0:3], a[112:115], v[224:227], a[0:3]\n"
    "ds_read_b64_tr_b16 a[76:77], v33 offset:32768\n"
    "v_mul_f32 v60, v23, v60\n"
    "v_mfma_f32_16x16x32_bf16 a[4:7], a[112:115], v[228:231], a[4:7]\n"
    "ds_read_b64_tr_b16 a[78:79], v33 offset:40960\n"
    "s_waitcnt lgkmcnt(10)\n"
    "v_mfma_f32_16x16x32_bf16 a[8:11], a[116:119], v[224:227], a[8:11]\n"
    "ds_read_b64_tr_b16 a[80:81], v30 offset:33792\n"
    "v_mul_f32 v61, v23, v61\n"
    "v_mfma_f32_16x16x32_bf16 a[12:15], a[116:119], v[228:231], a[12:15]\n"
    "ds_read_b64_tr_b16 a[82:83], v30 offset:41984\n"
    "v_mul_f32 v62, v23, v62\n"
    "v_mfma_f32_16x16x32_bf16 a[16:19], a[120:123], v[224:227], a[16:19]\n"
    "ds_read_b64_tr_b16 a[84:85], v31 offset:33792\n"
    "v_mul_f32 v63, v23, v63\n"
    "v_mfma_f32_16x16x32_bf16 a[20:23], a[120:123], v[228:231], a[20:23]\n"
    "ds_read_b64_tr_b16 a[86:87], v31 offset:41984\n"
    "s_waitcnt lgkmcnt(10)\n"
    "v_mfma_f32_16x16x32_bf16 a[24:27], a[124:127], v[224:227], a[24:27]\n"
    "ds_read_b64_tr_b16 a[88:89], v32 offset:33792\n"
    "v_lshlrev_b32 v64, 16, v44\n"
    "v_mfma_f32_16x16x32_bf16 a[28:31], a[124:127], v[228:231], a[28:31]\n"
    "ds_read_b64_tr_b16 a[90:91], v32 offset:41984\n"
    "v_and_b32 v65, s46, v44\n"
    "v_mfma_f32_16x16x32_bf16 a[0:3], a[64:67], v[232:235], a[0:3]\n"
    "ds_read_b64_tr_b16 a[92:93], v33 offset:33792\n"
    "v_lshlrev_b32 v66, 16, v45\n"
    "v_mfma_f32_16x16x32_bf16 a[4:7], a[64:67], v[236:239], a[4:7]\n"
    "ds_read_b64_tr_b16 a[94:95], v33 offset:41984\n"
    "s_waitcnt lgkmcnt(10)\n"
    "v_mfma_f32_16x16x32_bf16 a[8:11], a[68:71], v[232:235], a[8:11]\n"
    "ds_read_b64_tr_b16 a[96:97], v30 offset:34816\n"
    "v_and_b32 v67, s46, v45\n"
    "v_mfma_f32_16x16x32_bf16 a[12:15], a[68:71], v[236:239], a[12:15]\n"
    "ds_read_b64_tr_b16 a[98:99], v30 offset:43008\n"
    "v_mul_f32 v64, v22, v64\n"
    "v_mfma_f32_16x16x32_bf16 a[16:19], a[72:75], v[232:235], a[16:19]\n"
    "ds_read_b64_tr_b16 a[100:101], v31 offset:34816\n"
    "v_mul_f32 v65, v22, v65\n"
    "v_mfma_f32_16x16x32_bf16 a[20:23], a[72:75], v[236:239], a[20:23]\n"
    "ds_read_b64_tr_b16 a[102:103], v31 offset:43008\n"
    "s_waitcnt lgkmcnt(10)\n"
    "v_mfma_f32_16x16x32_bf16 a[24:27], a[76:79], v[232:235], a[24:27]\n"
    "ds_read_b64_tr_b16 a[104:105], v32 offset:34816\n"
    "v_mul_f32 v66, v22, v66\n"
    "v_mfma_f32_16x16x32_bf16 a[28:31], a[76:79], v[236:239], a[28:31]\n"
    "ds_read_b64_tr_b16 a[106:107], v32 offset:43008\n"
    "v_mul_f32 v67, v22, v67\n"
    "v_mfma_f32_16x16x32_bf16 a[0:3], a[80:83], v[240:243], a[0:3]\n"
    "ds_read_b64_tr_b16 a[108:109], v33 offset:34816\n"
    "v_lshlrev_b32 v68, 16, v46\n"
    "v_mfma_f32_16x16x32_bf16 a[4:7], a[80:83], v[244:247], a[4:7]\n"
    "ds_read_b64_tr_b16 a[110:111], v33 offset:43008\n"
    "s_waitcnt lgkmcnt(10)\n"
    "v_mfma_f32_16x16x32_bf16 a[8:11], a[84:87], v[240:243], a[8:11]\n"
    "ds_read_b64_tr_b16 a[112:113], v30 offset:35840\n"
    "v_and_b32 v69, s46, v46\n"
    "v_mfma_f32_16x16x32_bf16 a[12:15], a[84:87], v[244:247], a[12:15]\n"
    "ds_read_b64_tr_b16 a[114:115], v30 offset:44032\n"
    "v_lshlrev_b32 v70, 16, v47\n"
    "v_mfma_f32_16x16x32_bf16 a[16:19], a[88:91], v[240:243], a[16:19]\n"
    "ds_read_b64_tr_b16 a[116:117], v31 offset:35840\n"
    "v_and_b32 v71, s46, v47\n"
    "v_mfma_f32_16x16x32_bf16 a[20:23], a[88:91], v[244:247], a[20:23]\n"
    "ds_read_b64_tr_b16 a[118:119], v31 offset:44032\n"
    "s_waitcnt lgkmcnt(10)\n"
    "v_mfma_f32_16x16x32_bf16 a[24:27], a[92:95], v[240:243], a[24:27]\n"
    "ds_read_b64_tr_b16 a[120:121], v32 offset:35840\n"
    "v_mul_f32 v68, v23, v68\n"
    "v_mfma_f32_16x16x32_bf16 a[28:31], a[92:95], v[244:247], a[28:31]\n"
    "ds_read_b64_tr_b16 a[122:123], v32 offset:44032\n"
    "v_mul_f32 v69, v23, v69\n"
    "v_mfma_f32_16x16x32_bf16 a[0:3], a[96:99], v[248:251], a[0:3]\n"
    "ds_read_b64_tr_b16 a[124:125], v33 offset:35840\n"
    "s_waitcnt vmcnt(4) lgkmcnt(9)\n"
    "v_mfma_f32_16x16x32_bf16 a[4:7], a[96:99], v[252:255], a[4:7]\n"
    "ds_read_b64_tr_b16 a[126:127], v33 offset:44032\n"
    "s_barrier\n"
    "v_mfma_f32_16x16x32_bf16 a[8:11], a[100:103], v[248:251], a[8:11]\n"
    "ds_read_b64_tr_b16 a[64:65], v30 offset:49152\n"
    "v_mul_f32 v70, v23, v70\n"
    "STUB10_%=:\n"
    "s_cbranch_execz WRET_%=\n"
    "v_mfma_f32_16x16x32_bf16 a[12:15], a[100:103], v[252:255], a[12:15]\n"
    "ds_read_b64_tr_b16 a[66:67], v30 offset:57344\n"
    "v_mul_f32 v71, v23, v71\n"
    "v_mfma_f32_16x16x32_bf16 a[16:19], a[104:107], v[248:251], a[16:19]\n"
    "ds_read_b64_tr_b16 a[68:69], v31 offset:49152\n"
    "v_lshlrev_b32 v72, 16, v48\n"
    "v_mfma_f32_16x16x32_bf16 a[20:23], a[104:107], v[252:255], a[20:23]\n"
    "ds_read_b64_tr_b16 a[70:71], v31 offset:57344\n"
    "s_waitcnt lgkmcnt(10)\n"
    "v_mfma_f32_16x16x32_bf16 a[24:27], a[108:111], v[248:251], a[24:27]\n"
    "ds_read_b64_tr_b16 a[72:73], v30 offset:50176\n"
    "v_and_b32 v73, s46, v48\n"
    "v_mfma_f32_16x16x32_bf16 a[28:31], a[108:111], v[252:255], a[28:31]\n"
    "ds_read_b64_tr_b16 a[74:75], v30 offset:58368\n"
    "v_lshlrev_b32 v74, 16, v49\n"
    "v_mfma_f32_16x16x32_bf16 a[0:3], a[112:115], a[160:163], a[0:3]\n"
    "ds_read_b64_tr_b16 a[76:77], v31 offset:50176\n"
    "v_and_b32 v75, s46, v49\n"
    "v_mfma_f32_16x16x32_bf16 a[4:7], a[112:115], a[164:167], a[4:7]\n"
    "ds_read_b64_tr_b16 a[78:79], v31 offset:58368\n"
    "s_waitcnt lgkmcnt(10)\n"
    "v_mfma_f32_16x16x32_bf16 a[8:11], a[116:119], a[160:163], a[8:11]\n"
    "ds_read_b64_tr_b16 a[80:81], v30 offset:51200\n"
    "v_mul_f32 v72, v22, v72\n"
    "v_mfma_f32_16x16x32_bf16 a[12:15], a[116:119], a[164:167], a[12:15]\n"
    "ds_read_b64_tr_b16 a[82:83], v30 offset:59392\n"
    "v_mul_f32 v73, v22, v73\n"
    "v_mfma_f32_16x16x32_bf16 a[16:19], a[120:123], a[160:163], a[16:19]\n"
    "ds_read_b64_tr_b16 a[84:85], v31 offset:51200\n"
    "v_mul_f32 v74, v22, v74\n"
    "v_mfma_f32_16x16x32_bf16 a[20:23], a[120:123], a[164:167], a[20:23]\n"
    "ds_read_b64_tr_b16 a[86:87], v31 offset:59392\n"
    "s_waitcnt lgkmcnt(10)\n"
    "v_mfma_f32_16x16x32_bf16 a[24:27], a[124:127], a[160:163], a[24:27]\n"
    "ds_read_b64_tr_b16 a[88:89], v30 offset:52224\n"
    "v_mul_f32 v75, v22, v75\n"
    "v_mfma_f32_16x16x32_bf16 a[28:31], a[124:127], a[164:167], a[28:31]\n"
    "ds_read_b64_tr_b16 a[90:91], v30 offset:60416\n"
    "v_lshlrev_b32 v76, 16, v50\n"
    "v_mfma_f32_16x16x32_bf16 a[0:3], a[64:67], a[168:171], a[0:3]\n"
    "ds_read_b64_tr_b16 a[92:93], v31 offset:52224\n"
    "v_and_b32 v77, s46, v50\n"
    "v_mfma_f32_16x16x32_bf16 a[4:7], a[64:67], a[172:175], a[4:7]\n"
    "ds_read_b64_tr_b16 a[94:95], v31 offset:60416\n"
    "s_waitcnt lgkmcnt(10)\n"
    "v_mfma_f32_16x16x32_bf16 a[8:11], a[68:71], a[168:171], a[8:11]\n"
    "ds_read_b64_tr_b16 a[96:97], v32 offset:49152\n"
    "v_lshlrev_b32 v78, 16, v51\n"
    "v_mfma_f32_16x16x32_bf16 a[12:15], a[68:71], a[172:175], a[12:15]\n"
    "ds_read_b64_tr_b16 a[98:99], v32 offset:57344\n"
    "v_and_b32 v79, s46, v51\n"
    "v_mfma_f32_16x16x32_bf16 a[0:3], a[72:75], a[176:179], a[0:3]\n"
    "ds_read_b64_tr_b16 a[100:101], v33 offset:49152\n"
    "v_mul_f32 v76, v23, v76\n"
    "v_mfma_f32_16x16x32_bf16 a[4:7], a[72:75], a[180:183], a[4:7]\n"
    "ds_read_b64_tr_b16 a[102:103], v33 offset:57344\n"
    "s_waitcnt lgkmcnt(10)\n"
    "v_mfma_f32_16x16x32_bf16 a[8:11], a[76:79], a[176:179], a[8:11]\n"
    "ds_read_b64_tr_b16 a[104:105], v32 offset:50176\n"
    "v_mul_f32 v77, v23, v77\n"
    "v_mfma_f32_16x16x32_bf16 a[12:15], a[76:79], a[180:183], a[12:15]\n"
    "ds_read_b64_tr_b16 a[106:107], v32 offset:58368\n"
    "v_mul_f32 v78, v23, v78\n"
    "v_mfma_f32_16x16x32_bf16 a[0:3], a[80:83], a[184:187], a[0:3]\n"
    "ds_read_b64_tr_b16 a[108:109], v33 offset:50176\n"
    "v_mul_f32 v79, v23, v79\n"
    "v_mfma_f32_16x16x32_bf16 a[4:7], a[80:83], a[188:191], a[4:7]\n"
    "ds_read_b64_tr_b16 a[110:111], v33 offset:58368\n"
    "s_waitcnt lgkmcnt(10)\n"
    "v_mfma_f32_16x16x32_bf16 a[8:11], a[84:87], a[184:187], a[8:11]\n"
    "ds_read_b64_tr_b16 a[112:113], v32 offset:51200\n"
    "v_lshlrev_b32 v80, 16, v52\n"
    "v_mfma_f32_16x16x32_bf16 a[12:15], a[84:87], a[188:191], a[12:15]\n"
    "ds_read_b64_tr_b16 a[114:115], v32 offset:59392\n"
    "v_and_b32 v81, s46, v52\n"
    "v_mfma_f32_16x16x32_bf16 a[0:3], a[88:91], a[192:195], a[0:3]\n"
    "ds_read_b64_tr_b16 a[116:117], v33 offset:51200\n"
    "v_lshlrev_b32 v82, 16, v53\n"
    "v_mfma_f32_16x16x32_bf16 a[4:7], a[88:91], a[196:199], a[4:7]\n"
    "ds_read_b64_tr_b16 a[118:119], v33 offset:59392\n"
    "s_waitcnt lgkmcnt(10)\n"
    "v_mfma_f32_16x16x32_bf16 a[8:11], a[92:95], a[192:195], a[8:11]\n"
    "ds_read_b64_tr_b16 a[120:121], v32 offset:52224\n"
    "v_and_b32 v83, s46, v53\n"
    "v_mfma_f32_16x16x32_bf16 a[12:15], a[92:95], a[196:199], a[12:15]\n"
    "ds_read_b64_tr_b16 a[122:123], v32 offset:60416\n"
    "v_mul_f32 v80, v22, v80\n"
    "v_mfma_f32_16x16x32_bf16 a[16:19], a[96:99], a[168:171], a[16:19]\n"
    "ds_read_b64_tr_b16 a[124:125], v33 offset:52224\n"
    "v_mul_f32 v81, v22, v81\n"
    "v_mul_f32 v82, v22, v82\n"
    "v_mul_f32 v83, v22, v83\n"
    "v_mfma_f32_16x16x32_bf16 a[20:23], a[96:99], a[172:175], a[20:23]\n"
    "ds_read_b64_tr_b16 a[126:127], v33 offset:60416\n"
    "v_lshlrev_b32 v84, 16, v54\n"
    "v_and_b32 v85, s46, v54\n"
    "s_waitcnt lgkmcnt(10)\n"
    "v_mfma_f32_16x16x32_bf16 a[24:27], a[100:103], a[168:171], a[24:27]\n"
    "v_lshlrev_b32 v86, 16, v55\n"
    "v_and_b32 v87, s46, v55\n"
    "v_mul_f32 v84, v23, v84\n"
    "v_mul_f32 v85, v23, v85\n"
    "v_mfma_f32_16x16x32_bf16 a[28:31], a[100:103], a[172:175], a[28:31]\n"
    "v_mul_f32 v86, v23, v86\n"
    "v_mul_f32 v87, v23, v87\n"
    "v_accvgpr_read_b32 v0, a0\n"
    "v_accvgpr_read_b32 v1, a1\n"
    "v_mfma_f32_16x16x32_bf16 a[16:19], a[104:107], a[176:179], a[16:19]\n"
    "v_accvgpr_read_b32 v2, a2\n"
    "v_accvgpr_read_b32 v3, a3\n"
    "v_accvgpr_read_b32 v4, a8\n"
    "v_accvgpr_read_b32 v5, a9\n"
    "v_mfma_f32_16x16x32_bf16 a[20:23], a[104:107], a[180:183], a[20:23]\n"
    "v_accvgpr_read_b32 v6, a10\n"
    "v_accvgpr_read_b32 v7, a11\n"
    "v_fmac_f32 v56, v20, v0\n"
    "s_waitcnt lgkmcnt(6)\n"
    "v_mfma_f32_16x16x32_bf16 a[24:27], a[108:111], a[176:179], a[24:27]\n"
    "v_fmac_f32 v57, v20, v1\n"
    "v_fmac_f32 v58, v20, v2\n"
    "v_fmac_f32 v59, v20, v3\n"
    "v_fmac_f32 v64, v20, v4\n"
    "v_mfma_f32_16x16x32_bf16 a[28:31], a[108:111], a[180:183], a[28:31]\n"
    "v_fmac_f32 v65, v20, v5\n"
    "v_fmac_f32 v66, v20, v6\n"
    "v_fmac_f32 v67, v20, v7\n"
    "v_cvt_pk_bf16_f32 v8, v56, v57\n"
    "v_mfma_f32_16x16x32_bf16 a[16:19], a[112:115], a[184:187], a[16:19]\n"
    "v_cvt_pk_bf16_f32 v9, v58, v59\n"
    "v_cvt_pk_bf16_f32 v10, v64, v65\n"
    "v_cvt_pk_bf16_f32 v11, v66, v67\n"
    "ds_write2_b64 v26, v[8:9], v[10:11] offset0:0 offset1:4\n"
    "v_mfma_f32_16x16x32_bf16 a[20:23], a[112:115], a[188:191], a[20:23]\n"
    "v_accvgpr_read_b32 v0, a4\n"
    "v_accvgpr_read_b32 v1, a5\n"
    "v_accvgpr_read_b32 v2, a6\n"
    "s_waitcnt lgkmcnt(3)\n"
    "v_mfma_f32_16x16x32_bf16 a[24:27], a[116:119], a[184:187], a[24:27]\n"
    "v_accvgpr_read_b32 v3, a7\n"
    "v_accvgpr_read_b32 v4, a12\n"
    "v_accvgpr_read_b32 v5, a13\n"
    "v_accvgpr_read_b32 v6, a14\n"
    "v_mfma_f32_16x16x32_bf16 a[28:31], a[116:119], a[188:191], a[28:31]\n"
    "v_accvgpr_read_b32 v7, a15\n"
    "v_fmac_f32 v60, v21, v0\n"
    "v_fmac_f32 v61, v21, v1\n"
    "v_fmac_f32 v62, v21, v2\n"
    "v_mfma_f32_16x16x32_bf16 a[16:19], a[120:123], a[192:195], a[16:19]\n"
    "v_fmac_f32 v63, v21, v3\n"
    "v_fmac_f32 v68, v21, v4\n"
    "v_fmac_f32 v69, v21, v5\n"
    "v_fmac_f32 v70, v21, v6\n"
    "v_mfma_f32_16x16x32_bf16 a[20:23], a[120:123], a[196:199], a[20:23]\n"
    "v_fmac_f32 v71, v21, v7\n"
    "v_cvt_pk_bf16_f32 v12, v60, v61\n"
    "v_cvt_pk_bf16_f32 v13, v62, v63\n"
    "s_waitcnt lgkmcnt(1)\n"
    "v_mfma_f32_16x16x32_bf16 a[24:27], a[124:127], a[192:195], a[24:27]\n"
    "v_cvt_pk_bf16_f32 v14, v68, v69\n"
    "v_cvt_pk_bf16_f32 v15, v70, v71\n"
    "ds_write2_b64 v26, v[12:13], v[14:15] offset0:16 offset1:20\n"
    "v_mfma_f32_16x16x32_bf16 a[28:31], a[124:127], a[196:199], a[28:31]\n"
    "v_accvgpr_read_b32 v0, a16\n"
    "v_accvgpr_read_b32 v1, a17\n"
    "v_accvgpr_read_b32 v2, a18\n"
    "v_accvgpr_read_b32 v3, a19\n"
    "v_accvgpr_read_b32 v4, a24\n"
    "v_accvgpr_read_b32 v5, a25\n"
    "v_accvgpr_read_b32 v6, a26\n"
    "v_accvgpr_read_b32 v7, a27\n"
    "v_fmac_f32 v72, v20, v0\n"
    "v_fmac_f32 v73, v20, v1\n"
    "v_fmac_f32 v74, v20, v2\n"
    "v_fmac_f32 v75, v20, v3\n"
    "v_fmac_f32 v80, v20, v4\n"
    "v_fmac_f32 v81, v20, v5\n"
    "v_fmac_f32 v82, v20, v6\n"
    "v_fmac_f32 v83, v20, v7\n"
    "v_cvt_pk_bf16_f32 v8, v72, v73\n"
    "v_cvt_pk_bf16_f32 v9, v74, v75\n"
    "v_cvt_pk_bf16_f32 v10, v80, v81\n"
    "v_cvt_pk_bf16_f32 v11, v82, v83\n"
    "ds_write2_b64 v26, v[8:9], v[10:11] offset0:8 offset1:12\n"
    "ds_read_b128 v[88:91], v29 offset:0\n"
    "ds_read_b128 v[92:95], v29 offset:2176\n"
    "v_accvgpr_read_b32 v0, a20\n"
    "v_accvgpr_read_b32 v1, a21\n"
    "v_accvgpr_read_b32 v2, a22\n"
    "v_accvgpr_read_b32 v3, a23\n"
    "v_accvgpr_read_b32 v4, a28\n"
    "v_accvgpr_read_b32 v5, a29\n"
    "v_accvgpr_read_b32 v6, a30\n"
    "v_accvgpr_read_b32 v7, a31\n"
    "v_fmac_f32 v76, v21, v0\n"
    "v_fmac_f32 v77, v21, v1\n"
    "v_fmac_f32 v78, v21, v2\n"
    "v_fmac_f32 v79, v21, v3\n"
    "v_fmac_f32 v84, v21, v4\n"
    "v_fmac_f32 v85, v21, v5\n"
    "v_fmac_f32 v86, v21, v6\n"
    "v_fmac_f32 v87, v21, v7\n"
    "v_cvt_pk_bf16_f32 v12, v76, v77\n"
    "v_cvt_pk_bf16_f32 v13, v78, v79\n"
    "v_cvt_pk_bf16_f32 v14, v84, v85\n"
    "v_cvt_pk_bf16_f32 v15, v86, v87\n"
    "ds_write2_b64 v26, v[12:13], v[14:15] offset0:24 offset1:28\n"
    "ds_read_b128 v[96:99], v29 offset:128\n"
    "ds_read_b128 v[100:103], v29 offset:2304\n"
    "s_waitcnt lgkmcnt(4)\n"
    "buffer_store_dwordx4 v[88:91], v25, s[20:23], s31 offen offset:128 sc1\n"
    "s_waitcnt lgkmcnt(3)\n"
    "buffer_store_dwordx4 v[92:95], v25, s[20:23], s31 offen offset:1152 sc1\n"
    "s_waitcnt lgkmcnt(1)\n"
    "buffer_store_dwordx4 v[96:99], v25, s[20:23], s31 offen offset:640 sc1\n"
    "s_waitcnt lgkmcnt(0)\n"
    "buffer_store_dwordx4 v[100:103], v25, s[20:23], s31 offen offset:1664 sc1\n"
    "s_endpgm\n"
    "HOTEND_%=:\n"
    "FAST_%=:\n"
    "FST2_%=:\n"
    "FST0_%=:\n"
    "s_cbranch_execz WRET2_%=\n"
    "v_mfma_f32_16x16x32_bf16 v[0:3], a[88:91], a[24:27], v[0:3]\n"
    "ds_read_b128 a[108:111], v25 offset:58368\n"
    "v_cvt_pk_bf16_f32 v110, v12, v13\n"
    "v_mfma_f32_16x16x32_bf16 v[4:7], a[88:91], a[56:59], v[4:7]\n"
    "v_exp_f32 v14, v14\n"
    "s_mov_b32 s40, 0x3400\n"
    "v_mfma_f32_16x16x32_bf16 v[0:3], a[92:95], a[28:31], v[0:3]\n"
    "ds_read_b128 a[112:115], v24 offset:59392\n"
    "v_mfma_f32_16x16x32_bf16 v[4:7], a[92:95], a[60:63], v[4:7]\n"
    "v_exp_f32 v15, v15\n"
    "s_waitcnt lgkmcnt(3)\n"
    "v_mfma_f32_16x16x32_bf16 v[8:11], a[96:99], a[0:3], v[16:19]\n"
    "ds_read_b128 a[116:119], v25 offset:59392\n"
    "v_cvt_pk_bf16_f32 v111, v14, v15\n"
    "v_mfma_f32_16x16x32_bf16 v[12:15], a[96:99], a[32:35], v[20:23]\n"
    "v_exp_f32 v0, v0\n"
    "v_mfma_f32_16x16x32_bf16 v[8:11], a[100:103], a[4:7], v[8:11]\n"
    "ds_read_b128 a[120:123], v24 offset:60416\n"
    "v_mfma_f32_16x16x32_bf16 v[12:15], a[100:103], a[36:39], v[12:15]\n"
    "v_exp_f32 v1, v1\n"
    "s_waitcnt lgkmcnt(3)\n"
    "v_mfma_f32_16x16x32_bf16 v[8:11], a[104:107], a[8:11], v[8:11]\n"
    "ds_read_b128 a[124:127], v25 offset:60416\n"
    "v_cvt_pk_bf16_f32 v120, v0, v1\n"
    "v_mfma_f32_16x16x32_bf16 v[12:15], a[104:107], a[40:43], v[12:15]\n"
    "v_exp_f32 v2, v2\n"
    "v_mfma_f32_16x16x32_bf16 a[204:207], a[208:211], v[60:63], a[204:207]\n"
    "s_waitcnt vmcnt(8)\n"
    "s_barrier\n"
    "v_mfma_f32_16x16x32_bf16 a[200:203], a[208:211], v[104:107], a[200:203]\n"
    "buffer_load_dwordx4 v34, s[12:15], s40 offen offset:0 lds\n"
    "v_mfma_f32_16x16x32_bf16 v[8:11], a[108:111], a[12:15], v[8:11]\n"
    "ds_read_b128 a[64:67], v26 offset:0\n"
    "v_mfma_f32_16x16x32_bf16 v[12:15], a[108:111], a[44:47], v[12:15]\n"
    "v_exp_f32 v3, v3\n"
    "s_waitcnt lgkmcnt(3)\n"
    "v_mfma_f32_16x16x32_bf16 v[8:11], a[112:115], a[16:19], v[8:11]\n"
    "ds_read_b128 a[68:71], v27 offset:0\n"
    "v_cvt_pk_bf16_f32 v121, v2, v3\n"
    "v_mfma_f32_16x16x32_bf16 v[12:15], a[112:115], a[48:51], v[12:15]\n"
    "v_exp_f32 v4, v4\n"
    "v_mfma_f32_16x16x32_bf16 v[8:11], a[116:119], a[20:23], v[8:11]\n"
    "ds_read_b128 a[72:75], v26 offset:1024\n"
    "buffer_load_dwordx4 v35, s[12:15], s40 offen offset:1024 lds\n"
    "v_mfma_f32_16x16x32_bf16 v[12:15], a[116:119], a[52:55], v[12:15]\n"
    "v_exp_f32 v5, v5\n"
    "s_waitcnt lgkmcnt(3)\n"
    "v_mfma_f32_16x16x32_bf16 v[8:11], a[120:123], a[24:27], v[8:11]\n"
    "ds_read_b128 a[76:79], v27 offset:1024\n"
    "v_cvt_pk_bf16_f32 v124, v4, v5\n"
    "v_mfma_f32_16x16x32_bf16 v[12:15], a[120:123], a[56:59], v[12:15]\n"
    "v_exp_f32 v6, v6\n"
    "v_mfma_f32_16x16x32_bf16 v[8:11], a[124:127], a[28:31], v[8:11]\n"
    "ds_read_b128 a[80:83], v26 offset:2048\n"
    "v_mfma_f32_16x16x32_bf16 v[12:15], a[124:127], a[60:63], v[12:15]\n"
    "v_exp_f32 v7, v7\n"
    "s_waitcnt vmcnt(6) lgkmcnt(3)\n"
    "v_mfma_f32_16x16x32_bf16 v[0:3], a[64:67], a[0:3], v[16:19]\n"
    "ds_read_b128 a[84:87], v27 offset:2048\n"
    "v_cvt_pk_bf16_f32 v125, v6, v7\n"
    "v_mfma_f32_16x16x32_bf16 v[4:7], a[64:67], a[32:35], v[20:23]\n"
    "v_exp_f32 v8, v8\n"
    "v_mfma_f32_16x16x32_bf16 v[0:3], a[68:71], a[4:7], v[0:3]\n"
    "ds_read_b128 a[88:91], v26 offset:3072\n"
    "buffer_load_dwordx4 v36, s[12:15], s40 offen offset:2048 lds\n"
    "v_mfma_f32_16x16x32_bf16 v[4:7], a[68:71], a[36:39], v[4:7]\n"
    "v_exp_f32 v9, v9\n"
    "s_waitcnt lgkmcnt(3)\n"
    "v_mfma_f32_16x16x32_bf16 v[0:3], a[72:75], a[8:11], v[0:3]\n"
    "ds_read_b128 a[92:95], v27 offset:3072\n"
    "v_cvt_pk_bf16_f32 v122, v8, v9\n"
    "v_mfma_f32_16x16x32_bf16 v[4:7], a[72:75], a[40:43], v[4:7]\n"
    "v_exp_f32 v10, v10\n"
    "v_mfma_f32_16x16x32_bf16 v[0:3], a[76:79], a[12:15], v[0:3]\n"
    "ds_read_b128 a[96:99], v26 offset:8192\n"
    "v_mfma_f32_16x16x32_bf16 v[4:7], a[76:79], a[44:47], v[4:7]\n"
    "v_exp_f32 v11, v11\n"
    "s_waitcnt lgkmcnt(3)\n"
    "v_mfma_f32_16x16x32_bf16 v[0:3], a[80:83], a[16:19], v[0:3]\n"
    "ds_read_b128 a[100:103], v27 offset:8192\n"
    "v_cvt_pk_bf16_f32 v123, v10, v11\n"
    "v_mfma_f32_16x16x32_bf16 v[4:7], a[80:83], a[48:51], v[4:7]\n"
    "v_exp_f32 v12, v12\n"
    "v_mfma_f32_16x16x32_bf16 v[0:3], a[84:87], a[20:23], v[0:3]\n"
    "ds_read_b128 a[104:107], v26 offset:9216\n"
    "buffer_load_dwordx4 v37, s[12:15], s40 offen offset:3072 lds\n"
    "v_mfma_f32_16x16x32_bf16 v[4:7], a[84:87], a[52:55], v[4:7]\n"
    "v_exp_f32 v13, v13\n"
    "s_waitcnt lgkmcnt(3)\n"
    "s_add_u32 m0, s28, 0x20000\n"
    "v_mfma_f32_16x16x32_bf16 v[0:3], a[88:91], a[24:27], v[0:3]\n"
    "ds_read_b128 a[108:111], v27 offset:9216\n"
    "v_cvt_pk_bf16_f32 v126, v12, v13\n"
    "v_mfma_f32_16x16x32_bf16 v[4:7], a[88:91], a[56:59], v[4:7]\n"
    "v_exp_f32 v14, v14\n"
    "s_mov_b32 s40, 0x20000\n"
    "v_mfma_f32_16x16x32_bf16 v[0:3], a[92:95], a[28:31], v[0:3]\n"
    "ds_read_b128 a[112:115], v26 offset:10240\n"
    "v_mfma_f32_16x16x32_bf16 v[4:7], a[92:95], a[60:63], v[4:7]\n"
    "v_exp_f32 v15, v15\n"
    "s_waitcnt lgkmcnt(3)\n"
    "v_mfma_f32_16x16x32_bf16 v[8:11], a[96:99], a[0:3], v[16:19]\n"
    "ds_read_b128 a[116:119], v27 offset:10240\n"
    "v_cvt_pk_bf16_f32 v127, v14, v15\n"
    "v_mfma_f32_16x16x32_bf16 v[12:15], a[96:99], a[32:35], v[20:23]\n"
    "v_exp_f32 v0, v0\n"
    "v_mfma_f32_16x16x32_bf16 v[8:11], a[100:103], a[4:7], v[8:11]\n"
    "ds_read_b128 a[120:123], v26 offset:11264\n"
    "buffer_load_dwordx4 v34, s[12:15], s40 offen offset:0 lds\n"
    "v_mfma_f32_16x16x32_bf16 v[12:15], a[100:103], a[36:39], v[12:15]\n"
    "v_exp_f32 v1, v1\n"
    "s_waitcnt lgkmcnt(3)\n"
    "v_mfma_f32_16x16x32_bf16 v[8:11], a[104:107], a[8:11], v[8:11]\n"
    "ds_read_b128 a[124:127], v27 offset:11264\n"
    "v_cvt_pk_bf16_f32 v48, v0, v1\n"
    "v_mfma_f32_16x16x32_bf16 v[12:15], a[104:107], a[40:43], v[12:15]\n"
    "v_exp_f32 v2, v2\n"
    "v_mfma_f32_16x16x32_bf16 a[204:207], a[208:211], v[108:111], a[204:207]\n"
    "s_barrier\n"
    "v_mfma_f32_16x16x32_bf16 a[200:203], a[208:211], v[120:123], a[200:203]\n"
    "buffer_load_dwordx4 v35, s[12:15], s40 offen offset:1024 lds\n"
    "v_mfma_f32_16x16x32_bf16 v[8:11], a[108:111], a[12:15], v[8:11]\n"
    "ds_read_b128 a[64:67], v26 offset:16384\n"
    "v_mfma_f32_16x16x32_bf16 v[12:15], a[108:111], a[44:47], v[12:15]\n"
    "v_exp_f32 v3, v3\n"
    "s_waitcnt lgkmcnt(3)\n"
    "v_mfma_f32_16x16x32_bf16 v[8:11], a[112:115], a[16:19], v[8:11]\n"
    "ds_read_b128 a[68:71], v27 offset:16384\n"
    "v_cvt_pk_bf16_f32 v49, v2, v3\n"
    "v_mfma_f32_16x16x32_bf16 v[12:15], a[112:115], a[48:51], v[12:15]\n"
    "v_exp_f32 v4, v4\n"
    "v_mfma_f32_16x16x32_bf16 v[8:11], a[116:119], a[20:23], v[8:11]\n"
    "ds_read_b128 a[72:75], v26 offset:17408\n"
    "buffer_load_dwordx4 v36, s[12:15], s40 offen offset:2048 lds\n"
    "v_mfma_f32_16x16x32_bf16 v[12:15], a[116:119], a[52:55], v[12:15]\n"
    "v_exp_f32 v5, v5\n"
    "s_waitcnt lgkmcnt(3)\n"
    "v_mfma_f32_16x16x32_bf16 v[8:11], a[120:123], a[24:27], v[8:11]\n"
    "ds_read_b128 a[76:79], v27 offset:17408\n"
    "v_cvt_pk_bf16_f32 v52, v4, v5\n"
    "v_mfma_f32_16x16x32_bf16 v[12:15], a[120:123], a[56:59], v[12:15]\n"
    "v_exp_f32 v6, v6\n"
    "v_mfma_f32_16x16x32_bf16 v[8:11], a[124:127], a[28:31], v[8:11]\n"
    "ds_read_b128 a[80:83], v26 offset:18432\n"
    "v_mfma_f32_16x16x32_bf16 v[12:15], a[124:127], a[60:63], v[12:15]\n"
    "v_exp_f32 v7, v7\n"
    "s_waitcnt vmcnt(7) lgkmcnt(3)\n"
    "v_mfma_f32_16x16x32_bf16 v[0:3], a[64:67], a[0:3], v[16:19]\n"
    "ds_read_b128 a[84:87], v27 offset:18432\n"
    "v_cvt_pk_bf16_f32 v53, v6, v7\n"
    "v_mfma_f32_16x16x32_bf16 v[4:7], a[64:67], a[32:35], v[20:23]\n"
    "v_exp_f32 v8, v8\n"
    "v_mfma_f32_16x16x32_bf16 v[0:3], a[68:71], a[4:7], v[0:3]\n"
    "ds_read_b128 a[88:91], v26 offset:19456\n"
    "buffer_load_dwordx4 v37, s[12:15], s40 offen offset:3072 lds\n"
    "v_mfma_f32_16x16x32_bf16 v[4:7], a[68:71], a[36:39], v[4:7]\n"
    "v_exp_f32 v9, v9\n"
    "s_waitcnt lgkmcnt(3)\n"
    "s_add_u32 m0, s28, 0x24000\n"
    "v_mfma_f32_16x16x32_bf16 v[0:3], a[72:75], a[8:11], v[0:3]\n"
    "ds_read_b128 a[92:95], v27 offset:19456\n"
    "v_cvt_pk_bf16_f32 v50, v8, v9\n"
    "v_mfma_f32_16x16x32_bf16 v[4:7], a[72:75], a[40:43], v[4:7]\n"
    "v_exp_f32 v10, v10\n"
    "s_mov_b32 s40, 0x21000\n"
    "v_mfma_f32_16x16x32_bf16 v[0:3], a[76:79], a[12:15], v[0:3]\n"
    "ds_read_b128 a[96:99], v26 offset:24576\n"
    "v_mfma_f32_16x16x32_bf16 v[4:7], a[76:79], a[44:47], v[4:7]\n"
    "v_exp_f32 v11, v11\n"
    "s_waitcnt lgkmcnt(3)\n"
    "v_mfma_f32_16x16x32_bf16 v[0:3], a[80:83], a[16:19], v[0:3]\n"
    "ds_read_b128 a[100:103], v27 offset:24576\n"
    "v_cvt_pk_bf16_f32 v51, v10, v11\n"
    "v_mfma_f32_16x16x32_bf16 v[4:7], a[80:83], a[48:51], v[4:7]\n"
    "v_exp_f32 v12, v12\n"
    "v_mfma_f32_16x16x32_bf16 v[0:3], a[84:87], a[20:23], v[0:3]\n"
    "ds_read_b128 a[104:107], v26 offset:25600\n"
    "ds_read_b128 a[108:111], v27 offset:25600\n"
    "buffer_load_dwordx4 v34, s[12:15], s40 offen offset:0 lds\n"
    "v_mfma_f32_16x16x32_bf16 v[4:7], a[84:87], a[52:55], v[4:7]\n"
    "v_exp_f32 v13, v13\n"
    "s_waitcnt lgkmcnt(4)\n"
    "v_mfma_f32_16x16x32_bf16 v[0:3], a[88:91], a[24:27], v[0:3]\n"
    "ds_read_b128 a[112:115], v26 offset:26624\n"
    "ds_read_b128 a[116:119], v27 offset:26624\n"
    "v_cvt_pk_bf16_f32 v54, v12, v13\n"
    "v_mfma_f32_16x16x32_bf16 v[4:7], a[88:91], a[56:59], v[4:7]\n"
    "v_exp_f32 v14, v14\n"
    "v_mfma_f32_16x16x32_bf16 v[0:3], a[92:95], a[28:31], v[0:3]\n"
    "ds_read_b128 a[120:123], v26 offset:27648\n"
    "ds_read_b128 a[124:127], v27 offset:27648\n"
    "v_mfma_f32_16x16x32_bf16 v[4:7], a[92:95], a[60:63], v[4:7]\n"
    "v_exp_f32 v15, v15\n"
    "s_waitcnt lgkmcnt(6)\n"
    "v_mfma_f32_16x16x32_bf16 a[204:207], a[208:211], v[124:127], a[204:207]\n"
    "s_barrier\n"
    "v_mfma_f32_16x16x32_bf16 v[8:11], a[96:99], a[0:3], v[16:19]\n"
    "ds_read_b128 a[64:67], v26 offset:32768\n"
    "ds_read_b128 a[68:71], v27 offset:32768\n"
    "v_cvt_pk_bf16_f32 v55, v14, v15\n"
    "v_mfma_f32_16x16x32_bf16 v[12:15], a[96:99], a[32:35], v[20:23]\n"
    "v_exp_f32 v0, v0\n"
    "v_mfma_f32_16x16x32_bf16 v[8:11], a[100:103], a[4:7], v[8:11]\n"
    "ds_read_b128 a[72:75], v26 offset:33792\n"
    "ds_read_b128 a[76:79], v27 offset:33792\n"
    "v_mfma_f32_16x16x32_bf16 v[12:15], a[100:103], a[36:39], v[12:15]\n"
    "v_exp_f32 v1, v1\n"
    "s_waitcnt lgkmcnt(8)\n"
    "v_mfma_f32_16x16x32_bf16 v[8:11], a[104:107], a[8:11], v[8:11]\n"
    "ds_read_b128 a[80:83], v26 offset:34816\n"
    "ds_read_b128 a[84:87], v27 offset:34816\n"
    "v_cvt_pk_bf16_f32 v64, v0, v1\n"
    "v_mfma_f32_16x16x32_bf16 v[12:15], a[104:107], a[40:43], v[12:15]\n"
    "v_exp_f32 v2, v2\n"
    "v_mfma_f32_16x16x32_bf16 a[200:203], a[208:211], v[48:51], a[200:203]\n"
    "buffer_load_dwordx4 v35, s[12:15], s40 offen offset:1024 lds\n"
    "v_mfma_f32_16x16x32_bf16 v[8:11], a[108:111], a[12:15], v[8:11]\n"
    "ds_read_b128 a[88:91], v26 offset:35840\n"
    "ds_read_b128 a[92:95], v27 offset:35840\n"
    "v_mfma_f32_16x16x32_bf16 v[12:15], a[108:111], a[44:47], v[12:15]\n"
    "v_exp_f32 v3, v3\n"
    "s_waitcnt lgkmcnt(10)\n"
    "v_mfma_f32_16x16x32_bf16 v[8:11], a[112:115], a[16:19], v[8:11]\n"
    "ds_read_b128 a[96:99], v26 offset:40960\n"
    "ds_read_b128 a[100:103], v27 offset:40960\n"
    "v_cvt_pk_bf16_f32 v65, v2, v3\n"
    "v_mfma_f32_16x16x32_bf16 v[12:15], a[112:115], a[48:51], v[12:15]\n"
    "v_exp_f32 v4, v4\n"
    "v_mfma_f32_16x16x32_bf16 v[8:11], a[116:119], a[20:23], v[8:11]\n"
    "ds_read_b128 a[104:107], v26 offset:41984\n"
    "buffer_load_dwordx4 v36, s[12:15], s40 offen offset:2048 lds\n"
    "v_mfma_f32_16x16x32_bf16 v[12:15], a[116:119], a[52:55], v[12:15]\n"
    "v_exp_f32 v5, v5\n"
    "s_waitcnt lgkmcnt(11)\n"
    "v_mfma_f32_16x16x32_bf16 v[8:11], a[120:123], a[24:27], v[8:11]\n"
    "ds_read_b128 a[108:111], v27 offset:41984\n"
    "v_cvt_pk_bf16_f32 v68, v4, v5\n"
    "v_mfma_f32_16x16x32_bf16 v[12:15], a[120:123], a[56:59], v[12:15]\n"
    "v_exp_f32 v6, v6\n"
    "v_mfma_f32_16x16x32_bf16 v[8:11], a[124:127], a[28:31], v[8:11]\n"
    "ds_read_b128 a[112:115], v26 offset:43008\n"
    "v_mfma_f32_16x16x32_bf16 v[12:15], a[124:127], a[60:63], v[12:15]\n"
    "v_exp_f32 v7, v7\n"
    "s_waitcnt vmcnt(7) lgkmcnt(11)\n"
    "v_mfma_f32_16x16x32_bf16 v[0:3], a[64:67], a[0:3], v[16:19]\n"
    "ds_read_b128 a[116:119], v27 offset:43008\n"
    "v_cvt_pk_bf16_f32 v69, v6, v7\n"
    "v_mfma_f32_16x16x32_bf16 v[4:7], a[64:67], a[32:35], v[20:23]\n"
    "v_exp_f32 v8, v8\n"
    "v_mfma_f32_16x16x32_bf16 v[0:3], a[68:71], a[4:7], v[0:3]\n"
    "ds_read_b128 a[120:123], v26 offset:44032\n"
    "buffer_load_dwordx4 v37, s[12:15], s40 offen offset:3072 lds\n"
    "v_mfma_f32_16x16x32_bf16 v[4:7], a[68:71], a[36:39], v[4:7]\n"
    "v_exp_f32 v9, v9\n"
    "s_waitcnt lgkmcnt(11)\n"
    "s_add_u32 m0, s28, 0x0\n"
    "v_mfma_f32_16x16x32_bf16 v[0:3], a[72:75], a[8:11], v[0:3]\n"
    "ds_read_b128 a[124:127], v27 offset:44032\n"
    "v_cvt_pk_bf16_f32 v66, v8, v9\n"
    "v_mfma_f32_16x16x32_bf16 v[4:7], a[72:75], a[40:43], v[4:7]\n"
    "v_exp_f32 v10, v10\n"
    "s_mov_b32 s40, 0x22000\n"
    "v_mfma_f32_16x16x32_bf16 a[204:207], a[208:211], v[52:55], a[204:207]\n"
    "s_barrier\n"
    "v_mfma_f32_16x16x32_bf16 v[0:3], a[76:79], a[12:15], v[0:3]\n"
    "ds_read_b128 a[64:67], v26 offset:49152\n"
    "v_mfma_f32_16x16x32_bf16 v[4:7], a[76:79], a[44:47], v[4:7]\n"
    "v_exp_f32 v11, v11\n"
    "s_waitcnt lgkmcnt(11)\n"
    "v_mfma_f32_16x16x32_bf16 v[0:3], a[80:83], a[16:19], v[0:3]\n"
    "ds_read_b128 a[68:71], v27 offset:49152\n"
    "v_cvt_pk_bf16_f32 v67, v10, v11\n"
    "v_mfma_f32_16x16x32_bf16 v[4:7], a[80:83], a[48:51], v[4:7]\n"
    "v_exp_f32 v12, v12\n"
    "v_mfma_f32_16x16x32_bf16 v[0:3], a[84:87], a[20:23], v[0:3]\n"
    "ds_read_b128 a[72:75], v26 offset:50176\n"
    "buffer_load_dwordx4 v34, s[12:15], s40 offen offset:0 lds\n"
    "v_mfma_f32_16x16x32_bf16 v[4:7], a[84:87], a[52:55], v[4:7]\n"
    "v_exp_f32 v13, v13\n"
    "s_waitcnt lgkmcnt(11)\n"
    "v_mfma_f32_16x16x32_bf16 v[0:3], a[88:91], a[24:27], v[0:3]\n"
    "ds_read_b128 a[76:79], v27 offset:50176\n"
    "v_cvt_pk_bf16_f32 v70, v12, v13\n"
    "v_mfma_f32_16x16x32_bf16 v[4:7], a[88:91], a[56:59], v[4:7]\n"
    "v_exp_f32 v14, v14\n"
    "v_mfma_f32_16x16x32_bf16 v[0:3], a[92:95], a[28:31], v[0:3]\n"
    "ds_read_b128 a[80:83], v26 offset:51200\n"
    "s_waitcnt lgkmcnt(5)\n"
    "v_mfma_f32_16x16x32_bf16 v[4:7], a[92:95], a[60:63], v[4:7]\n"
    "v_exp_f32 v15, v15\n"
    "v_mfma_f32_16x16x32_bf16 v[8:11], a[96:99], a[0:3], v[16:19]\n"
    "ds_read_b128 a[84:87], v27 offset:51200\n"
    "v_cvt_pk_bf16_f32 v71, v14, v15\n"
    "v_mfma_f32_16x16x32_bf16 v[12:15], a[96:99], a[32:35], v[20:23]\n"
    "v_exp_f32 v0, v0\n"
    "v_mfma_f32_16x16x32_bf16 v[8:11], a[100:103], a[4:7], v[8:11]\n"
    "ds_read_b128 a[88:91], v26 offset:52224\n"
    "buffer_load_dwordx4 v35, s[12:15], s40 offen offset:1024 lds\n"
    "v_mfma_f32_16x16x32_bf16 v[12:15], a[100:103], a[36:39], v[12:15]\n"
    "v_exp_f32 v1, v1\n"
    "v_mfma_f32_16x16x32_bf16 v[8:11], a[104:107], a[8:11], v[8:11]\n"
    "ds_read_b128 a[92:95], v27 offset:52224\n"
    "v_cvt_pk_bf16_f32 v112, v0, v1\n"
    "v_mfma_f32_16x16x32_bf16 v[12:15], a[104:107], a[40:43], v[12:15]\n"
    "v_exp_f32 v2, v2\n"
    "v_mfma_f32_16x16x32_bf16 a[200:203], a[208:211], v[64:67], a[200:203]\n"
    "buffer_load_dwordx4 v36, s[12:15], s40 offen offset:2048 lds\n"
    "v_mfma_f32_16x16x32_bf16 v[8:11], a[108:111], a[12:15], v[8:11]\n"
    "ds_read_b128 a[96:99], v26 offset:57344\n"
    "v_mfma_f32_16x16x32_bf16 v[12:15], a[108:111], a[44:47], v[12:15]\n"
    "v_exp_f32 v3, v3\n"
    "v_mfma_f32_16x16x32_bf16 v[8:11], a[112:115], a[16:19], v[8:11]\n"
    "ds_read_b128 a[100:103], v27 offset:57344\n"
    "v_cvt_pk_bf16_f32 v113, v2, v3\n"
    "v_mfma_f32_16x16x32_bf16 v[12:15], a[112:115], a[48:51], v[12:15]\n"
    "v_exp_f32 v4, v4\n"
    "v_mfma_f32_16x16x32_bf16 v[8:11], a[116:119], a[20:23], v[8:11]\n"
    "ds_read_b128 a[104:107], v26 offset:58368\n"
    "buffer_load_dwordx4 v37, s[12:15], s40 offen offset:3072 lds\n"
    "v_mfma_f32_16x16x32_bf16 v[12:15], a[116:119], a[52:55], v[12:15]\n"
    "v_exp_f32 v5, v5\n"
    "s_add_u32 m0, s28, 0x4000\n"
    "v_mfma_f32_16x16x32_bf16 v[8:11], a[120:123], a[24:27], v[8:11]\n"
    "ds_read_b128 a[108:111], v27 offset:58368\n"
    "v_cvt_pk_bf16_f32 v116, v4, v5\n"
    "v_mfma_f32_16x16x32_bf16 v[12:15], a[120:123], a[56:59], v[12:15]\n"
    "v_exp_f32 v6, v6\n"
    "s_mov_b32 s40, 0x23000\n"
    "v_mfma_f32_16x16x32_bf16 v[8:11], a[124:127], a[28:31], v[8:11]\n"
    "ds_read_b128 a[112:115], v26 offset:59392\n"
    "s_waitcnt vmcnt(8) lgkmcnt(5)\n"
    "v_mfma_f32_16x16x32_bf16 v[12:15], a[124:127], a[60:63], v[12:15]\n"
    "v_exp_f32 v7, v7\n"
    "v_mfma_f32_16x16x32_bf16 v[0:3], a[64:67], a[0:3], v[16:19]\n"
    "ds_read_b128 a[116:119], v27 offset:59392\n"
    "v_cvt_pk_bf16_f32 v117, v6, v7\n"
    "v_mfma_f32_16x16x32_bf16 v[4:7], a[64:67], a[32:35], v[20:23]\n"
    "v_exp_f32 v8, v8\n"
    "v_mfma_f32_16x16x32_bf16 v[0:3], a[68:71], a[4:7], v[0:3]\n"
    "ds_read_b128 a[120:123], v26 offset:60416\n"
    "buffer_load_dwordx4 v34, s[12:15], s40 offen offset:0 lds\n"
    "v_mfma_f32_16x16x32_bf16 v[4:7], a[68:71], a[36:39], v[4:7]\n"
    "v_exp_f32 v9, v9\n"
    "v_mfma_f32_16x16x32_bf16 v[0:3], a[72:75], a[8:11], v[0:3]\n"
    "ds_read_b128 a[124:127], v27 offset:60416\n"
    "v_cvt_pk_bf16_f32 v114, v8, v9\n"
    "v_mfma_f32_16x16x32_bf16 v[4:7], a[72:75], a[40:43], v[4:7]\n"
    "v_exp_f32 v10, v10\n"
    "v_mfma_f32_16x16x32_bf16 a[204:207], a[208:211], v[68:71], a[204:207]\n"
    "s_barrier\n"
    "v_mfma_f32_16x16x32_bf16 v[0:3], a[76:79], a[12:15], v[0:3]\n"
    "ds_read_b128 a[64:67], v28 offset:0\n"
    "v_mfma_f32_16x16x32_bf16 v[4:7], a[76:79], a[44:47], v[4:7]\n"
    "v_exp_f32 v11, v11\n"
    "v_mfma_f32_16x16x32_bf16 v[0:3], a[80:83], a[16:19], v[0:3]\n"
    "ds_read_b128 a[68:71], v29 offset:0\n"
    "v_cvt_pk_bf16_f32 v115, v10, v11\n"
    "v_mfma_f32_16x16x32_bf16 v[4:7], a[80:83], a[48:51], v[4:7]\n"
    "v_exp_f32 v12, v12\n"
    "v_mfma_f32_16x16x32_bf16 v[0:3], a[84:87], a[20:23], v[0:3]\n"
    "ds_read_b128 a[72:75], v28 offset:1024\n"
    "buffer_load_dwordx4 v35, s[12:15], s40 offen offset:1024 lds\n"
    "v_mfma_f32_16x16x32_bf16 v[4:7], a[84:87], a[52:55], v[4:7]\n"
    "v_exp_f32 v13, v13\n"
    "v_mfma_f32_16x16x32_bf16 v[0:3], a[88:91], a[24:27], v[0:3]\n"
    "ds_read_b128 a[76:79], v29 offset:1024\n"
    "v_cvt_pk_bf16_f32 v118, v12, v13\n"
    "v_mfma_f32_16x16x32_bf16 v[4:7], a[88:91], a[56:59], v[4:7]\n"
    "v_exp_f32 v14, v14\n"
    "v_mfma_f32_16x16x32_bf16 v[0:3], a[92:95], a[28:31], v[0:3]\n"
    "ds_read_b128 a[80:83], v28 offset:2048\n"
    "s_waitcnt lgkmcnt(5)\n"
    "v_mfma_f32_16x16x32_bf16 v[4:7], a[92:95], a[60:63], v[4:7]\n"
    "v_exp_f32 v15, v15\n"
    "v_mfma_f32_16x16x32_bf16 v[8:11], a[96:99], a[0:3], v[16:19]\n"
    "ds_read_b128 a[84:87], v29 offset:2048\n"
    "v_cvt_pk_bf16_f32 v119, v14, v15\n"
    "v_mfma_f32_16x16x32_bf16 v[12:15], a[96:99], a[32:35], v[20:23]\n"
    "v_exp_f32 v0, v0\n"
    "v_mfma_f32_16x16x32_bf16 v[8:11], a[100:103], a[4:7], v[8:11]\n"
    "ds_read_b128 a[88:91], v28 offset:3072\n"
    "FST1_%=:\n"
    "s_cbranch_execz WRET2_%=\n"
    "v_mfma_f32_16x16x32_bf16 v[12:15], a[100:103], a[36:39], v[12:15]\n"
    "v_exp_f32 v1, v1\n"
    "v_mfma_f32_16x16x32_bf16 v[8:11], a[104:107], a[8:11], v[8:11]\n"
    "ds_read_b128 a[92:95], v29 offset:3072\n"
    "v_cvt_pk_bf16_f32 v128, v0, v1\n"
    "v_mfma_f32_16x16x32_bf16 v[12:15], a[104:107], a[40:43], v[12:15]\n"
    "v_exp_f32 v2, v2\n"
    "v_mfma_f32_16x16x32_bf16 a[200:203], a[208:211], v[112:115], a[200:203]\n"
    "buffer_load_dwordx4 v36, s[12:15], s40 offen offset:2048 lds\n"
    "v_mfma_f32_16x16x32_bf16 v[8:11], a[108:111], a[12:15], v[8:11]\n"
    "ds_read_b128 a[96:99], v28 offset:8192\n"
    "v_mfma_f32_16x16x32_bf16 v[12:15], a[108:111], a[44:47], v[12:15]\n"
    "v_exp_f32 v3, v3\n"
    "v_mfma_f32_16x16x32_bf16 v[8:11], a[112:115], a[16:19], v[8:11]\n"
    "ds_read_b128 a[100:103], v29 offset:8192\n"
    "v_cvt_pk_bf16_f32 v129, v2, v3\n"
    "v_mfma_f32_16x16x32_bf16 v[12:15], a[112:115], a[48:51], v[12:15]\n"
    "v_exp_f32 v4, v4\n"
    "v_mfma_f32_16x16x32_bf16 v[8:11], a[116:119], a[20:23], v[8:11]\n"
    "ds_read_b128 a[104:107], v28 offset:9216\n"
    "buffer_load_dwordx4 v37, s[12:15], s40 offen offset:3072 lds\n"
    "v_mfma_f32_16x16x32_bf16 v[12:15], a[116:119], a[52:55], v[12:15]\n"
    "v_exp_f32 v5, v5\n"
    "v_mfma_f32_16x16x32_bf16 v[8:11], a[120:123], a[24:27], v[8:11]\n"
    "ds_read_b128 a[108:111], v29 offset:9216\n"
    "v_cvt_pk_bf16_f32 v132, v4, v5\n"
    "v_mfma_f32_16x16x32_bf16 v[12:15], a[120:123], a[56:59], v[12:15]\n"
    "v_exp_f32 v6, v6\n"
    "v_mfma_f32_16x16x32_bf16 v[8:11], a[124:127], a[28:31], v[8:11]\n"
    "ds_read_b128 a[112:115], v28 offset:10240\n"
    "s_waitcnt vmcnt(8) lgkmcnt(5)\n"
    "v_mfma_f32_16x16x32_bf16 v[12:15], a[124:127], a[60:63], v[12:15]\n"
    "v_exp_f32 v7, v7\n"
    "v_mfma_f32_16x16x32_bf16 v[0:3], a[64:67], a[0:3], v[16:19]\n"
    "ds_read_b128 a[116:119], v29 offset:10240\n"
    "v_cvt_pk_bf16_f32 v133, v6, v7\n"
    "v_mfma_f32_16x16x32_bf16 v[4:7], a[64:67], a[32:35], v[20:23]\n"
    "v_exp_f32 v8, v8\n"
    "v_mfma_f32_16x16x32_bf16 v[0:3], a[68:71], a[4:7], v[0:3]\n"
    "ds_read_b128 a[120:123], v28 offset:11264\n"
    "s_add_u32 m0, s28, 0x8000\n"
    "s_mov_b32 s40, 0x20400\n"
    "buffer_load_dwordx4 v34, s[12:15], s40 offen offset:0 lds\n"
    "v_mfma_f32_16x16x32_bf16 v[4:7], a[68:71], a[36:39], v[4:7]\n"
    "v_exp_f32 v9, v9\n"
    "v_mfma_f32_16x16x32_bf16 v[0:3], a[72:75], a[8:11], v[0:3]\n"
    "ds_read_b128 a[124:127], v29 offset:11264\n"
    "v_cvt_pk_bf16_f32 v130, v8, v9\n"
    "v_mfma_f32_16x16x32_bf16 v[4:7], a[72:75], a[40:43], v[4:7]\n"
    "v_exp_f32 v10, v10\n"
    "v_mfma_f32_16x16x32_bf16 a[204:207], a[208:211], v[116:119], a[204:207]\n"
    "s_barrier\n"
    "v_mfma_f32_16x16x32_bf16 v[0:3], a[76:79], a[12:15], v[0:3]\n"
    "ds_read_b128 a[64:67], v28 offset:16384\n"
    "v_mfma_f32_16x16x32_bf16 v[4:7], a[76:79], a[44:47], v[4:7]\n"
    "v_exp_f32 v11, v11\n"
    "v_mfma_f32_16x16x32_bf16 v[0:3], a[80:83], a[16:19], v[0:3]\n"
    "ds_read_b128 a[68:71], v29 offset:16384\n"
    "v_cvt_pk_bf16_f32 v131, v10, v11\n"
    "v_mfma_f32_16x16x32_bf16 v[4:7], a[80:83], a[48:51], v[4:7]\n"
    "v_exp_f32 v12, v12\n"
    "v_mfma_f32_16x16x32_bf16 v[0:3], a[84:87], a[20:23], v[0:3]\n"
    "ds_read_b128 a[72:75], v28 offset:17408\n"
    "buffer_load_dwordx4 v35, s[12:15], s40 offen offset:1024 lds\n"
    "v_mfma_f32_16x16x32_bf16 v[4:7], a[84:87], a[52:55], v[4:7]\n"
    "v_exp_f32 v13, v13\n"
    "v_mfma_f32_16x16x32_bf16 v[0:3], a[88:91], a[24:27], v[0:3]\n"
    "ds_read_b128 a[76:79], v29 offset:17408\n"
    "v_cvt_pk_bf16_f32 v134, v12, v13\n"
    "v_mfma_f32_16x16x32_bf16 v[4:7], a[88:91], a[56:59], v[4:7]\n"
    "v_exp_f32 v14, v14\n"
    "v_mfma_f32_16x16x32_bf16 v[0:3], a[92:95], a[28:31], v[0:3]\n"
    "ds_read_b128 a[80:83], v28 offset:18432\n"
    "s_waitcnt lgkmcnt(5)\n"
    "v_mfma_f32_16x16x32_bf16 v[4:7], a[92:95], a[60:63], v[4:7]\n"
    "v_exp_f32 v15, v15\n"
    "v_mfma_f32_16x16x32_bf16 v[8:11], a[96:99], a[0:3], v[16:19]\n"
    "ds_read_b128 a[84:87], v29 offset:18432\n"
    "v_cvt_pk_bf16_f32 v135, v14, v15\n"
    "v_mfma_f32_16x16x32_bf16 v[12:15], a[96:99], a[32:35], v[20:23]\n"
    "v_exp_f32 v0, v0\n"
    "v_mfma_f32_16x16x32_bf16 v[8:11], a[100:103], a[4:7], v[8:11]\n"
    "buffer_load_dwordx4 v36, s[12:15], s40 offen offset:2048 lds\n"
    "v_mfma_f32_16x16x32_bf16 v[12:15], a[100:103], a[36:39], v[12:15]\n"
    "v_exp_f32 v1, v1\n"
    "v_mfma_f32_16x16x32_bf16 v[8:11], a[104:107], a[8:11], v[8:11]\n"
    "ds_read_b128 a[88:91], v28 offset:19456\n"
    "v_cvt_pk_bf16_f32 v72, v0, v1\n"
    "v_mfma_f32_16x16x32_bf16 v[12:15], a[104:107], a[40:43], v[12:15]\n"
    "v_exp_f32 v2, v2\n"
    "v_mfma_f32_16x16x32_bf16 a[200:203], a[208:211], v[128:131], a[200:203]\n"
    "buffer_load_dwordx4 v37, s[12:15], s40 offen offset:3072 lds\n"
    "v_mfma_f32_16x16x32_bf16 v[8:11], a[108:111], a[12:15], v[8:11]\n"
    "ds_read_b128 a[92:95], v29 offset:19456\n"
    "ds_read_b128 a[96:99], v28 offset:24576\n"
    "v_mfma_f32_16x16x32_bf16 v[12:15], a[108:111], a[44:47], v[12:15]\n"
    "v_exp_f32 v3, v3\n"
    "v_mfma_f32_16x16x32_bf16 v[8:11], a[112:115], a[16:19], v[8:11]\n"
    "ds_read_b128 a[100:103], v29 offset:24576\n"
    "v_cvt_pk_bf16_f32 v73, v2, v3\n"
    "v_mfma_f32_16x16x32_bf16 v[12:15], a[112:115], a[48:51], v[12:15]\n"
    "v_exp_f32 v4, v4\n"
    "v_mfma_f32_16x16x32_bf16 v[8:11], a[116:119], a[20:23], v[8:11]\n"
    "ds_read_b128 a[104:107], v28 offset:25600\n"
    "s_add_u32 m0, s28, 0xc000\n"
    "s_mov_b32 s40, 0x21400\n"
    "buffer_load_dwordx4 v34, s[12:15], s40 offen offset:0 lds\n"
    "v_mfma_f32_16x16x32_bf16 v[12:15], a[116:119], a[52:55], v[12:15]\n"
    "v_exp_f32 v5, v5\n"
    "v_mfma_f32_16x16x32_bf16 v[8:11], a[120:123], a[24:27], v[8:11]\n"
    "ds_read_b128 a[108:111], v29 offset:25600\n"
    "v_cvt_pk_bf16_f32 v76, v4, v5\n"
    "v_mfma_f32_16x16x32_bf16 v[12:15], a[120:123], a[56:59], v[12:15]\n"
    "v_exp_f32 v6, v6\n"
    "v_mfma_f32_16x16x32_bf16 v[8:11], a[124:127], a[28:31], v[8:11]\n"
    "ds_read_b128 a[112:115], v28 offset:26624\n"
    "s_waitcnt vmcnt(9) lgkmcnt(5)\n"
    "v_mfma_f32_16x16x32_bf16 v[12:15], a[124:127], a[60:63], v[12:15]\n"
    "v_exp_f32 v7, v7\n"
    "v_mfma_f32_16x16x32_bf16 v[0:3], a[64:67], a[0:3], v[16:19]\n"
    "ds_read_b128 a[116:119], v29 offset:26624\n"
    "v_cvt_pk_bf16_f32 v77, v6, v7\n"
    "v_mfma_f32_16x16x32_bf16 v[4:7], a[64:67], a[32:35], v[20:23]\n"
    "v_exp_f32 v8, v8\n"
    "v_mfma_f32_16x16x32_bf16 v[0:3], a[68:71], a[4:7], v[0:3]\n"
    "buffer_load_dwordx4 v35, s[12:15], s40 offen offset:1024 lds\n"
    "v_mfma_f32_16x16x32_bf16 v[4:7], a[68:71], a[36:39], v[4:7]\n"
    "v_exp_f32 v9, v9\n"
    "v_mfma_f32_16x16x32_bf16 v[0:3], a[72:75], a[8:11], v[0:3]\n"
    "ds_read_b128 a[120:123], v28 offset:27648\n"
    "v_cvt_pk_bf16_f32 v74, v8, v9\n"
    "v_mfma_f32_16x16x32_bf16 v[4:7], a[72:75], a[40:43], v[4:7]\n"
    "v_exp_f32 v10, v10\n"
    "v_mfma_f32_16x16x32_bf16 a[204:207], a[208:211], v[132:135], a[204:207]\n"
    "s_barrier\n"
    "v_mfma_f32_16x16x32_bf16 v[0:3], a[76:79], a[12:15], v[0:3]\n"
    "ds_read_b128 a[124:127], v29 offset:27648\n"
    "ds_read_b128 a[64:67], v24 offset:0\n"
    "v_mfma_f32_16x16x32_bf16 v[4:7], a[76:79], a[44:47], v[4:7]\n"
    "v_exp_f32 v11, v11\n"
    "v_mfma_f32_16x16x32_bf16 v[0:3], a[80:83], a[16:19], v[0:3]\n"
    "ds_read_b128 a[68:71], v25 offset:0\n"
    "v_cvt_pk_bf16_f32 v75, v10, v11\n"
    "v_mfma_f32_16x16x32_bf16 v[4:7], a[80:83], a[48:51], v[4:7]\n"
    "v_exp_f32 v12, v12\n"
    "v_mfma_f32_16x16x32_bf16 v[0:3], a[84:87], a[20:23], v[0:3]\n"
    "ds_read_b128 a[72:75], v24 offset:1024\n"
    "buffer_load_dwordx4 v36, s[12:15], s40 offen offset:2048 lds\n"
    "v_mfma_f32_16x16x32_bf16 v[4:7], a[84:87], a[52:55], v[4:7]\n"
    "v_exp_f32 v13, v13\n"
    "v_mfma_f32_16x16x32_bf16 v[0:3], a[88:91], a[24:27], v[0:3]\n"
    "ds_read_b128 a[76:79], v25 offset:1024\n"
    "v_cvt_pk_bf16_f32 v78, v12, v13\n"
    "v_mfma_f32_16x16x32_bf16 v[4:7], a[88:91], a[56:59], v[4:7]\n"
    "v_exp_f32 v14, v14\n"
    "v_mfma_f32_16x16x32_bf16 v[0:3], a[92:95], a[28:31], v[0:3]\n"
    "ds_read_b128 a[80:83], v24 offset:2048\n"
    "s_waitcnt lgkmcnt(5)\n"
    "v_mfma_f32_16x16x32_bf16 v[4:7], a[92:95], a[60:63], v[4:7]\n"
    "v_exp_f32 v15, v15\n"
    "v_mfma_f32_16x16x32_bf16 v[8:11], a[96:99], a[0:3], v[16:19]\n"
    "ds_read_b128 a[84:87], v25 offset:2048\n"
    "v_cvt_pk_bf16_f32 v79, v14, v15\n"
    "v_mfma_f32_16x16x32_bf16 v[12:15], a[96:99], a[32:35], v[20:23]\n"
    "v_exp_f32 v0, v0\n"
    "v_mfma_f32_16x16x32_bf16 v[8:11], a[100:103], a[4:7], v[8:11]\n"
    "ds_read_b128 a[88:91], v24 offset:3072\n"
    "v_mfma_f32_16x16x32_bf16 v[12:15], a[100:103], a[36:39], v[12:15]\n"
    "v_exp_f32 v1, v1\n"
    "v_mfma_f32_16x16x32_bf16 v[8:11], a[104:107], a[8:11], v[8:11]\n"
    "ds_read_b128 a[92:95], v25 offset:3072\n"
    "v_cvt_pk_bf16_f32 v88, v0, v1\n"
    "v_mfma_f32_16x16x32_bf16 v[12:15], a[104:107], a[40:43], v[12:15]\n"
    "v_exp_f32 v2, v2\n"
    "v_mfma_f32_16x16x32_bf16 a[200:203], a[208:211], v[72:75], a[200:203]\n"
    "buffer_load_dwordx4 v37, s[12:15], s40 offen offset:3072 lds\n"
    "v_mfma_f32_16x16x32_bf16 v[8:11], a[108:111], a[12:15], v[8:11]\n"
    "ds_read_b128 a[96:99], v24 offset:8192\n"
    "s_add_u32 m0, s28, 0x10000\n"
    "v_mfma_f32_16x16x32_bf16 v[12:15], a[108:111], a[44:47], v[12:15]\n"
    "v_exp_f32 v3, v3\n"
    "v_mfma_f32_16x16x32_bf16 v[8:11], a[112:115], a[16:19], v[8:11]\n"
    "ds_read_b128 a[100:103], v25 offset:8192\n"
    "v_cvt_pk_bf16_f32 v89, v2, v3\n"
    "v_mfma_f32_16x16x32_bf16 v[12:15], a[112:115], a[48:51], v[12:15]\n"
    "v_exp_f32 v4, v4\n"
    "v_mfma_f32_16x16x32_bf16 v[8:11], a[116:119], a[20:23], v[8:11]\n"
    "ds_read_b128 a[104:107], v24 offset:9216\n"
    "s_mov_b32 s40, 0x22400\n"
    "buffer_load_dwordx4 v34, s[12:15], s40 offen offset:0 lds\n"
    "v_mfma_f32_16x16x32_bf16 v[12:15], a[116:119], a[52:55], v[12:15]\n"
    "v_exp_f32 v5, v5\n"
    "v_mfma_f32_16x16x32_bf16 v[8:11], a[120:123], a[24:27], v[8:11]\n"
    "ds_read_b128 a[108:111], v25 offset:9216\n"
    "v_cvt_pk_bf16_f32 v92, v4, v5\n"
    "v_mfma_f32_16x16x32_bf16 v[12:15], a[120:123], a[56:59], v[12:15]\n"
    "v_exp_f32 v6, v6\n"
    "v_mfma_f32_16x16x32_bf16 v[8:11], a[124:127], a[28:31], v[8:11]\n"
    "ds_read_b128 a[112:115], v24 offset:10240\n"
    "s_waitcnt vmcnt(9) lgkmcnt(5)\n"
    "v_mfma_f32_16x16x32_bf16 v[12:15], a[124:127], a[60:63], v[12:15]\n"
    "v_exp_f32 v7, v7\n"
    "v_mfma_f32_16x16x32_bf16 v[0:3], a[64:67], a[0:3], v[16:19]\n"
    "ds_read_b128 a[116:119], v25 offset:10240\n"
    "v_cvt_pk_bf16_f32 v93, v6, v7\n"
    "v_mfma_f32_16x16x32_bf16 v[4:7], a[64:67], a[32:35], v[20:23]\n"
    "v_exp_f32 v8, v8\n"
    "v_mfma_f32_16x16x32_bf16 v[0:3], a[68:71], a[4:7], v[0:3]\n"
    "buffer_load_dwordx4 v35, s[12:15], s40 offen offset:1024 lds\n"
    "v_mfma_f32_16x16x32_bf16 v[4:7], a[68:71], a[36:39], v[4:7]\n"
    "v_exp_f32 v9, v9\n"
    "v_mfma_f32_16x16x32_bf16 v[0:3], a[72:75], a[8:11], v[0:3]\n"
    "ds_read_b128 a[120:123], v24 offset:11264\n"
    "v_cvt_pk_bf16_f32 v90, v8, v9\n"
    "v_mfma_f32_16x16x32_bf16 v[4:7], a[72:75], a[40:43], v[4:7]\n"
    "v_exp_f32 v10, v10\n"
    "v_mfma_f32_16x16x32_bf16 a[204:207], a[208:211], v[76:79], a[204:207]\n"
    "s_barrier\n"
    "v_mfma_f32_16x16x32_bf16 v[0:3], a[76:79], a[12:15], v[0:3]\n"
    "ds_read_b128 a[124:127], v25 offset:11264\n"
    "ds_read_b128 a[64:67], v24 offset:16384\n"
    "v_mfma_f32_16x16x32_bf16 v[4:7], a[76:79], a[44:47], v[4:7]\n"
    "v_exp_f32 v11, v11\n"
    "v_mfma_f32_16x16x32_bf16 v[0:3], a[80:83], a[16:19], v[0:3]\n"
    "ds_read_b128 a[68:71], v25 offset:16384\n"
    "v_cvt_pk_bf16_f32 v91, v10, v11\n"
    "v_mfma_f32_16x16x32_bf16 v[4:7], a[80:83], a[48:51], v[4:7]\n"
    "v_exp_f32 v12, v12\n"
    "v_mfma_f32_16x16x32_bf16 v[0:3], a[84:87], a[20:23], v[0:3]\n"
    "ds_read_b128 a[72:75], v24 offset:17408\n"
    "buffer_load_dwordx4 v36, s[12:15], s40 offen offset:2048 lds\n"
    "v_mfma_f32_16x16x32_bf16 v[4:7], a[84:87], a[52:55], v[4:7]\n"
    "v_exp_f32 v13, v13\n"
    "v_mfma_f32_16x16x32_bf16 v[0:3], a[88:91], a[24:27], v[0:3]\n"
    "ds_read_b128 a[76:79], v25 offset:17408\n"
    "v_cvt_pk_bf16_f32 v94, v12, v13\n"
    "v_mfma_f32_16x16x32_bf16 v[4:7], a[88:91], a[56:59], v[4:7]\n"
    "v_exp_f32 v14, v14\n"
    "v_mfma_f32_16x16x32_bf16 v[0:3], a[92:95], a[28:31], v[0:3]\n"
    "ds_read_b128 a[80:83], v24 offset:18432\n"
    "s_waitcnt lgkmcnt(5)\n"
    "v_mfma_f32_16x16x32_bf16 v[4:7], a[92:95], a[60:63], v[4:7]\n"
    "v_exp_f32 v15, v15\n"
    "v_mfma_f32_16x16x32_bf16 v[8:11], a[96:99], a[0:3], v[16:19]\n"
    "ds_read_b128 a[84:87], v25 offset:18432\n"
    "v_cvt_pk_bf16_f32 v95, v14, v15\n"
    "v_mfma_f32_16x16x32_bf16 v[12:15], a[96:99], a[32:35], v[20:23]\n"
    "v_exp_f32 v0, v0\n"
    "v_mfma_f32_16x16x32_bf16 v[8:11], a[100:103], a[4:7], v[8:11]\n"
    "buffer_load_dwordx4 v37, s[12:15], s40 offen offset:3072 lds\n"
    "v_mfma_f32_16x16x32_bf16 v[12:15], a[100:103], a[36:39], v[12:15]\n"
    "v_exp_f32 v1, v1\n"
    "v_mfma_f32_16x16x32_bf16 v[8:11], a[104:107], a[8:11], v[8:11]\n"
    "ds_read_b128 a[88:91], v24 offset:19456\n"
    "v_cvt_pk_bf16_f32 v136, v0, v1\n"
    "v_mfma_f32_16x16x32_bf16 v[12:15], a[104:107], a[40:43], v[12:15]\n"
    "v_exp_f32 v2, v2\n"
    "v_mfma_f32_16x16x32_bf16 a[200:203], a[208:211], v[88:91], a[200:203]\n"
    "s_add_u32 m0, s28, 0x14000\n"
    "s_mov_b32 s40, 0x23400\n"
    "buffer_load_dwordx4 v34, s[12:15], s40 offen offset:0 lds\n"
    "v_mfma_f32_16x16x32_bf16 v[8:11], a[108:111], a[12:15], v[8:11]\n"
    "ds_read_b128 a[92:95], v25 offset:19456\n"
    "ds_read_b128 a[96:99], v24 offset:24576\n"
    "v_mfma_f32_16x16x32_bf16 v[12:15], a[108:111], a[44:47], v[12:15]\n"
    "v_exp_f32 v3, v3\n"
    "v_mfma_f32_16x16x32_bf16 v[8:11], a[112:115], a[16:19], v[8:11]\n"
    "ds_read_b128 a[100:103], v25 offset:24576\n"
    "v_cvt_pk_bf16_f32 v137, v2, v3\n"
    "v_mfma_f32_16x16x32_bf16 v[12:15], a[112:115], a[48:51], v[12:15]\n"
    "v_exp_f32 v4, v4\n"
    "v_mfma_f32_16x16x32_bf16 v[8:11], a[116:119], a[20:23], v[8:11]\n"
    "ds_read_b128 a[104:107], v24 offset:25600\n"
    "buffer_load_dwordx4 v35, s[12:15], s40 offen offset:1024 lds\n"
    "v_mfma_f32_16x16x32_bf16 v[12:15], a[116:119], a[52:55], v[12:15]\n"
    "v_exp_f32 v5, v5\n"
    "v_mfma_f32_16x16x32_bf16 v[8:11], a[120:123], a[24:27], v[8:11]\n"
    "ds_read_b128 a[108:111], v25 offset:25600\n"
    "v_cvt_pk_bf16_f32 v140, v4, v5\n"
    "v_mfma_f32_16x16x32_bf16 v[12:15], a[120:123], a[56:59], v[12:15]\n"
    "v_exp_f32 v6, v6\n"
    "v_mfma_f32_16x16x32_bf16 v[8:11], a[124:127], a[28:31], v[8:11]\n"
    "ds_read_b128 a[112:115], v24 offset:26624\n"
    "s_waitcnt vmcnt(10) lgkmcnt(5)\n"
    "v_mfma_f32_16x16x32_bf16 v[12:15], a[124:127], a[60:63], v[12:15]\n"
    "v_exp_f32 v7, v7\n"
    "v_mfma_f32_16x16x32_bf16 v[0:3], a[64:67], a[0:3], v[16:19]\n"
    "ds_read_b128 a[116:119], v25 offset:26624\n"
    "v_cvt_pk_bf16_f32 v141, v6, v7\n"
    "v_mfma_f32_16x16x32_bf16 v[4:7], a[64:67], a[32:35], v[20:23]\n"
    "v_exp_f32 v8, v8\n"
    "v_mfma_f32_16x16x32_bf16 v[0:3], a[68:71], a[4:7], v[0:3]\n"
    "buffer_load_dwordx4 v36, s[12:15], s40 offen offset:2048 lds\n"
    "v_mfma_f32_16x16x32_bf16 v[4:7], a[68:71], a[36:39], v[4:7]\n"
    "v_exp_f32 v9, v9\n"
    "v_mfma_f32_16x16x32_bf16 v[0:3], a[72:75], a[8:11], v[0:3]\n"
    "ds_read_b128 a[120:123], v24 offset:27648\n"
    "v_cvt_pk_bf16_f32 v138, v8, v9\n"
    "v_mfma_f32_16x16x32_bf16 v[4:7], a[72:75], a[40:43], v[4:7]\n"
    "v_exp_f32 v10, v10\n"
    "v_mfma_f32_16x16x32_bf16 a[204:207], a[208:211], v[92:95], a[204:207]\n"
    "s_barrier\n"
    "v_mfma_f32_16x16x32_bf16 v[0:3], a[76:79], a[12:15], v[0:3]\n"
    "ds_read_b128 a[124:127], v25 offset:27648\n"
    "ds_read_b128 a[64:67], v24 offset:32768\n"
    "v_mfma_f32_16x16x32_bf16 v[4:7], a[76:79], a[44:47], v[4:7]\n"
    "v_exp_f32 v11, v11\n"
    "v_mfma_f32_16x16x32_bf16 v[0:3], a[80:83], a[16:19], v[0:3]\n"
    "ds_read_b128 a[68:71], v25 offset:32768\n"
    "v_cvt_pk_bf16_f32 v139, v10, v11\n"
    "v_mfma_f32_16x16x32_bf16 v[4:7], a[80:83], a[48:51], v[4:7]\n"
    "v_exp_f32 v12, v12\n"
    "v_mfma_f32_16x16x32_bf16 v[0:3], a[84:87], a[20:23], v[0:3]\n"
    "ds_read_b128 a[72:75], v24 offset:33792\n"
    "buffer_load_dwordx4 v37, s[12:15], s40 offen offset:3072 lds\n"
    "v_mfma_f32_16x16x32_bf16 v[4:7], a[84:87], a[52:55], v[4:7]\n"
    "v_exp_f32 v13, v13\n"
    "v_mfma_f32_16x16x32_bf16 v[0:3], a[88:91], a[24:27], v[0:3]\n"
    "ds_read_b128 a[76:79], v25 offset:33792\n"
    "v_cvt_pk_bf16_f32 v142, v12, v13\n"
    "v_mfma_f32_16x16x32_bf16 v[4:7], a[88:91], a[56:59], v[4:7]\n"
    "v_exp_f32 v14, v14\n"
    "v_mfma_f32_16x16x32_bf16 v[0:3], a[92:95], a[28:31], v[0:3]\n"
    "ds_read_b128 a[80:83], v24 offset:34816\n"
    "s_waitcnt lgkmcnt(5)\n"
    "v_mfma_f32_16x16x32_bf16 v[4:7], a[92:95], a[60:63], v[4:7]\n"
    "v_exp_f32 v15, v15\n"
    "v_mfma_f32_16x16x32_bf16 v[8:11], a[96:99], a[0:3], v[16:19]\n"
    "ds_read_b128 a[84:87], v25 offset:34816\n"
    "v_cvt_pk_bf16_f32 v143, v14, v15\n"
    "v_mfma_f32_16x16x32_bf16 v[12:15], a[96:99], a[32:35], v[20:23]\n"
    "v_exp_f32 v0, v0\n"
    "v_mfma_f32_16x16x32_bf16 v[8:11], a[100:103], a[4:7], v[8:11]\n"
    "ds_read_b128 a[88:91], v24 offset:35840\n"
    "s_add_u32 m0, s28, 0x18000\n"
    "v_mfma_f32_16x16x32_bf16 v[12:15], a[100:103], a[36:39], v[12:15]\n"
    "v_exp_f32 v1, v1\n"
    "v_mfma_f32_16x16x32_bf16 v[8:11], a[104:107], a[8:11], v[8:11]\n"
    "ds_read_b128 a[92:95], v25 offset:35840\n"
    "v_cvt_pk_bf16_f32 v152, v0, v1\n"
    "v_mfma_f32_16x16x32_bf16 v[12:15], a[104:107], a[40:43], v[12:15]\n"
    "v_exp_f32 v2, v2\n"
    "v_mfma_f32_16x16x32_bf16 a[200:203], a[208:211], v[136:139], a[200:203]\n"
    "s_mov_b32 s40, 0x40000\n"
    "buffer_load_dwordx4 v34, s[12:15], s40 offen offset:0 lds\n"
    "v_mfma_f32_16x16x32_bf16 v[8:11], a[108:111], a[12:15], v[8:11]\n"
    "ds_read_b128 a[96:99], v24 offset:40960\n"
    "v_mfma_f32_16x16x32_bf16 v[12:15], a[108:111], a[44:47], v[12:15]\n"
    "v_exp_f32 v3, v3\n"
    "v_mfma_f32_16x16x32_bf16 v[8:11], a[112:115], a[16:19], v[8:11]\n"
    "ds_read_b128 a[100:103], v25 offset:40960\n"
    "v_cvt_pk_bf16_f32 v153, v2, v3\n"
    "v_mfma_f32_16x16x32_bf16 v[12:15], a[112:115], a[48:51], v[12:15]\n"
    "v_exp_f32 v4, v4\n"
    "v_mfma_f32_16x16x32_bf16 v[8:11], a[116:119], a[20:23], v[8:11]\n"
    "ds_read_b128 a[104:107], v24 offset:41984\n"
    "buffer_load_dwordx4 v35, s[12:15], s40 offen offset:1024 lds\n"
    "v_mfma_f32_16x16x32_bf16 v[12:15], a[116:119], a[52:55], v[12:15]\n"
    "v_exp_f32 v5, v5\n"
    "v_mfma_f32_16x16x32_bf16 v[8:11], a[120:123], a[24:27], v[8:11]\n"
    "ds_read_b128 a[108:111], v25 offset:41984\n"
    "v_cvt_pk_bf16_f32 v156, v4, v5\n"
    "v_mfma_f32_16x16x32_bf16 v[12:15], a[120:123], a[56:59], v[12:15]\n"
    "v_exp_f32 v6, v6\n"
    "v_mfma_f32_16x16x32_bf16 v[8:11], a[124:127], a[28:31], v[8:11]\n"
    "ds_read_b128 a[112:115], v24 offset:43008\n"
    "s_waitcnt vmcnt(10) lgkmcnt(5)\n"
    "v_mfma_f32_16x16x32_bf16 v[12:15], a[124:127], a[60:63], v[12:15]\n"
    "v_exp_f32 v7, v7\n"
    "v_mfma_f32_16x16x32_bf16 v[0:3], a[64:67], a[0:3], v[16:19]\n"
    "ds_read_b128 a[116:119], v25 offset:43008\n"
    "v_cvt_pk_bf16_f32 v157, v6, v7\n"
    "v_mfma_f32_16x16x32_bf16 v[4:7], a[64:67], a[32:35], v[20:23]\n"
    "v_exp_f32 v8, v8\n"
    "v_mfma_f32_16x16x32_bf16 v[0:3], a[68:71], a[4:7], v[0:3]\n"
    "buffer_load_dwordx4 v36, s[12:15], s40 offen offset:2048 lds\n"
    "v_mfma_f32_16x16x32_bf16 v[4:7], a[68:71], a[36:39], v[4:7]\n"
    "v_exp_f32 v9, v9\n"
    "v_mfma_f32_16x16x32_bf16 v[0:3], a[72:75], a[8:11], v[0:3]\n"
    "ds_read_b128 a[120:123], v24 offset:44032\n"
    "v_cvt_pk_bf16_f32 v154, v8, v9\n"
    "v_mfma_f32_16x16x32_bf16 v[4:7], a[72:75], a[40:43], v[4:7]\n"
    "v_exp_f32 v10, v10\n"
    "v_mfma_f32_16x16x32_bf16 a[204:207], a[208:211], v[140:143], a[204:207]\n"
    "s_barrier\n"
    "v_mfma_f32_16x16x32_bf16 v[0:3], a[76:79], a[12:15], v[0:3]\n"
    "ds_read_b128 a[124:127], v25 offset:44032\n"
    "ds_read_b128 a[64:67], v24 offset:49152\n"
    "v_mfma_f32_16x16x32_bf16 v[4:7], a[76:79], a[44:47], v[4:7]\n"
    "v_exp_f32 v11, v11\n"
    "v_mfma_f32_16x16x32_bf16 v[0:3], a[80:83], a[16:19], v[0:3]\n"
    "ds_read_b128 a[68:71], v25 offset:49152\n"
    "v_cvt_pk_bf16_f32 v155, v10, v11\n"
    "v_mfma_f32_16x16x32_bf16 v[4:7], a[80:83], a[48:51], v[4:7]\n"
    "v_exp_f32 v12, v12\n"
    "v_mfma_f32_16x16x32_bf16 v[0:3], a[84:87], a[20:23], v[0:3]\n"
    "ds_read_b128 a[72:75], v24 offset:50176\n"
    "buffer_load_dwordx4 v37, s[12:15], s40 offen offset:3072 lds\n"
    "v_mfma_f32_16x16x32_bf16 v[4:7], a[84:87], a[52:55], v[4:7]\n"
    "v_exp_f32 v13, v13\n"
    "v_mfma_f32_16x16x32_bf16 v[0:3], a[88:91], a[24:27], v[0:3]\n"
    "ds_read_b128 a[76:79], v25 offset:50176\n"
    "v_cvt_pk_bf16_f32 v158, v12, v13\n"
    "v_mfma_f32_16x16x32_bf16 v[4:7], a[88:91], a[56:59], v[4:7]\n"
    "v_exp_f32 v14, v14\n"
    "v_mfma_f32_16x16x32_bf16 v[0:3], a[92:95], a[28:31], v[0:3]\n"
    "ds_read_b128 a[80:83], v24 offset:51200\n"
    "s_waitcnt lgkmcnt(5)\n"
    "v_mfma_f32_16x16x32_bf16 v[4:7], a[92:95], a[60:63], v[4:7]\n"
    "v_exp_f32 v15, v15\n"
    "v_mfma_f32_16x16x32_bf16 v[8:11], a[96:99], a[0:3], v[16:19]\n"
    "ds_read_b128 a[84:87], v25 offset:51200\n"
    "v_cvt_pk_bf16_f32 v159, v14, v15\n"
    "v_mfma_f32_16x16x32_bf16 v[12:15], a[96:99], a[32:35], v[20:23]\n"
    "v_exp_f32 v0, v0\n"
    "v_mfma_f32_16x16x32_bf16 v[8:11], a[100:103], a[4:7], v[8:11]\n"
    "ds_read_b128 a[88:91], v24 offset:52224\n"
    "s_add_u32 m0, s28, 0x1c000\n"
    "s_mov_b32 s40, 0x41000\n"
    "buffer_load_dwordx4 v34, s[12:15], s40 offen offset:0 lds\n"
    "v_mfma_f32_16x16x32_bf16 v[12:15], a[100:103], a[36:39], v[12:15]\n"
    "v_exp_f32 v1, v1\n"
    "v_mfma_f32_16x16x32_bf16 v[8:11], a[104:107], a[8:11], v[8:11]\n"
    "ds_read_b128 a[92:95], v25 offset:52224\n"
    "v_cvt_pk_bf16_f32 v80, v0, v1\n"
    "v_mfma_f32_16x16x32_bf16 v[12:15], a[104:107], a[40:43], v[12:15]\n"
    "v_exp_f32 v2, v2\n"
    "v_mfma_f32_16x16x32_bf16 a[200:203], a[208:211], v[152:155], a[200:203]\n"
    "buffer_load_dwordx4 v35, s[12:15], s40 offen offset:1024 lds\n"
    "v_mfma_f32_16x16x32_bf16 v[8:11], a[108:111], a[12:15], v[8:11]\n"
    "ds_read_b128 a[96:99], v24 offset:57344\n"
    "v_mfma_f32_16x16x32_bf16 v[12:15], a[108:111], a[44:47], v[12:15]\n"
    "v_exp_f32 v3, v3\n"
    "v_mfma_f32_16x16x32_bf16 v[8:11], a[112:115], a[16:19], v[8:11]\n"
    "ds_read_b128 a[100:103], v25 offset:57344\n"
    "v_cvt_pk_bf16_f32 v81, v2, v3\n"
    "v_mfma_f32_16x16x32_bf16 v[12:15], a[112:115], a[48:51], v[12:15]\n"
    "v_exp_f32 v4, v4\n"
    "v_mfma_f32_16x16x32_bf16 v[8:11], a[116:119], a[20:23], v[8:11]\n"
    "ds_read_b128 a[104:107], v24 offset:58368\n"
    "buffer_load_dwordx4 v36, s[12:15], s40 offen offset:2048 lds\n"
    "v_mfma_f32_16x16x32_bf16 v[12:15], a[116:119], a[52:55], v[12:15]\n"
    "v_exp_f32 v5, v5\n"
    "v_mfma_f32_16x16x32_bf16 v[8:11], a[120:123], a[24:27], v[8:11]\n"
    "ds_read_b128 a[108:111], v25 offset:58368\n"
    "v_cvt_pk_bf16_f32 v84, v4, v5\n"
    "v_mfma_f32_16x16x32_bf16 v[12:15], a[120:123], a[56:59], v[12:15]\n"
    "v_exp_f32 v6, v6\n"
    "v_mfma_f32_16x16x32_bf16 v[8:11], a[124:127], a[28:31], v[8:11]\n"
    "ds_read_b128 a[112:115], v24 offset:59392\n"
    "s_waitcnt vmcnt(11) lgkmcnt(5)\n"
    "v_mfma_f32_16x16x32_bf16 v[12:15], a[124:127], a[60:63], v[12:15]\n"
    "v_exp_f32 v7, v7\n"
    "v_mfma_f32_16x16x32_bf16 v[0:3], a[64:67], a[0:3], v[16:19]\n"
    "ds_read_b128 a[116:119], v25 offset:59392\n"
    "v_cvt_pk_bf16_f32 v85, v6, v7\n"
    "v_mfma_f32_16x16x32_bf16 v[4:7], a[64:67], a[32:35], v[20:23]\n"
    "v_exp_f32 v8, v8\n"
    "v_mfma_f32_16x16x32_bf16 v[0:3], a[68:71], a[4:7], v[0:3]\n"
    "buffer_load_dwordx4 v37, s[12:15], s40 offen offset:3072 lds\n"
    "v_mfma_f32_16x16x32_bf16 v[4:7], a[68:71], a[36:39], v[4:7]\n"
    "v_exp_f32 v9, v9\n"
    "v_mfma_f32_16x16x32_bf16 v[0:3], a[72:75], a[8:11], v[0:3]\n"
    "ds_read_b128 a[120:123], v24 offset:60416\n"
    "v_cvt_pk_bf16_f32 v82, v8, v9\n"
    "v_mfma_f32_16x16x32_bf16 v[4:7], a[72:75], a[40:43], v[4:7]\n"
    "v_exp_f32 v10, v10\n"
    "v_mfma_f32_16x16x32_bf16 a[204:207], a[208:211], v[156:159], a[204:207]\n"
    "s_barrier\n"
    "v_mfma_f32_16x16x32_bf16 v[0:3], a[76:79], a[12:15], v[0:3]\n"
    "ds_read_b128 a[124:127], v25 offset:60416\n"
    "ds_read_b128 a[64:67], v26 offset:0\n"
    "v_mfma_f32_16x16x32_bf16 v[4:7], a[76:79], a[44:47], v[4:7]\n"
    "v_exp_f32 v11, v11\n"
    "v_mfma_f32_16x16x32_bf16 v[0:3], a[80:83], a[16:19], v[0:3]\n"
    "ds_read_b128 a[68:71], v27 offset:0\n"
    "v_cvt_pk_bf16_f32 v83, v10, v11\n"
    "v_mfma_f32_16x16x32_bf16 v[4:7], a[80:83], a[48:51], v[4:7]\n"
    "v_exp_f32 v12, v12\n"
    "v_mfma_f32_16x16x32_bf16 v[0:3], a[84:87], a[20:23], v[0:3]\n"
    "ds_read_b128 a[72:75], v26 offset:1024\n"
    "s_add_u32 m0, s28, 0x20000\n"
    "s_mov_b32 s40, 0x42000\n"
    "buffer_load_dwordx4 v34, s[12:15], s40 offen offset:0 lds\n"
    "v_mfma_f32_16x16x32_bf16 v[4:7], a[84:87], a[52:55], v[4:7]\n"
    "v_exp_f32 v13, v13\n"
    "v_mfma_f32_16x16x32_bf16 v[0:3], a[88:91], a[24:27], v[0:3]\n"
    "ds_read_b128 a[76:79], v27 offset:1024\n"
    "v_cvt_pk_bf16_f32 v86, v12, v13\n"
    "v_mfma_f32_16x16x32_bf16 v[4:7], a[88:91], a[56:59], v[4:7]\n"
    "v_exp_f32 v14, v14\n"
    "v_mfma_f32_16x16x32_bf16 v[0:3], a[92:95], a[28:31], v[0:3]\n"
    "ds_read_b128 a[80:83], v26 offset:2048\n"
    "s_waitcnt lgkmcnt(5)\n"
    "v_mfma_f32_16x16x32_bf16 v[4:7], a[92:95], a[60:63], v[4:7]\n"
    "v_exp_f32 v15, v15\n"
    "v_mfma_f32_16x16x32_bf16 v[8:11], a[96:99], a[0:3], v[16:19]\n"
    "ds_read_b128 a[84:87], v27 offset:2048\n"
    "v_cvt_pk_bf16_f32 v87, v14, v15\n"
    "v_mfma_f32_16x16x32_bf16 v[12:15], a[96:99], a[32:35], v[20:23]\n"
    "v_exp_f32 v0, v0\n"
    "v_mfma_f32_16x16x32_bf16 v[8:11], a[100:103], a[4:7], v[8:11]\n"
    "ds_read_b128 a[88:91], v26 offset:3072\n"
    "v_mfma_f32_16x16x32_bf16 v[12:15], a[100:103], a[36:39], v[12:15]\n"
    "v_exp_f32 v1, v1\n"
    "v_mfma_f32_16x16x32_bf16 v[8:11], a[104:107], a[8:11], v[8:11]\n"
    "ds_read_b128 a[92:95], v27 offset:3072\n"
    "v_cvt_pk_bf16_f32 v96, v0, v1\n"
    "v_mfma_f32_16x16x32_bf16 v[12:15], a[104:107], a[40:43], v[12:15]\n"
    "v_exp_f32 v2, v2\n"
    "v_mfma_f32_16x16x32_bf16 a[200:203], a[208:211], v[80:83], a[200:203]\n"
    "buffer_load_dwordx4 v35, s[12:15], s40 offen offset:1024 lds\n"
    "v_mfma_f32_16x16x32_bf16 v[8:11], a[108:111], a[12:15], v[8:11]\n"
    "ds_read_b128 a[96:99], v26 offset:8192\n"
    "v_mfma_f32_16x16x32_bf16 v[12:15], a[108:111], a[44:47], v[12:15]\n"
    "v_exp_f32 v3, v3\n"
    "v_mfma_f32_16x16x32_bf16 v[8:11], a[112:115], a[16:19], v[8:11]\n"
    "ds_read_b128 a[100:103], v27 offset:8192\n"
    "v_cvt_pk_bf16_f32 v97, v2, v3\n"
    "v_mfma_f32_16x16x32_bf16 v[12:15], a[112:115], a[48:51], v[12:15]\n"
    "v_exp_f32 v4, v4\n"
    "v_mfma_f32_16x16x32_bf16 v[8:11], a[116:119], a[20:23], v[8:11]\n"
    "ds_read_b128 a[104:107], v26 offset:9216\n"
    "buffer_load_dwordx4 v36, s[12:15], s40 offen offset:2048 lds\n"
    "v_mfma_f32_16x16x32_bf16 v[12:15], a[116:119], a[52:55], v[12:15]\n"
    "v_exp_f32 v5, v5\n"
    "v_mfma_f32_16x16x32_bf16 v[8:11], a[120:123], a[24:27], v[8:11]\n"
    "ds_read_b128 a[108:111], v27 offset:9216\n"
    "v_cvt_pk_bf16_f32 v100, v4, v5\n"
    "v_mfma_f32_16x16x32_bf16 v[12:15], a[120:123], a[56:59], v[12:15]\n"
    "v_exp_f32 v6, v6\n"
    "v_mfma_f32_16x16x32_bf16 v[8:11], a[124:127], a[28:31], v[8:11]\n"
    "ds_read_b128 a[112:115], v26 offset:10240\n"
    "s_waitcnt vmcnt(11) lgkmcnt(5)\n"
    "v_mfma_f32_16x16x32_bf16 v[12:15], a[124:127], a[60:63], v[12:15]\n"
    "v_exp_f32 v7, v7\n"
    "s_branch JOIN_%=\n"
    "FEND_%=:\n"
    "SPINA_%=:\n"
    "s_mov_b32 s42, 0x20000\n"
    "SPINLA_%=:\n"
    "buffer_load_dword v28, v27, s[24:27], s33 offen offset:1024 sc1\n"
    "s_waitcnt vmcnt(0)\n"
    "v_cmp_eq_u32 vcc, v28, v38\n"
    "s_nop 4\n"
    "s_cbranch_vccz BACKA_%=\n"
    "s_sleep 2\n"
    "s_sub_u32 s42, s42, 1\n"
    "s_cmp_lg_u32 s42, 0\n"
    "s_cbranch_scc1 SPINLA_%=\n"
    "s_branch BACKA_%=\n"
    "SPINB_%=:\n"
    "s_mov_b32 s42, 0x20000\n"
    "SPINLB_%=:\n"
    "buffer_load_dword v28, v27, s[24:27], s33 offen offset:1280 sc1\n"
    "s_waitcnt vmcnt(0)\n"
    "v_cmp_eq_u32 vcc, v28, v38\n"
    "s_nop 4\n"
    "s_cbranch_vccz BACKB_%=\n"
    "s_sleep 2\n"
    "s_sub_u32 s42, s42, 1\n"
    "s_cmp_lg_u32 s42, 0\n"
    "s_cbranch_scc1 SPINLB_%=\n"
    "s_branch BACKB_%=\n"
    "FALLBACK_%=:\n"
    "s_waitcnt vmcnt(0) lgkmcnt(0)\n"
    "s_nop 15\n"
    "s_barrier\n"
    "v_lshrrev_b32 v0, 3, v39\n"
    "v_lshrrev_b32 v2, 4, v39\n"
    "v_and_b32 v1, 7, v39\n"
    "v_xor_b32 v3, v1, v2\n"
    "v_lshlrev_b32 v3, 4, v3\n"
    "v_lshl_add_u32 v3, v0, 11, v3\n"
    "v_add_u32 v104, 3072, v3\n"
    "v_add_u32 v105, 2176, v3\n"
    "v_add_u32 v106, 1280, v3\n"
    "v_add_u32 v107, 384, v3\n"
    "v_lshlrev_b32 v3, 14, v0\n"
    "v_lshlrev_b32 v5, 11, v0\n"
    "v_sub_u32 v5, v3, v5\n"
    "v_xad_u32 v34, v104, s29, v5\n"
    "v_xad_u32 v35, v105, s29, v5\n"
    "v_xad_u32 v36, v106, s29, v5\n"
    "v_xad_u32 v37, v107, s29, v5\n"
    "v_lshlrev_b32 v2, 1, v2\n"
    "v_xor_b32 v1, v1, v2\n"
    "v_lshlrev_b32 v1, 4, v1\n"
    "v_add_u32 v38, v3, v1\n"
    "v_bfe_u32 v0, v39, 1, 3\n"
    "v_lshrrev_b32 v1, 4, v39\n"
    "v_xor_b32 v0, v0, v1\n"
    "v_lshlrev_b32 v0, 4, v0\n"
    "v_and_b32 v1, 7, v39\n"
    "v_lshl_add_u32 v0, v1, 7, v0\n"
    "v_bfe_u32 v1, v39, 3, 1\n"
    "v_lshl_add_u32 v2, v1, 12, v0\n"
    "v_add_u32 v24, %3, v2\n"
    "v_xor_b32 v25, 64, v24\n"
    "v_add_u32 v26, 0x10000, v24\n"
    "v_add_u32 v27, 0x10000, v25\n"
    "v_add_u32 v28, 0x20000, v24\n"
    "v_add_u32 v29, 0x20000, v25\n"
    "v_lshrrev_b32 v0, 5, v39\n"
    "v_bfe_u32 v1, v39, 2, 3\n"
    "v_lshlrev_b32 v1, 7, v1\n"
    "v_lshl_add_u32 v0, v0, 12, v1\n"
    "v_and_b32 v1, 3, v39\n"
    "v_lshl_add_u32 v0, v1, 3, v0\n"
    "v_bfe_u32 v1, v39, 3, 2\n"
    "v_lshl_add_u32 v0, v1, 5, v0\n"
    "v_add_u32 v30, %3, v0\n"
    "v_xor_b32 v31, 32, v30\n"
    "v_xor_b32 v32, 64, v30\n"
    "v_xor_b32 v33, 96, v30\n"
    "s_mov_b32 s41, 1\n"
    "s_mov_b32 s42, 0\n"
    "v_mov_b32 v16, 0xff800000\n"
    "v_mov_b32 v20, 0xff800000\n"
    "FBL_%=:\n"
    "s_bfe_u32 s40, s42, 0x20000\n"
    "s_lshl_b32 s40, s40, 12\n"
    "s_bfe_u32 s43, s42, 0x10002\n"
    "s_lshl_b32 s43, s43, 10\n"
    "s_or_b32 s40, s40, s43\n"
    "s_bfe_u32 s43, s42, 0x20003\n"
    "s_lshl_b32 s43, s43, 17\n"
    "s_or_b32 s40, s40, s43\n"
    "s_mov_b32 m0, s28\n"
    "s_nop 0\n"
    "buffer_load_dwordx4 v34, s[12:15], s40 offen offset:0 lds\n"
    "buffer_load_dwordx4 v35, s[12:15], s40 offen offset:1024 lds\n"
    "buffer_load_dwordx4 v36, s[12:15], s40 offen offset:2048 lds\n"
    "buffer_load_dwordx4 v37, s[12:15], s40 offen offset:3072 lds\n"
    "s_waitcnt vmcnt(0) lgkmcnt(0)\n"
    "s_barrier\n"
    "ds_read_b128 a[64:67], v24 offset:0\n"
    "ds_read_b128 a[68:71], v25 offset:0\n"
    "ds_read_b128 a[72:75], v24 offset:1024\n"
    "ds_read_b128 a[76:79], v25 offset:1024\n"
    "ds_read_b128 a[80:83], v24 offset:2048\n"
    "ds_read_b128 a[84:87], v25 offset:2048\n"
    "ds_read_b128 a[88:91], v24 offset:3072\n"
    "ds_read_b128 a[92:95], v25 offset:3072\n"
    "s_waitcnt vmcnt(0) lgkmcnt(0)\n"
    "v_mfma_f32_16x16x32_bf16 v[0:3], a[64:67], a[0:3], 0\n"
    "v_mfma_f32_16x16x32_bf16 v[4:7], a[64:67], a[32:35], 0\n"
    "v_mfma_f32_16x16x32_bf16 v[0:3], a[68:71], a[4:7], v[0:3]\n"
    "v_mfma_f32_16x16x32_bf16 v[4:7], a[68:71], a[36:39], v[4:7]\n"
    "v_mfma_f32_16x16x32_bf16 v[0:3], a[72:75], a[8:11], v[0:3]\n"
    "v_mfma_f32_16x16x32_bf16 v[4:7], a[72:75], a[40:43], v[4:7]\n"
    "v_mfma_f32_16x16x32_bf16 v[0:3], a[76:79], a[12:15], v[0:3]\n"
    "v_mfma_f32_16x16x32_bf16 v[4:7], a[76:79], a[44:47], v[4:7]\n"
    "v_mfma_f32_16x16x32_bf16 v[0:3], a[80:83], a[16:19], v[0:3]\n"
    "v_mfma_f32_16x16x32_bf16 v[4:7], a[80:83], a[48:51], v[4:7]\n"
    "v_mfma_f32_16x16x32_bf16 v[0:3], a[84:87], a[20:23], v[0:3]\n"
    "v_mfma_f32_16x16x32_bf16 v[4:7], a[84:87], a[52:55], v[4:7]\n"
    "v_mfma_f32_16x16x32_bf16 v[0:3], a[88:91], a[24:27], v[0:3]\n"
    "v_mfma_f32_16x16x32_bf16 v[4:7], a[88:91], a[56:59], v[4:7]\n"
    "v_mfma_f32_16x16x32_bf16 v[0:3], a[92:95], a[28:31], v[0:3]\n"
    "v_mfma_f32_16x16x32_bf16 v[4:7], a[92:95], a[60:63], v[4:7]\n"
    "ds_read_b128 a[96:99], v24 offset:8192\n"
    "ds_read_b128 a[100:103], v25 offset:8192\n"
    "ds_read_b128 a[104:107], v24 offset:9216\n"
    "ds_read_b128 a[108:111], v25 offset:9216\n"
    "ds_read_b128 a[112:115], v24 offset:10240\n"
    "ds_read_b128 a[116:119], v25 offset:10240\n"
    "ds_read_b128 a[120:123], v24 offset:11264\n"
    "ds_read_b128 a[124:127], v25 offset:11264\n"
    "s_waitcnt vmcnt(0) lgkmcnt(0)\n"
    "v_mfma_f32_16x16x32_bf16 v[8:11], a[96:99], a[0:3], 0\n"
    "v_mfma_f32_16x16x32_bf16 v[12:15], a[96:99], a[32:35], 0\n"
    "v_mfma_f32_16x16x32_bf16 v[8:11], a[100:103], a[4:7], v[8:11]\n"
    "v_mfma_f32_16x16x32_bf16 v[12:15], a[100:103], a[36:39], v[12:15]\n"
    "v_mfma_f32_16x16x32_bf16 v[8:11], a[104:107], a[8:11], v[8:11]\n"
    "v_mfma_f32_16x16x32_bf16 v[12:15], a[104:107], a[40:43], v[12:15]\n"
    "v_mfma_f32_16x16x32_bf16 v[8:11], a[108:111], a[12:15], v[8:11]\n"
    "v_mfma_f32_16x16x32_bf16 v[12:15], a[108:111], a[44:47], v[12:15]\n"
    "v_mfma_f32_16x16x32_bf16 v[8:11], a[112:115], a[16:19], v[8:11]\n"
    "v_mfma_f32_16x16x32_bf16 v[12:15], a[112:115], a[48:51], v[12:15]\n"
    "v_mfma_f32_16x16x32_bf16 v[8:11], a[116:119], a[20:23], v[8:11]\n"
    "v_mfma_f32_16x16x32_bf16 v[12:15], a[116:119], a[52:55], v[12:15]\n"
    "v_mfma_f32_16x16x32_bf16 v[8:11], a[120:123], a[24:27], v[8:11]\n"
    "v_mfma_f32_16x16x32_bf16 v[12:15], a[120:123], a[56:59], v[12:15]\n"
    "v_mfma_f32_16x16x32_bf16 v[8:11], a[124:127], a[28:31], v[8:11]\n"
    "v_mfma_f32_16x16x32_bf16 v[12:15], a[124:127], a[60:63], v[12:15]\n"
    "s_barrier\n"
    "v_max3_f32 v16, v16, v0, v1\n"
    "v_max3_f32 v20, v20, v4, v5\n"
    "v_max3_f32 v16, v16, v2, v3\n"
    "v_max3_f32 v20, v20, v6, v7\n"
    "s_nop 1\n"
    "v_max3_f32 v16, v16, v8, v9\n"
    "v_max3_f32 v20, v20, v12, v13\n"
    "v_max3_f32 v16, v16, v10, v11\n"
    "v_max3_f32 v20, v20, v14, v15\n"
    "s_add_u32 s42, s42, 1\n"
    "s_cmp_lt_u32 s42, 32\n"
    "s_cbranch_scc1 FBL_%=\n"
    "v_accvgpr_write_b32 a212, v16\n"
    "v_accvgpr_write_b32 a213, v20\n"
    "v_accvgpr_read_b32 v192, a0\n"
    "v_accvgpr_read_b32 v193, a1\n"
    "v_accvgpr_read_b32 v194, a2\n"
    "v_accvgpr_read_b32 v195, a3\n"
    "v_accvgpr_read_b32 v196, a4\n"
    "v_accvgpr_read_b32 v197, a5\n"
    "v_accvgpr_read_b32 v198, a6\n"
    "v_accvgpr_read_b32 v199, a7\n"
    "v_accvgpr_read_b32 v200, a8\n"
    "v_accvgpr_read_b32 v201, a9\n"
    "v_accvgpr_read_b32 v202, a10\n"
    "v_accvgpr_read_b32 v203, a11\n"
    "v_accvgpr_read_b32 v204, a12\n"
    "v_accvgpr_read_b32 v205, a13\n"
    "v_accvgpr_read_b32 v206, a14\n"
    "v_accvgpr_read_b32 v207, a15\n"
    "v_accvgpr_read_b32 v208, a16\n"
    "v_accvgpr_read_b32 v209, a17\n"
    "v_accvgpr_read_b32 v210, a18\n"
    "v_accvgpr_read_b32 v211, a19\n"
    "v_accvgpr_read_b32 v212, a20\n"
    "v_accvgpr_read_b32 v213, a21\n"
    "v_accvgpr_read_b32 v214, a22\n"
    "v_accvgpr_read_b32 v215, a23\n"
    "v_accvgpr_read_b32 v216, a24\n"
    "v_accvgpr_read_b32 v217, a25\n"
    "v_accvgpr_read_b32 v218, a26\n"
    "v_accvgpr_read_b32 v219, a27\n"
    "v_accvgpr_read_b32 v220, a28\n"
    "v_accvgpr_read_b32 v221, a29\n"
    "v_accvgpr_read_b32 v222, a30\n"
    "v_accvgpr_read_b32 v223, a31\n"
    "v_accvgpr_read_b32 v224, a32\n"
    "v_accvgpr_read_b32 v225, a33\n"
    "v_accvgpr_read_b32 v226, a34\n"
    "v_accvgpr_read_b32 v227, a35\n"
    "v_accvgpr_read_b32 v228, a36\n"
    "v_accvgpr_read_b32 v229, a37\n"
    "v_accvgpr_read_b32 v230, a38\n"
    "v_accvgpr_read_b32 v231, a39\n"
    "v_accvgpr_read_b32 v232, a40\n"
    "v_accvgpr_read_b32 v233, a41\n"
    "v_accvgpr_read_b32 v234, a42\n"
    "v_accvgpr_read_b32 v235, a43\n"
    "v_accvgpr_read_b32 v236, a44\n"
    "v_accvgpr_read_b32 v237, a45\n"
    "v_accvgpr_read_b32 v238, a46\n"
    "v_accvgpr_read_b32 v239, a47\n"
    "v_accvgpr_read_b32 v240, a48\n"
    "v_accvgpr_read_b32 v241, a49\n"
    "v_accvgpr_read_b32 v242, a50\n"
    "v_accvgpr_read_b32 v243, a51\n"
    "v_accvgpr_read_b32 v244, a52\n"
    "v_accvgpr_read_b32 v245, a53\n"
    "v_accvgpr_read_b32 v246, a54\n"
    "v_accvgpr_read_b32 v247, a55\n"
    "v_accvgpr_read_b32 v248, a56\n"
    "v_accvgpr_read_b32 v249, a57\n"
    "v_accvgpr_read_b32 v250, a58\n"
    "v_accvgpr_read_b32 v251, a59\n"
    "v_accvgpr_read_b32 v252, a60\n"
    "v_accvgpr_read_b32 v253, a61\n"
    "v_accvgpr_read_b32 v254, a62\n"
    "v_accvgpr_read_b32 v255, a63\n"
    "s_mov_b32 s45, 1.0\n"
    "s_add_u32 m0, s28, 0x0\n"
    "s_mov_b32 s40, 0x0\n"
    "buffer_load_dwordx4 v34, s[12:15], s40 offen offset:0 lds\n"
    "buffer_load_dwordx4 v35, s[12:15], s40 offen offset:1024 lds\n"
    "buffer_load_dwordx4 v36, s[12:15], s40 offen offset:2048 lds\n"
    "buffer_load_dwordx4 v37, s[12:15], s40 offen offset:3072 lds\n"
    "s_add_u32 m0, s28, 0x4000\n"
    "s_mov_b32 s40, 0x1000\n"
    "buffer_load_dwordx4 v34, s[12:15], s40 offen offset:0 lds\n"
    "buffer_load_dwordx4 v35, s[12:15], s40 offen offset:1024 lds\n"
    "buffer_load_dwordx4 v36, s[12:15], s40 offen offset:2048 lds\n"
    "buffer_load_dwordx4 v37, s[12:15], s40 offen offset:3072 lds\n"
    "s_add_u32 m0, s28, 0x8000\n"
    "s_mov_b32 s40, 0x2000\n"
    "buffer_load_dwordx4 v34, s[12:15], s40 offen offset:0 lds\n"
    "buffer_load_dwordx4 v35, s[12:15], s40 offen offset:1024 lds\n"
    "buffer_load_dwordx4 v36, s[12:15], s40 offen offset:2048 lds\n"
    "s_add_u32 m0, s28, 0x8000\n"
    "s_mov_b32 s40, 0x2000\n"
    "s_waitcnt vmcnt(0) lgkmcnt(0)\n"
    "s_nop 7\n"
    "s_branch MAIN_%=\n"
    "END_%=:\n"
    :
    : "s"((unsigned long long)__builtin_amdgcn_kernarg_segment_ptr()), "s"(blockIdx.x),
      "s"(__builtin_amdgcn_readfirstlane(threadIdx.x)), "s"((unsigned)(size_t)lds)
    : "v0","v1","v2","v3","v4","v5","v6","v7","v8","v9","v10","v11","v12","v13","v14","v15","v16","v17","v18","v19","v20","v21","v22","v23","v24","v25","v26","v27","v28","v29","v30","v31","v32","v33","v34","v35","v36","v37","v38","v39","v40","v41","v42","v43","v44","v45","v46","v47","v48","v49","v50","v51","v52","v53","v54","v55","v56","v57","v58","v59","v60","v61","v62","v63","v64","v65","v66","v67","v68","v69","v70","v71","v72","v73","v74","v75","v76","v77","v78","v79","v80","v81","v82","v83","v84","v85","v86","v87","v88","v89","v90","v91","v92","v93","v94","v95","v96","v97","v98","v99","v100","v101","v102","v103","v104","v105","v106","v107","v108","v109","v110","v111","v112","v113","v114","v115","v116","v117","v118","v119","v120","v121","v122","v123","v124","v125","v126","v127","v128","v129","v130","v131","v132","v133","v134","v135","v136","v137","v138","v139","v140","v141","v142","v143","v144","v145","v146","v147","v148","v149","v150","v151","v152","v153","v154","v155","v156","v157","v158","v159","v160","v161","v162","v163","v164","v165","v166","v167","v168","v169","v170","v171","v172","v173","v174","v175","v176","v177","v178","v179","v180","v181","v182","v183","v184","v185","v186","v187","v188","v189","v190","v191","v192","v193","v194","v195","v196","v197","v198","v199","v200","v201","v202","v203","v204","v205","v206","v207","v208","v209","v210","v211","v212","v213","v214","v215","v216","v217","v218","v219","v220","v221","v222","v223","v224","v225","v226","v227","v228","v229","v230","v231","v232","v233","v234","v235","v236","v237","v238","v239","v240","v241","v242","v243","v244","v245","v246","v247","v248","v249","v250","v251","v252","v253","v254","v255","a0","a1","a2","a3","a4","a5","a6","a7","a8","a9","a10","a11","a12","a13","a14","a15","a16","a17","a18","a19","a20","a21","a22","a23","a24","a25","a26","a27","a28","a29","a30","a31","a32","a33","a34","a35","a36","a37","a38","a39","a40","a41","a42","a43","a44","a45","a46","a47","a48","a49","a50","a51","a52","a53","a54","a55","a56","a57","a58","a59","a60","a61","a62","a63","a64","a65","a66","a67","a68","a69","a70","a71","a72","a73","a74","a75","a76","a77","a78","a79","a80","a81","a82","a83","a84","a85","a86","a87","a88","a89","a90","a91","a92","a93","a94","a95","a96","a97","a98","a99","a100","a101","a102","a103","a104","a105","a106","a107","a108","a109","a110","a111","a112","a113","a114","a115","a116","a117","a118","a119","a120","a121","a122","a123","a124","a125","a126","a127","a128","a129","a130","a131","a132","a133","a134","a135","a136","a137","a138","a139","a140","a141","a142","a143","a144","a145","a146","a147","a148","a149","a150","a151","a152","a153","a154","a155","a156","a157","a158","a159","a160","a161","a162","a163","a164","a165","a166","a167","a168","a169","a170","a171","a172","a173","a174","a175","a176","a177","a178","a179","a180","a181","a182","a183","a184","a185","a186","a187","a188","a189","a190","a191","a192","a193","a194","a195","a196","a197","a198","a199","a200","a201","a202","a203","a204","a205","a206","a207","a208","a209","a210","a211","a212","a213","a214","a215","a216","a217","a218","a219","a220","a221","a222","a223","a224","a225","a226","a227","a228","a229","a230","a231","a232","a233","a234","a235","a236","a237","a238","a239","a240","a241","a242","a243","a244","a245","a246","a247","a248","a249","a250","a251","a252","a253","a254","a255","s7","s8","s9","s10","s11","s12","s13","s14","s15","s16","s17","s18","s19","s20","s21","s22","s23","s24","s25","s26","s27","s28","s29","s30","s31","s33","s34","s35","s36","s37","s38","s39","s40","s41","s42","s43","s44","s45","s46","s47","s48","s49","s50","s51","s52","s53","s54","s55","s56","s57","s58","s59","s60","s61","s62","s63","s64","s65","s66","s67","s68","s69","s70","s71","s72","s73","s74","s75","s76","s77","s78","s79","s80","s81","s82","s83","s84","s85","s86","s87","s88","s89","s90","s91","s92","s93","s94","s95","s96","s97","s98","s99","vcc","scc","memory");
}

extern "C" void kernel_launch(void* const* d_in, const int* in_sizes, int n_in, void* d_out, int out_size,
                              void* d_ws, size_t ws_size, hipStream_t stream) {
  gqa_attn_kernel<<<256, 256, 0, stream>>>((const char*)d_in[0], (const char*)d_in[1], (const char*)d_in[2],
                                           (char*)d_out, (char*)d_ws);
}
```
